```python
import torch

def _softmax(x, axis=-1):
    return torch.softmax(x, dim=axis)
def _gelu(x):
    return torch.nn.functional.gelu(x, approximate="tanh")
def _silu(x):
    return torch.nn.functional.silu(x)
def _sigmoid(x):
    return torch.sigmoid(x)
def _layernorm(x, axis=-1, eps=1e-5):
    m = x.mean(dim=axis, keepdim=True)
    v = x.var(dim=axis, keepdim=True, unbiased=False)
    return (x - m) / torch.sqrt(v + eps)
def _scatter_add(tgt, idx, src, axis=0):
    return tgt.clone().index_add_(axis, idx, src)
def _segment_sum(src, seg, n):
    out = torch.zeros((n,) + src.shape[1:], dtype=src.dtype)
    return out.index_add_(0, seg, src)
def _pad(x, pw):
    flat = tuple(v for pair in reversed(pw) for v in pair)
    return torch.nn.functional.pad(x, flat)

kernel_name = 'sequential_preLN__softmax_full_mha__mixer_channel_mlp__prompt'

def setup_inputs():

    _w = (lambda a: (lambda t: t.to(torch.bfloat16) if t.is_floating_point() else t)(torch.as_tensor(a)))
    def _A_setup():
        B, S, D = 1, 2048, 448
        return {
            "x": _w((lambda *s: torch.randn(*s, dtype=torch.bfloat16) * 0.05)(B, S, D)),
            "wqkv": _w((lambda *s: torch.randn(*s, dtype=torch.bfloat16) * 0.05)(D, 3 * D)),
            "wo": _w((lambda *s: torch.randn(*s, dtype=torch.bfloat16) * 0.05)(D, D)),
        }
    _A_in = _A_setup()

    def _B_setup():
        B, S, D, F = 1, 2048, 448, 1792
        return {
            "x": _w((lambda *s: torch.randn(*s, dtype=torch.bfloat16) * 0.05)(B, S, D)),
            "w1": _w((lambda *s: torch.randn(*s, dtype=torch.bfloat16) * 1.6)(D, F) * (D ** -0.5)),
            "b1": _w((lambda *s: torch.randn(*s, dtype=torch.bfloat16) * 0.8)(F)),
            "w2": _w((lambda *s: torch.randn(*s, dtype=torch.bfloat16) * 0.05)(F, D) * (F ** -0.5)),
            "b2": _w((lambda *s: torch.randn(*s, dtype=torch.bfloat16) * 0.05)(D)),
        }
    _B_in = _B_setup()

    return {
        "A_x": _A_in["x"],
        "A_wqkv": _A_in["wqkv"],
        "A_wo": _A_in["wo"],
        "B_x": _B_in["x"],
        "B_w1": _B_in["w1"],
        "B_b1": _B_in["b1"],
        "B_w2": _B_in["w2"],
        "B_b2": _B_in["b2"],
    }

def reference(A_x, A_wqkv, A_wo, B_x, B_w1, B_b1, B_w2, B_b2):
    def _A_ref(x, wqkv, wo):
        B, S, D = x.shape
        H = 16; Dh = D // H
        qkv = (lambda x, *d: x.permute(*d))((x @ wqkv).reshape(B, S, 3, H, Dh), 2, 0, 3, 1, 4)
        q, k, v = qkv[0], qkv[1], qkv[2]
        scale = Dh ** -0.5
        scores = torch.einsum("bhqd,bhkd->bhqk", q, k) * scale
        p = _softmax(scores, axis=-1)
        o = torch.einsum("bhqk,bhkd->bhqd", p, v)
        return (lambda x, *d: x.permute(*d))(o, 0, 2, 1, 3).reshape(B, S, D) @ wo

    def _B_ref(x, w1, b1, w2, b2):
        y = _layernorm(x, axis=-1)
        h = _gelu(torch.einsum("bsd,df->bsf", y, w1) + b1)
        return torch.einsum("bsf,fd->bsd", h, w2) + b2

    x = A_x
    h = x + _A_ref(_layernorm(x, axis=-1), A_wqkv, A_wo)
    h2 = h + _B_ref(_layernorm(h, axis=-1), B_w1, B_b1, B_w2, B_b2)
    return h2
```

```cpp
#include <hip/hip_runtime.h>
#include <stdint.h>

#define LAS __attribute__((address_space(3)))
typedef __bf16 bf2 __attribute__((ext_vector_type(2)));
typedef __bf16 bf8 __attribute__((ext_vector_type(8)));
typedef __bf16 bf16v __attribute__((ext_vector_type(16)));
typedef float f32x4 __attribute__((ext_vector_type(4)));
typedef float f32x8 __attribute__((ext_vector_type(8)));
typedef float f32x16 __attribute__((ext_vector_type(16)));
typedef unsigned u32x2 __attribute__((ext_vector_type(2)));
typedef unsigned u32x4 __attribute__((ext_vector_type(4)));
typedef short s16x4 __attribute__((ext_vector_type(4)));
typedef __amdgpu_buffer_rsrc_t rsrc_t;

constexpr int S = 2048, D = 448, H = 16, F = 1792;
constexpr int NT = 512, NWG = 256;
constexpr float EPS = 1e-5f;
constexpr int LDS_BYTES = 160 * 1024;
constexpr unsigned WS_KV = 0;
constexpr unsigned WS_Q = WS_KV + 16 * 64 * 4096;
constexpr unsigned WS_O = WS_Q + 16 * 64 * 2048;
constexpr unsigned WS_H = WS_O + 64 * 28 * 1024;
constexpr unsigned WS_G = WS_H + 64 * 28 * 1024;
constexpr unsigned WS_END = WS_G + 64 * 112 * 1024;

struct Params {
    const void *x, *wqkv, *wo, *w1, *b1, *w2, *b2;
    void *out, *ws;
};

__device__ __forceinline__ rsrc_t mkrs(const void* p, unsigned bytes) { return __builtin_amdgcn_make_buffer_rsrc((void*)p, 0, (int)bytes, 0x00020000); }
__device__ __forceinline__ void dma16(rsrc_t rs, unsigned voff, unsigned soff, unsigned lds) {
    asm volatile("s_mov_b32 m0, %0\n\ts_nop 0\n\tbuffer_load_dwordx4 %1, %2, %3 offen lds" ::"s"(lds), "v"(voff), "s"(rs), "s"(soff) : "memory");
}
template <int N> __device__ __forceinline__ void wait_bar() { asm volatile("s_waitcnt vmcnt(%0) lgkmcnt(0)\n\ts_barrier" ::"i"(N) : "memory"); }
__device__ __forceinline__ void lds_bar() { asm volatile("s_waitcnt lgkmcnt(0)\n\ts_barrier" ::: "memory"); }
__device__ __forceinline__ void lds_wait() { asm volatile("s_waitcnt lgkmcnt(0)" ::: "memory"); }
__device__ __forceinline__ f32x16 mfma(u32x4 a, u32x4 b, f32x16 c) {
    return __builtin_amdgcn_mfma_f32_32x32x16_bf16(__builtin_bit_cast(bf8, a), __builtin_bit_cast(bf8, b), c, 0, 0, 0);
}
__device__ __forceinline__ float dot2(unsigned a, unsigned b, float c) {
    return __builtin_amdgcn_fdot2_f32_bf16(__builtin_bit_cast(bf2, a), __builtin_bit_cast(bf2, b), c, false);
}
__device__ __forceinline__ unsigned cvtpk(float lo, float hi) {
    typedef float f2 __attribute__((ext_vector_type(2)));
    return __builtin_bit_cast(unsigned, __builtin_convertvector((f2){lo, hi}, bf2));
}
__device__ __forceinline__ float halfsum(float v) {
    auto r = __builtin_amdgcn_permlane32_swap(__float_as_uint(v), __float_as_uint(v), false, false);
    return __uint_as_float(r[0]) + __uint_as_float(r[1]);
}
template <int AUX> __device__ __forceinline__ void st128(u32x4 v, rsrc_t rs, unsigned voff, unsigned soff) { __builtin_amdgcn_raw_buffer_store_b128(v, rs, voff + soff, 0, AUX); }
__device__ __forceinline__ u32x4 ldsr128(LAS char* p) { return *(LAS u32x4*)p; }
__device__ __forceinline__ u32x2 trread(LAS char* p) {
    return __builtin_bit_cast(u32x2, __builtin_amdgcn_ds_read_tr16_b64_v4i16((LAS s16x4*)p));
}
template <int ESTEP> __device__ __forceinline__ u32x4 wfrag(LAS char* p) {
    const u32x2 a = trread(p), b = trread(p + ESTEP);
    return (u32x4){a[0], a[1], b[0], b[1]};
}
__device__ __forceinline__ u32x4 pack8(const f32x16& v, int s) {
    return (u32x4){cvtpk(v[8 * s], v[8 * s + 1]), cvtpk(v[8 * s + 2], v[8 * s + 3]), cvtpk(v[8 * s + 4], v[8 * s + 5]), cvtpk(v[8 * s + 6], v[8 * s + 7])};
}
__device__ __forceinline__ float bflo(unsigned u) { return __uint_as_float(u << 16); }
__device__ __forceinline__ float bfhi(unsigned u) { return __uint_as_float(u & 0xFFFF0000u); }
constexpr unsigned ONES = 0x3F803F80u;
#define PIN(x) asm volatile("" : "+v"(x))
#define SBAR() __builtin_amdgcn_sched_barrier(0)

constexpr int P1_XS = 0, P1_WS = 96 * 1024, P1_MISC = 152 * 1024, P1_WQ = 16 * 1024;

template <bool VT>
__device__ __forceinline__ void p1_compute(const Params& p, LAS char* L, rsrc_t rws, int w, int lane, int rb, int pair, int head) {
    const rsrc_t rx = mkrs(p.x, S * D * 2);
    const int r = lane & 31, hh = lane >> 5;
    unsigned vx[2];
#pragma unroll
    for (int i = 0; i < 2; ++i) vx[i] = (lane >> 3) * 896 + (((lane & 7) ^ ((4 * i + (lane >> 4)) & 7)) << 4);
    const unsigned xrow = (rb * 128 + w * 32) * 896;
#pragma unroll
    for (int c = 0; c < 6; ++c)
#pragma unroll
        for (int i = 0; i < 4; ++i) dma16(rx, vx[i & 1], xrow + i * 8 * 896 + c * 128, P1_XS + c * 16384 + w * 4096 + i * 1024);
    LAS char* xa[4];
#pragma unroll
    for (int uu = 0; uu < 4; ++uu) xa[uu] = L + P1_XS + w * 4096 + r * 128 + (((2 * uu + hh) ^ ((r >> 1) & 7)) << 4);
    const int q = (lane & 15) >> 2, pp = lane & 3, g1 = (lane >> 4) & 1;
    LAS char* wb[2];
#pragma unroll
    for (int t = 0; t < 2; ++t) wb[t] = L + P1_WS + (8 * hh + q) * 128 + (((7 * t + 4 * g1 + pp) ^ ((q >> 1) << 3)) << 3);
    u32x4 xf[28];
    f32x16 acc[2] = {};
    float sx[2] = {}, sxx[2] = {}, cs[2][2] = {};
#pragma unroll
    for (int c = 0; c < 7; ++c) {
        if (c == 0 || c == 1) wait_bar<20>();
        else if (c == 2) wait_bar<16>();
        else if (c == 3) wait_bar<12>();
        else if (c == 4) wait_bar<8>();
        else if (c == 5) wait_bar<4>();
        else wait_bar<0>();
#pragma unroll
        for (int uu = 0; uu < 4; ++uu) xf[4 * c + uu] = ldsr128(xa[uu] + (c % 6) * 16384);
        if (c == 0) {
            lds_wait();
#pragma unroll
            for (int i = 0; i < 4; ++i) dma16(rx, vx[i & 1], xrow + i * 8 * 896 + 6 * 128, P1_XS + w * 4096 + i * 1024);
        }
#pragma unroll
        for (int uu = 0; uu < 4; ++uu) {
            const int u = 4 * c + uu;
            const u32x4 a = xf[u];
#pragma unroll
            for (int t = 0; t < 2; ++t) {
                const u32x4 b = wfrag<512>(wb[t] + u * 2048);
                acc[t] = VT ? mfma(a, b, acc[t]) : mfma(b, a, acc[t]);
#pragma unroll
                for (int j = 0; j < 4; ++j) cs[t][j & 1] = dot2(b[j], ONES, cs[t][j & 1]);
            }
#pragma unroll
            for (int j = 0; j < 4; ++j) { sx[j & 1] = dot2(a[j], ONES, sx[j & 1]); sxx[j & 1] = dot2(a[j], a[j], sxx[j & 1]); }
        }
        PIN(cs[0][0]); PIN(cs[0][1]); PIN(cs[1][0]); PIN(cs[1][1]); PIN(sx[0]); PIN(sx[1]); PIN(sxx[0]); PIN(sxx[1]); PIN(acc[0]); PIN(acc[1]);
    }
    const float mu = halfsum(sx[0] + sx[1]) * (1.0f / D);
    const float var = fmaxf(halfsum(sxx[0] + sxx[1]) * (1.0f / D) - mu * mu, 0.f);
    const float rstd = __builtin_amdgcn_rsqf(var + EPS);
    float cst[2];
#pragma unroll
    for (int t = 0; t < 2; ++t) cst[t] = halfsum(cs[t][0] + cs[t][1]);
    LAS float* ex = (LAS float*)(L + P1_MISC + w * 512);
    const int tile = rb * 4 + w;
    if (VT) {
        ex[lane] = hh ? rstd : -mu * rstd;
        lds_wait();
        f32x4 a4[4], b4[4];
#pragma unroll
        for (int i = 0; i < 4; ++i) { a4[i] = *(LAS f32x4*)(ex + 8 * i + 4 * hh); b4[i] = *(LAS f32x4*)(ex + 32 + 8 * i + 4 * hh); }
#pragma unroll
        for (int t = 0; t < 2; ++t) {
            f32x16 v;
#pragma unroll
            for (int k = 0; k < 16; ++k) v[k] = fmaf(acc[t][k], b4[k >> 2][k & 3], a4[k >> 2][k & 3] * cst[t]);
#pragma unroll
            for (int s = 0; s < 2; ++s) {
                u32x4 o = pack8(v, s);
                if (r >= 28) { const unsigned c = r == 28 ? ONES : 0u; o = (u32x4){c, c, c, c}; }
                st128<0>(o, rws, lane * 16, WS_KV + ((2 * pair + t) * 64 + tile) * 4096 + (2 + s) * 1024);
            }
        }
    } else {
        ex[lane] = hh ? cst[1] : cst[0];
        lds_wait();
        const int rp = (r & 19) | ((r & 4) << 1) | ((r & 8) >> 1);
        const float nm = -mu * rstd;
#pragma unroll
        for (int t = 0; t < 2; ++t) {
            f32x16 v;
#pragma unroll
            for (int i = 0; i < 4; ++i) {
                const f32x4 c4 = *(LAS f32x4*)(ex + 32 * t + 8 * i + 4 * hh);
#pragma unroll
                for (int k = 0; k < 4; ++k) v[4 * i + k] = fmaf(acc[t][4 * i + k], rstd, nm * c4[k]);
            }
#pragma unroll
            for (int s = 0; s < 2; ++s) {
                u32x4 o = pack8(v, s);
                if (s == 1 && hh) { o[2] = ONES; o[3] = 0x00003F80u; }
                st128<0>(o, rws, (rp + 32 * hh) * 16, WS_KV + ((2 * pair + t) * 64 + tile) * 4096 + s * 1024);
            }
        }
    }
    lds_bar();
    LAS char* wq = L + P1_WQ + (8 * hh + q) * 64 + ((head & 1) + 4 * g1 + pp) * 8;
    f32x16 aq = {};
    float cq[2] = {};
#pragma unroll
    for (int u = 0; u < 28; ++u) {
        const u32x4 b = wfrag<256>(wq + u * 1024);
        aq = mfma(b, xf[u], aq);
#pragma unroll
        for (int j = 0; j < 4; ++j) cq[j & 1] = dot2(b[j], ONES, cq[j & 1]);
        if ((u & 3) == 3) { PIN(cq[0]); PIN(cq[1]); SBAR(); }
    }
    ex[64 + lane] = halfsum(cq[0] + cq[1]);
    lds_wait();
    const float qs = rstd * (0.18898223650461362f * 1.4426950408889634f), nmq = -mu * qs;
    f32x16 v;
#pragma unroll
    for (int i = 0; i < 4; ++i) {
        const f32x4 c4 = *(LAS f32x4*)(ex + 64 + 8 * i + 4 * hh);
#pragma unroll
        for (int k = 0; k < 4; ++k) v[4 * i + k] = fmaf(aq[4 * i + k], qs, nmq * c4[k]);
    }
#pragma unroll
    for (int s = 0; s < 2; ++s) {
        u32x4 o = pack8(v, s);
        if (s == 1 && hh) { o[2] = 0u; o[3] = 0u; }
        st128<0>(o, rws, lane * 16, WS_Q + (head * 64 + tile) * 2048 + s * 1024);
    }
}

__device__ __forceinline__ void p1_helper(const Params& p, int hw, int lane, int pair, int kind, int head) {
    const rsrc_t rw = mkrs(p.wqkv, D * 3 * D * 2);
    int piece = (lane & 7) ^ (((lane >> 4) & 1) << 2);
    if (piece == 7) piece = 6;
    const unsigned vw = (lane >> 3) * 2688 + piece * 16;
    const unsigned cb = (448 * (1 + kind) + 56 * pair) * 2;
#pragma unroll
    for (int c = 0; c < 7; ++c)
#pragma unroll
        for (int i = 0; i < 2; ++i) { const int k0 = c * 64 + (2 * hw + i) * 8; dma16(rw, vw, k0 * 2688 + cb, P1_WS + k0 * 128); }
    wait_bar<12>(); wait_bar<10>(); wait_bar<8>(); wait_bar<6>();
    const unsigned vq = (lane >> 2) * 2688 + (lane & 3) * 16;
    const unsigned qb = 56 * head - 8 * (head & 1);
#pragma unroll
    for (int i = 0; i < 7; ++i) { const int k0 = (hw * 7 + i) * 16; dma16(rw, vq, k0 * 2688 + qb, P1_WQ + k0 * 64); }
    wait_bar<11>(); wait_bar<9>(); wait_bar<7>();
    wait_bar<0>();
}

__global__ void __launch_bounds__(NT, 2) k_p1(Params p) {
    extern __shared__ __attribute__((aligned(16))) char smem[];
    LAS char* L = (LAS char*)smem;
    const int lane = threadIdx.x & 63, w = __builtin_amdgcn_readfirstlane(threadIdx.x >> 6);
    const int b = blockIdx.x, xcd = b & 7, j = b >> 3;
    const int rb = (xcd & 1) * 8 + (j & 7), sub = j >> 3, head = 4 * (xcd >> 1) + sub, pair = 2 * (xcd >> 1) + (sub >> 1), kind = sub & 1;
    const rsrc_t rws = mkrs(p.ws, WS_END);
    if (w < 4) {
        if (kind) p1_compute<true>(p, L, rws, w, lane, rb, pair, head);
        else p1_compute<false>(p, L, rws, w, lane, rb, pair, head);
    } else p1_helper(p, w - 4, lane, pair, kind, head);
}

__device__ __forceinline__ f32x16 pv(u32x4 v, f32x8 pr, f32x16 o) {
    return __builtin_amdgcn_smfmac_f32_32x32x32_bf16(__builtin_bit_cast(bf8, v), __builtin_bit_cast(bf16v, pr), o, 0xDDDD, 0, 0);
}
__device__ __forceinline__ f32x16 qk(const u32x4* k, const u32x4* q) {
    f32x16 s = {};
    s = mfma(k[0], q[0], s);
    return mfma(k[1], q[1], s);
}
__device__ __forceinline__ void softpv(f32x16 s, const u32x4* v, f32x16& o) {
#pragma unroll
    for (int i = 0; i < 16; ++i) s[i] = __builtin_amdgcn_exp2f(s[i]);
    o = pv(v[0], __builtin_shufflevector(s, s, 0, 1, 2, 3, 4, 5, 6, 7), o);
    o = pv(v[1], __builtin_shufflevector(s, s, 8, 9, 10, 11, 12, 13, 14, 15), o);
}

__global__ void __launch_bounds__(NT, 2) k_attn(Params p) {
    extern __shared__ __attribute__((aligned(16))) char smem[];
    LAS char* L = (LAS char*)smem;
    const int lane = threadIdx.x & 63, w = __builtin_amdgcn_readfirstlane(threadIdx.x >> 6);
    const int b = blockIdx.x, xcd = b & 7, j = b >> 3;
    const int rb = (xcd & 1) * 8 + (j & 7), head = 4 * (xcd >> 1) + (j >> 3);
    const rsrc_t rws = mkrs(p.ws, WS_END);
    u32x4 qf[4][2], kv[8][4];
#pragma unroll
    for (int t = 0; t < 2; ++t)
#pragma unroll
        for (int s = 0; s < 2; ++s) qf[t][s] = __builtin_amdgcn_raw_buffer_load_b128(rws, lane * 16 + s * 1024, WS_Q + (head * 64 + rb * 4 + t) * 2048, 0);
#pragma unroll
    for (int t = 0; t < 8; ++t)
#pragma unroll
        for (int s = 0; s < 4; ++s) kv[t][s] = __builtin_amdgcn_raw_buffer_load_b128(rws, lane * 16 + s * 1024, WS_KV + (head * 64 + w * 8 + t) * 4096, 0);
#pragma unroll
    for (int t = 2; t < 4; ++t)
#pragma unroll
        for (int s = 0; s < 2; ++s) qf[t][s] = __builtin_amdgcn_raw_buffer_load_b128(rws, lane * 16 + s * 1024, WS_Q + (head * 64 + rb * 4 + t) * 2048, 0);
    __builtin_amdgcn_sched_barrier(0);
    f32x16 o[4] = {};
    f32x16 s = qk(kv[0], qf[0]);
#pragma unroll
    for (int n = 0; n < 32; ++n) {
        const int qt = n < 16 ? (n & 1) : (n >> 3), kt = n < 16 ? (n >> 1) : (n & 7);
        const int m = n + 1, qn = m < 16 ? (m & 1) : (m >> 3), kn = m < 16 ? (m >> 1) : (m & 7);
        f32x16 sn;
        if (m < 32) sn = qk(kv[kn], qf[qn]);
        softpv(s, &kv[kt][2], o[qt]);
        s = sn;
        SBAR();
        if (kt == 7) {
#pragma unroll
            for (int i = 0; i < 4; ++i)
                *(LAS f32x4*)(L + ((qt * 8 + w) * 4 + i) * 1024 + lane * 16) = (f32x4){o[qt][4 * i], o[qt][4 * i + 1], o[qt][4 * i + 2], o[qt][4 * i + 3]};
        }
    }
    lds_bar();
    {
        const int qt = w >> 1, i0 = 2 * (w & 1), r = lane & 31, hh = lane >> 5;
        f32x4 a[2] = {};
        float l = 0.f;
#pragma unroll
        for (int ww = 0; ww < 8; ++ww) {
            LAS char* base = L + (qt * 8 + ww) * 4096;
            a[0] += *(LAS f32x4*)(base + i0 * 1024 + lane * 16);
            a[1] += *(LAS f32x4*)(base + (i0 + 1) * 1024 + lane * 16);
            l += *(LAS float*)(base + 3 * 1024 + (32 + r) * 16);
        }
        const float il = __builtin_amdgcn_rcpf(l);
#pragma unroll
        for (int i = 0; i < 2; ++i) {
            const int dg = 2 * (i0 + i) + hh, G = 7 * head + dg;
            const u32x2 ov = {cvtpk(a[i][0] * il, a[i][1] * il), cvtpk(a[i][2] * il, a[i][3] * il)};
            if (dg < 7) __builtin_amdgcn_raw_buffer_store_b64(ov, rws, (G >> 2) * 1024 + ((G & 1) * 32 + r) * 16 + ((G >> 1) & 1) * 8, WS_O + (rb * 4 + qt) * 28 * 1024, 0);
        }
    }
}

__device__ __forceinline__ unsigned slab_voff(int lane, int pitch) { return (lane >> 3) * pitch + (((lane & 7) ^ (((lane >> 4) & 1) << 2)) << 4); }
__device__ __forceinline__ int slab_rd(int lane, int t) {
    const int q = (lane & 15) >> 2, pp = lane & 3, g1 = (lane >> 4) & 1, hh = lane >> 5;
    return (4 * hh + q) * 128 + (((8 * t + 4 * g1 + pp) ^ ((q >> 1) << 3)) << 3);
}

__global__ void __launch_bounds__(NT, 2) k_oproj(Params p) {
    extern __shared__ __attribute__((aligned(16))) char smem[];
    LAS char* L = (LAS char*)smem;
    const int lane = threadIdx.x & 63, w = __builtin_amdgcn_readfirstlane(threadIdx.x >> 6);
    const int b = blockIdx.x, xcd = b & 7, j = b >> 3;
    if (j >= 28) return;
    const int r4 = j / 7, ct = j % 7;
    const rsrc_t rws = mkrs(p.ws, WS_END);
    constexpr int RED = 64 * 1024;
    if (w >= 4) {
        const rsrc_t rw = mkrs(p.wo, D * D * 2);
        const unsigned vw = slab_voff(lane, 896);
#pragma unroll
        for (int i = 0; i < 14; ++i) { const int k0 = (i * 4 + (w - 4)) * 8; dma16(rw, vw, k0 * 896 + ct * 128, k0 * 128); }
        wait_bar<0>();
        lds_bar();
        return;
    }
    const int m = w & 1, kh = w >> 1, r = lane & 31, hh = lane >> 5;
    const int rt = xcd * 8 + r4 * 2 + m;
    u32x4 af[14];
#pragma unroll
    for (int u = 0; u < 14; ++u) af[u] = __builtin_amdgcn_raw_buffer_load_b128(rws, lane * 16, WS_O + (rt * 28 + kh * 14 + u) * 1024, 0);
    const rsrc_t rx = mkrs(p.x, S * D * 2);
    u32x2 xr[4];
#pragma unroll
    for (int i = 0; i < 4; ++i) xr[i] = __builtin_amdgcn_raw_buffer_load_b64(rx, r * 896 + hh * 8 + i * 16, rt * 32 * 896 + ct * 128 + kh * 64, 0);
    __builtin_amdgcn_sched_barrier(0);
    lds_bar();
    f32x16 acc[2] = {};
#pragma unroll
    for (int u = 0; u < 14; ++u)
#pragma unroll
        for (int t = 0; t < 2; ++t) acc[t] = mfma(wfrag<1024>(L + slab_rd(lane, t) + kh * 14 * 2048 + u * 2048), af[u], acc[t]);
    const f32x16 snd = kh ? acc[0] : acc[1];
    f32x16 hv = kh ? acc[1] : acc[0];
#pragma unroll
    for (int i = 0; i < 4; ++i) *(LAS f32x4*)(L + RED + (w * 4 + i) * 1024 + lane * 16) = (f32x4){snd[4 * i], snd[4 * i + 1], snd[4 * i + 2], snd[4 * i + 3]};
    lds_bar();
#pragma unroll
    for (int i = 0; i < 4; ++i) {
        const f32x4 o = *(LAS f32x4*)(L + RED + ((w ^ 2) * 4 + i) * 1024 + lane * 16);
        hv[4 * i] += o[0] + bflo(xr[i][0]); hv[4 * i + 1] += o[1] + bfhi(xr[i][0]);
        hv[4 * i + 2] += o[2] + bflo(xr[i][1]); hv[4 * i + 3] += o[3] + bfhi(xr[i][1]);
    }
#pragma unroll
    for (int s = 0; s < 2; ++s) st128<0>(pack8(hv, s), rws, lane * 16, WS_H + (rt * 28 + ct * 4 + kh * 2 + s) * 1024);
}

__global__ void __launch_bounds__(NT, 2) k_fc1(Params p) {
    extern __shared__ __attribute__((aligned(16))) char smem[];
    LAS char* L = (LAS char*)smem;
    const int lane = threadIdx.x & 63, w = __builtin_amdgcn_readfirstlane(threadIdx.x >> 6);
    const int b = blockIdx.x, xcd = b & 7, j = b >> 3;
    if (j >= 28) return;
    const int r2 = j / 14, ct = j % 14, r = lane & 31, hh = lane >> 5;
    const rsrc_t rws = mkrs(p.ws, WS_END);
    constexpr int SLAB = 448 * 128, CS = 2 * SLAB;
    if (w >= 4) {
        const int hw = w - 4;
        const rsrc_t rw = mkrs(p.w1, D * F * 2);
        const unsigned vw = slab_voff(lane, 3584);
#pragma unroll
        for (int hf = 0; hf < 2; ++hf)
#pragma unroll
            for (int i = 0; i < 14; ++i) { const int k0 = (i * 4 + hw) * 8; dma16(rw, vw, k0 * 3584 + ct * 256 + hf * 128, hf * SLAB + k0 * 128); }
        const unsigned short bb = ((const unsigned short*)p.b1)[ct * 128 + hw * 32 + r];
        wait_bar<14>();
        wait_bar<0>();
        float c2[2] = {};
#pragma unroll
        for (int u = 0; u < 28; ++u) {
            const u32x4 f = wfrag<1024>(L + (hw >> 1) * SLAB + slab_rd(lane, hw & 1) + u * 2048);
#pragma unroll
            for (int k = 0; k < 4; ++k) c2[k & 1] = dot2(f[k], ONES, c2[k & 1]);
        }
        const float c = halfsum(c2[0] + c2[1]);
        ((LAS float*)(L + CS))[hh * 128 + hw * 32 + r] = hh ? bflo(bb) : c;
        lds_bar();
        return;
    }
    const int rt = xcd * 8 + r2 * 4 + w;
    u32x4 af[28];
#pragma unroll
    for (int u = 0; u < 28; ++u) af[u] = __builtin_amdgcn_raw_buffer_load_b128(rws, lane * 16, WS_H + (rt * 28 + u) * 1024, 0);
    __builtin_amdgcn_sched_barrier(0);
    lds_bar();
    f32x16 acc[4] = {};
    float sx[2] = {}, sxx[2] = {};
#pragma unroll
    for (int u = 0; u < 28; ++u) {
#pragma unroll
        for (int t = 0; t < 2; ++t) acc[t] = mfma(wfrag<1024>(L + slab_rd(lane, t) + u * 2048), af[u], acc[t]);
#pragma unroll
        for (int k = 0; k < 4; ++k) { sx[k & 1] = dot2(af[u][k], ONES, sx[k & 1]); sxx[k & 1] = dot2(af[u][k], af[u][k], sxx[k & 1]); }
    }
    lds_bar();
#pragma unroll
    for (int t = 2; t < 4; ++t)
#pragma unroll
        for (int u = 0; u < 28; ++u) acc[t] = mfma(wfrag<1024>(L + SLAB + slab_rd(lane, t & 1) + u * 2048), af[u], acc[t]);
    lds_bar();
    const float mu = halfsum(sx[0] + sx[1]) * (1.0f / D);
    const float var = fmaxf(halfsum(sxx[0] + sxx[1]) * (1.0f / D) - mu * mu, 0.f);
    const float r1 = __builtin_amdgcn_rsqf(var + EPS);
    const float rs = r1 * __builtin_amdgcn_rsqf(var * r1 * r1 + EPS), nm = -mu * rs;
    constexpr float C0 = -2.0f * 0.7978845608028654f * 1.4426950408889634f, C1 = C0 * 0.044715f;
#pragma unroll
    for (int t = 0; t < 4; ++t) {
        f32x16 v;
#pragma unroll
        for (int i = 0; i < 4; ++i) {
            const f32x4 c4 = *(LAS f32x4*)(L + CS + (t * 32 + 8 * i + 4 * hh) * 4), b4 = *(LAS f32x4*)(L + CS + (128 + t * 32 + 8 * i + 4 * hh) * 4);
#pragma unroll
            for (int k = 0; k < 4; ++k) {
                const float u = fmaf(acc[t][4 * i + k], rs, fmaf(nm, c4[k], b4[k]));
                const float e = __builtin_amdgcn_exp2f(u * fmaf(u * u, C1, C0));
                v[4 * i + k] = u * __builtin_amdgcn_rcpf(1.0f + e);
            }
        }
#pragma unroll
        for (int s = 0; s < 2; ++s) st128<0>(pack8(v, s), rws, lane * 16, WS_G + (rt * 112 + ct * 8 + t * 2 + s) * 1024);
    }
}

template <int C> __device__ __forceinline__ void fc2_helper_step(rsrc_t rw, unsigned vw, int hw, int ct) {
    constexpr int issued = (9 + (C > 1 ? C - 1 : 0)) < 14 ? (9 + (C > 1 ? C - 1 : 0)) : 14;
    wait_bar<4 * (issued - (C + 1))>();
    if (C >= 1 && C + 8 < 14) {
#pragma unroll
        for (int i = 0; i < 4; ++i) { const int rr = hw * 4 + i, k0 = (rr >> 3) * 896 + (C + 8) * 64 + (rr & 7) * 8; dma16(rw, vw, k0 * 896 + ct * 128, ((C + 8) % 9) * 16384 + rr * 1024); }
    }
    if constexpr (C + 1 < 14) fc2_helper_step<C + 1>(rw, vw, hw, ct);
}

__global__ void __launch_bounds__(NT, 2) k_fc2(Params p) {
    extern __shared__ __attribute__((aligned(16))) char smem[];
    LAS char* L = (LAS char*)smem;
    const int lane = threadIdx.x & 63, w = __builtin_amdgcn_readfirstlane(threadIdx.x >> 6);
    const int b = blockIdx.x, xcd = b & 7, j = b >> 3;
    if (j >= 28) return;
    const int r4 = j / 7, ct = j % 7;
    const rsrc_t rws = mkrs(p.ws, WS_END);
    constexpr int RED = 144 * 1024;
    if (w >= 4) {
        const int hw = w - 4;
        const rsrc_t rw = mkrs(p.w2, F * D * 2);
        const unsigned vw = slab_voff(lane, 896);
#pragma unroll
        for (int c = 0; c < 9; ++c)
#pragma unroll
            for (int i = 0; i < 4; ++i) { const int rr = hw * 4 + i, k0 = (rr >> 3) * 896 + c * 64 + (rr & 7) * 8; dma16(rw, vw, k0 * 896 + ct * 128, c * 16384 + rr * 1024); }
        fc2_helper_step<0>(rw, vw, hw, ct);
        lds_bar();
        return;
    }
    const int m = w & 1, kh = w >> 1, r = lane & 31, hh = lane >> 5;
    const int rt = xcd * 8 + r4 * 2 + m;
    constexpr int WIN = 28;
    u32x4 af[WIN];
    const unsigned abase = WS_G + (rt * 112 + kh * 56) * 1024;
#pragma unroll
    for (int u = 0; u < WIN; ++u) af[u] = __builtin_amdgcn_raw_buffer_load_b128(rws, lane * 16, abase + u * 1024, 0);
    u32x4 hf[2];
#pragma unroll
    for (int s = 0; s < 2; ++s) hf[s] = __builtin_amdgcn_raw_buffer_load_b128(rws, lane * 16, WS_H + (rt * 28 + ct * 4 + kh * 2 + s) * 1024, 0);
    const rsrc_t rb2 = mkrs(p.b2, D * 2);
    u32x2 bb[4];
#pragma unroll
    for (int i = 0; i < 4; ++i) bb[i] = __builtin_amdgcn_raw_buffer_load_b64(rb2, hh * 8 + i * 16, ct * 128 + kh * 64, 0);
    __builtin_amdgcn_sched_barrier(0);
    f32x16 acc[2] = {};
#pragma unroll
    for (int c = 0; c < 14; ++c) {
        lds_bar();
#pragma unroll
        for (int uu = 0; uu < 4; ++uu) {
            const int u = 4 * c + uu;
            const u32x4 a = af[u % WIN];
#pragma unroll
            for (int t = 0; t < 2; ++t) acc[t] = mfma(wfrag<1024>(L + slab_rd(lane, t) + kh * 8192 + (c % 9) * 16384 + uu * 2048), a, acc[t]);
            if (u + WIN < 56) af[u % WIN] = __builtin_amdgcn_raw_buffer_load_b128(rws, lane * 16, abase + (u + WIN) * 1024, 0);
        }
    }
    const f32x16 snd = kh ? acc[0] : acc[1];
    f32x16 v = kh ? acc[1] : acc[0];
#pragma unroll
    for (int i = 0; i < 4; ++i) *(LAS f32x4*)(L + RED + (w * 4 + i) * 1024 + lane * 16) = (f32x4){snd[4 * i], snd[4 * i + 1], snd[4 * i + 2], snd[4 * i + 3]};
    lds_bar();
    unsigned o[8];
#pragma unroll
    for (int i = 0; i < 4; ++i) {
        const f32x4 t = *(LAS f32x4*)(L + RED + ((w ^ 2) * 4 + i) * 1024 + lane * 16);
        const unsigned h0 = hf[i >> 1][2 * (i & 1)], h1 = hf[i >> 1][2 * (i & 1) + 1];
        o[2 * i] = cvtpk(v[4 * i] + t[0] + bflo(bb[i][0]) + bflo(h0), v[4 * i + 1] + t[1] + bfhi(bb[i][0]) + bfhi(h0));
        o[2 * i + 1] = cvtpk(v[4 * i + 2] + t[2] + bflo(bb[i][1]) + bflo(h1), v[4 * i + 3] + t[3] + bfhi(bb[i][1]) + bfhi(h1));
    }
    const rsrc_t ro = mkrs(p.out, S * D * 2);
#pragma unroll
    for (int k = 0; k < 2; ++k) {
        auto s0 = __builtin_amdgcn_permlane32_swap(o[4 * k], o[4 * k + 2], false, false);
        auto s1 = __builtin_amdgcn_permlane32_swap(o[4 * k + 1], o[4 * k + 3], false, false);
        st128<0>((u32x4){s0[0], s1[0], s0[1], s1[1]}, ro, r * 896 + hh * 16 + k * 32, rt * 32 * 896 + ct * 128 + kh * 64);
    }
}

extern "C" void kernel_launch(void* const* d_in, const int* in_sizes, int n_in, void* d_out, int out_size, void* d_ws, size_t ws_size, hipStream_t stream) {
    Params p;
    p.x = d_in[0]; p.wqkv = d_in[1]; p.wo = d_in[2]; p.w1 = d_in[4]; p.b1 = d_in[5]; p.w2 = d_in[6]; p.b2 = d_in[7];
    p.out = d_out; p.ws = d_ws;
    (void)hipFuncSetAttribute((const void*)k_p1, hipFuncAttributeMaxDynamicSharedMemorySize, LDS_BYTES);
    (void)hipFuncSetAttribute((const void*)k_attn, hipFuncAttributeMaxDynamicSharedMemorySize, LDS_BYTES);
    (void)hipFuncSetAttribute((const void*)k_oproj, hipFuncAttributeMaxDynamicSharedMemorySize, LDS_BYTES);
    (void)hipFuncSetAttribute((const void*)k_fc1, hipFuncAttributeMaxDynamicSharedMemorySize, LDS_BYTES);
    (void)hipFuncSetAttribute((const void*)k_fc2, hipFuncAttributeMaxDynamicSharedMemorySize, LDS_BYTES);
    k_p1<<<NWG, NT, LDS_BYTES, stream>>>(p);
    k_attn<<<NWG, NT, LDS_BYTES, stream>>>(p);
    k_oproj<<<NWG, NT, LDS_BYTES, stream>>>(p);
    k_fc1<<<NWG, NT, LDS_BYTES, stream>>>(p);
    k_fc2<<<NWG, NT, LDS_BYTES, stream>>>(p);
}
```

```cpp
#include <hip/hip_runtime.h>
#include <stdint.h>
#include <utility>

#define LAS __attribute__((address_space(3)))
#define GAS __attribute__((address_space(1)))
typedef __bf16 bf2 __attribute__((ext_vector_type(2)));
typedef __bf16 bf8 __attribute__((ext_vector_type(8)));
typedef __bf16 bf16v __attribute__((ext_vector_type(16)));
typedef float f32x4 __attribute__((ext_vector_type(4)));
typedef float f32x8 __attribute__((ext_vector_type(8)));
typedef float f32x16 __attribute__((ext_vector_type(16)));
typedef unsigned u32x2 __attribute__((ext_vector_type(2)));
typedef unsigned u32x4 __attribute__((ext_vector_type(4)));
typedef short s16x4 __attribute__((ext_vector_type(4)));
typedef __amdgpu_buffer_rsrc_t rsrc_t;

constexpr int S = 2048, D = 448, H = 16, F = 1792;
constexpr int NT = 512, NWG = 256;
constexpr float EPS = 1e-5f;
constexpr int LDS_BYTES = 160 * 1024;
constexpr int SC1 = 16;
constexpr unsigned WS_GEN = 0;
constexpr unsigned WS_F1 = 4096;
constexpr unsigned WS_F2 = WS_F1 + 256 * 128;
constexpr unsigned WS_F3 = WS_F2 + 256 * 128;
constexpr unsigned WS_F4 = WS_F3 + 256 * 128;
constexpr unsigned WS_HS = WS_F4 + 256 * 128;
constexpr unsigned WS_KV = 512 * 1024;
constexpr unsigned WS_O = WS_KV + 16 * 64 * 4096;
constexpr unsigned WS_H = WS_O + 64 * 28 * 1024;
constexpr unsigned WS_G = WS_H + 64 * 28 * 1024;
constexpr unsigned WS_END = WS_G + 64 * 112 * 1024;

struct Params {
    const void *x, *wqkv, *wo, *w1, *b1, *w2, *b2;
    void *out, *ws;
};

template <class Fn, int... I> __device__ __forceinline__ void sfor_(Fn&& f, std::integer_sequence<int, I...>) { (f(std::integral_constant<int, I>{}), ...); }
template <int N, class Fn> __device__ __forceinline__ void sfor(Fn&& f) { sfor_(f, std::make_integer_sequence<int, N>{}); }
#define INL __attribute__((always_inline))
__device__ __forceinline__ rsrc_t mkrs(const void* p, unsigned bytes) { return __builtin_amdgcn_make_buffer_rsrc((void*)p, 0, (int)bytes, 0x00020000); }
__device__ __forceinline__ void dma16(rsrc_t rs, unsigned voff, unsigned soff, unsigned lds) {
    asm volatile("s_mov_b32 m0, %0\n\ts_nop 0\n\tbuffer_load_dwordx4 %1, %2, %3 offen lds" ::"s"(lds), "v"(voff), "s"(rs), "s"(soff) : "memory");
}
__device__ __forceinline__ void dma4(rsrc_t rs, unsigned voff, unsigned soff, unsigned lds) {
    asm volatile("s_mov_b32 m0, %0\n\ts_nop 0\n\tbuffer_load_dword %1, %2, %3 offen lds" ::"s"(lds), "v"(voff), "s"(rs), "s"(soff) : "memory");
}
template <int N> __device__ __forceinline__ void wait_bar() { asm volatile("s_waitcnt vmcnt(%0) lgkmcnt(0)\n\ts_barrier" ::"i"(N) : "memory"); }
template <int N> __device__ __forceinline__ void vm_wait() { asm volatile("s_waitcnt vmcnt(%0)" ::"i"(N) : "memory"); }
__device__ __forceinline__ void lds_bar() { asm volatile("s_waitcnt lgkmcnt(0)\n\ts_barrier" ::: "memory"); }
__device__ __forceinline__ void bar() { asm volatile("s_barrier" ::: "memory"); }
__device__ __forceinline__ void lds_wait() { asm volatile("s_waitcnt lgkmcnt(0)" ::: "memory"); }
__device__ __forceinline__ f32x16 mfma(u32x4 a, u32x4 b, f32x16 c) {
    return __builtin_amdgcn_mfma_f32_32x32x16_bf16(__builtin_bit_cast(bf8, a), __builtin_bit_cast(bf8, b), c, 0, 0, 0);
}
__device__ __forceinline__ float dot2(unsigned a, unsigned b, float c) {
    return __builtin_amdgcn_fdot2_f32_bf16(__builtin_bit_cast(bf2, a), __builtin_bit_cast(bf2, b), c, false);
}
__device__ __forceinline__ unsigned cvtpk(float lo, float hi) {
    typedef float f2 __attribute__((ext_vector_type(2)));
    return __builtin_bit_cast(unsigned, __builtin_convertvector((f2){lo, hi}, bf2));
}
__device__ __forceinline__ float halfsum(float v) {
    auto r = __builtin_amdgcn_permlane32_swap(__float_as_uint(v), __float_as_uint(v), false, false);
    return __uint_as_float(r[0]) + __uint_as_float(r[1]);
}
template <int AUX> __device__ __forceinline__ void st128(u32x4 v, rsrc_t rs, unsigned voff, unsigned soff) { __builtin_amdgcn_raw_buffer_store_b128(v, rs, voff + soff, 0, AUX); }
__device__ __forceinline__ u32x4 ldh(rsrc_t rs, unsigned voff, unsigned soff) { return __builtin_amdgcn_raw_buffer_load_b128(rs, voff, soff, SC1); }
__device__ __forceinline__ u32x4 ldsr128(LAS char* p) { return *(LAS u32x4*)p; }
__device__ __forceinline__ u32x2 trread(LAS char* p) {
    return __builtin_bit_cast(u32x2, __builtin_amdgcn_ds_read_tr16_b64_v4i16((LAS s16x4*)p));
}
template <int ESTEP> __device__ __forceinline__ u32x4 wfrag(LAS char* p) {
    const u32x2 a = trread(p), b = trread(p + ESTEP);
    return (u32x4){a[0], a[1], b[0], b[1]};
}
__device__ __forceinline__ u32x4 pack8(const f32x16& v, int s) {
    return (u32x4){cvtpk(v[8 * s], v[8 * s + 1]), cvtpk(v[8 * s + 2], v[8 * s + 3]), cvtpk(v[8 * s + 4], v[8 * s + 5]), cvtpk(v[8 * s + 6], v[8 * s + 7])};
}
__device__ __forceinline__ float bflo(unsigned u) { return __uint_as_float(u << 16); }
__device__ __forceinline__ float bfhi(unsigned u) { return __uint_as_float(u & 0xFFFF0000u); }
constexpr unsigned ONES = 0x3F803F80u;
#define PIN(x) asm volatile("" : "+v"(x))
#define SBAR() __builtin_amdgcn_sched_barrier(0)
#define ST(id)
#define ST_DECL
#define ST_ARG
#define ST_PASS
#define ST_FLUSH()

__device__ __forceinline__ void set_flag(rsrc_t rws, unsigned off, unsigned tag, bool me) {
    __builtin_amdgcn_raw_buffer_store_b32(tag, rws, me ? off : 0x7FFFFFF0u, 0, SC1);
}
__device__ __forceinline__ bool flags_ok(u32x4 f, unsigned tag) { return __all(f[0] == tag && f[1] == tag && f[2] == tag && f[3] == tag); }
__device__ __forceinline__ void wait_flags(rsrc_t rws, unsigned off, unsigned tag) {
    for (unsigned spins = 0; spins < (1u << 20); ++spins) {
        asm volatile("" ::: "memory");
        if (flags_ok(ldh(rws, off, 0), tag)) break;
        __builtin_amdgcn_s_sleep(2);
    }
    asm volatile("" ::: "memory");
}
__device__ __forceinline__ unsigned load_tag(const Params& p) {
    unsigned g;
    asm volatile("s_load_dword %0, %1, 0x0\n\ts_waitcnt lgkmcnt(0)" : "=s"(g) : "s"(p.ws) : "memory");
    return g + 1;
}
__device__ __forceinline__ int mod7(int v) { return v >= 7 ? v - 7 : v; }

constexpr int K1 = 1024;
constexpr int P1_XS = 0, P1_WS = 64 * K1, P1_WQ = 120 * K1, P1_EX = 148 * K1;
constexpr int L_P = 32 * K1, L_WO = 96 * K1, L_QF = 152 * K1;
constexpr int L_W1 = 0, SLAB = 56 * K1, L_RED1 = 80 * K1, L_EXT = 156 * K1, L_RED2 = 144 * K1;
__host__ __device__ constexpr int w2slot(int c) { return ((c % 9) < 2 ? 112 + 16 * (c % 9) : 16 * ((c % 9) - 2)) * K1; }

__device__ __forceinline__ int p1_wrd(int lane, int t) {
    const int q = (lane & 15) >> 2, pp = lane & 3, g1 = (lane >> 4) & 1, hh = lane >> 5;
    return P1_WS + (8 * hh + q) * 128 + (((7 * t + 4 * g1 + pp) ^ ((q >> 1) << 3)) << 3);
}
template <bool VT>
__device__ __forceinline__ void p1_compute(LAS char* L, rsrc_t rws, int w, int lane, int rb, int pair, int head, unsigned tag ST_ARG) {
    const int r = lane & 31, hh = lane >> 5;
    LAS char* xa[4];
#pragma unroll
    for (int uu = 0; uu < 4; ++uu) xa[uu] = L + P1_XS + w * 4096 + r * 128 + (((2 * uu + hh) ^ ((r >> 1) & 7)) << 4);
    LAS char* wb[2] = {L + p1_wrd(lane, 0), L + p1_wrd(lane, 1)};
    u32x4 ones = {ONES, ONES, ONES, ONES};
    PIN(ones);
    u32x4 xf[28];
    f32x16 acc[2] = {}, sa = {};
    float sxx[2] = {};
    u32x4 bf[4][2];
    auto RD = [&](int u) INL {
        xf[u] = ldsr128(xa[u & 3] + ((u >> 2) & 3) * 16384);
        bf[u & 3][0] = wfrag<512>(wb[0] + u * 2048);
        bf[u & 3][1] = wfrag<512>(wb[1] + u * 2048);
        SBAR();
    };
    auto MM = [&](int u) INL {
        const u32x4 a = xf[u];
        sa = mfma(ones, a, sa); PIN(sa); SBAR();
        sxx[0] = dot2(a[0], a[0], sxx[0]); PIN(sxx[0]); SBAR();
        acc[0] = VT ? mfma(a, bf[u & 3][0], acc[0]) : mfma(bf[u & 3][0], a, acc[0]); PIN(acc[0]); SBAR();
        sxx[1] = dot2(a[1], a[1], sxx[1]); sxx[0] = dot2(a[2], a[2], sxx[0]); PIN(sxx[0]); PIN(sxx[1]); SBAR();
        acc[1] = VT ? mfma(a, bf[u & 3][1], acc[1]) : mfma(bf[u & 3][1], a, acc[1]); PIN(acc[1]); SBAR();
        sxx[1] = dot2(a[3], a[3], sxx[1]); PIN(sxx[1]); SBAR();
    };
    sfor<7>([&](auto ic) INL {
        constexpr int c = ic.value;
        bar();
        if (c == 0) ST(1);
        RD(4 * c); RD(4 * c + 1);
        if constexpr (c > 0) MM(4 * c - 1);
        RD(4 * c + 2); MM(4 * c);
        RD(4 * c + 3); MM(4 * c + 1); MM(4 * c + 2);
    });
    MM(27);
    ST(2);
    const float mu = sa[0] * (1.0f / D);
    const float var = fmaxf(halfsum(sxx[0] + sxx[1]) * (1.0f / D) - mu * mu, 0.f);
    const float rstd = __builtin_amdgcn_rsqf(var + EPS);
    LAS float* ex = (LAS float*)(L + P1_EX + w * 256);
    LAS float* cs = (LAS float*)(L + P1_EX + 1024);
    const int tile = rb * 4 + w;
    if (VT) ex[lane] = hh ? rstd : -mu * rstd;
    lds_bar();
    if (VT) {
        f32x4 a4[4], b4[4];
#pragma unroll
        for (int i = 0; i < 4; ++i) { a4[i] = *(LAS f32x4*)(ex + 8 * i + 4 * hh); b4[i] = *(LAS f32x4*)(ex + 32 + 8 * i + 4 * hh); }
#pragma unroll
        for (int t = 0; t < 2; ++t) {
            const float c = cs[32 * t + r];
            f32x16 v;
#pragma unroll
            for (int k = 0; k < 16; ++k) v[k] = fmaf(acc[t][k], b4[k >> 2][k & 3], a4[k >> 2][k & 3] * c);
#pragma unroll
            for (int s = 0; s < 2; ++s) {
                u32x4 o = pack8(v, s);
                if (r >= 28) { const unsigned z = r == 28 ? ONES : 0u; o = (u32x4){z, z, z, z}; }
                st128<SC1>(o, rws, lane * 16, WS_KV + ((2 * pair + t) * 64 + tile) * 4096 + (2 + s) * 1024);
            }
        }
    } else {
        const int rp = (r & 19) | ((r & 4) << 1) | ((r & 8) >> 1);
        const float nm = -mu * rstd;
#pragma unroll
        for (int t = 0; t < 2; ++t) {
            f32x16 v;
#pragma unroll
            for (int i = 0; i < 4; ++i) {
                const f32x4 c4 = *(LAS f32x4*)(cs + 32 * t + 8 * i + 4 * hh);
#pragma unroll
                for (int k = 0; k < 4; ++k) v[4 * i + k] = fmaf(acc[t][4 * i + k], rstd, nm * c4[k]);
            }
#pragma unroll
            for (int s = 0; s < 2; ++s) {
                u32x4 o = pack8(v, s);
                if (s == 1 && hh) { o[2] = 0u; o[3] = 0u; }
                st128<SC1>(o, rws, (rp + 32 * hh) * 16, WS_KV + ((2 * pair + t) * 64 + tile) * 4096 + s * 1024);
            }
        }
    }
    ST(3);
    lds_bar();
    LAS char* wq = L + P1_WQ + (8 * hh + ((lane & 15) >> 2)) * 64 + ((head & 1) + 4 * ((lane >> 4) & 1) + (lane & 3)) * 8;
    f32x16 aq = {}, cq = {};
    u32x4 bq[4];
    bq[0] = wfrag<256>(wq); bq[1] = wfrag<256>(wq + 1024); bq[2] = wfrag<256>(wq + 2048);
    sfor<28>([&](auto ic) INL {
        constexpr int u = ic.value;
        if constexpr (u + 3 < 28) bq[(u + 3) & 3] = wfrag<256>(wq + (u + 3) * 1024);
        SBAR();
        aq = mfma(bq[u & 3], xf[u], aq);
        cq = mfma(bq[u & 3], ones, cq);
        PIN(aq); PIN(cq); SBAR();
        if constexpr (u == 11) { vm_wait<0>(); set_flag(rws, WS_F1 + ((pair * 16 + rb) * 2 + (VT ? 1 : 0)) * 128 + w * 4, tag, lane == 0); SBAR(); }
    });
    const float qs = rstd * (0.18898223650461362f * 1.4426950408889634f), nmq = -mu * qs;
    f32x16 v;
#pragma unroll
    for (int k = 0; k < 16; ++k) v[k] = fmaf(aq[k], qs, nmq * cq[k]);
#pragma unroll
    for (int s = 0; s < 2; ++s) {
        u32x4 o = pack8(v, s);
        if (s == 1 && hh) { o[2] = 0u; o[3] = 0u; }
        *(LAS u32x4*)(L + L_QF + (w * 2 + s) * 1024 + lane * 16) = o;
    }
}

__device__ __forceinline__ void p1_loader(const Params& p, int lw, int lane, int rb, int pair, int kind, int head, int rot, unsigned& tag) {
    const rsrc_t rx = mkrs(p.x, S * D * 2), rw = mkrs(p.wqkv, D * 3 * D * 2);
    unsigned vx[2];
#pragma unroll
    for (int i = 0; i < 2; ++i) vx[i] = (lane >> 3) * 896 + (((lane & 7) ^ ((4 * i + (lane >> 4)) & 7)) << 4);
    int piece = (lane & 7) ^ (((lane >> 4) & 1) << 2);
    if (piece == 7) piece = 6;
    const unsigned vw = (lane >> 3) * 2688 + piece * 16;
    const unsigned xb = (rb * 128 + lw * 64) * 896, wb = lw * 32 * 2688 + (448 * (1 + kind) + 56 * pair) * 2;
    auto chunk = [&](int c) INL {
        const int cc = mod7(c + rot);
#pragma unroll
        for (int i = 0; i < 4; ++i) dma16(rw, vw, wb + (cc * 64 + i * 8) * 2688, P1_WS + (c * 64 + lw * 32 + i * 8) * 128);
#pragma unroll
        for (int i = 0; i < 8; ++i) dma16(rx, vx[i & 1], xb + i * 8 * 896 + cc * 128, P1_XS + (c & 3) * 16384 + lw * 8192 + i * 1024);
    };
    const unsigned vq = (lane >> 2) * 2688 + (lane & 3) * 16;
    const unsigned qb = 56 * head - 8 * (head & 1) + lw * 32 * 2688;
    auto wq = [&](int c) INL {
#pragma unroll
        for (int i = 0; i < 2; ++i) dma16(rw, vq, (mod7(c + rot) * 64 + i * 16) * 2688 + qb, P1_WQ + (c * 64 + lw * 32 + i * 16) * 64);
    };
    chunk(0);
#pragma unroll
    for (int c = 0; c < 6; ++c) { chunk(c + 1); wait_bar<12>(); }
    wq(0); wq(1); wq(2);
    wait_bar<6>();
    wq(3); wq(4); wq(5); wq(6);
    tag = load_tag(p);
    bar();
    wait_bar<0>();
}

__device__ __forceinline__ void p1_colsum(LAS char* L, rsrc_t rws, int t, int lane, int head, unsigned tag) {
    LAS char* wb = L + p1_wrd(lane, t);
    float c2[2] = {};
#pragma unroll
    for (int c = 0; c < 7; ++c) {
        lds_bar();
#pragma unroll
        for (int uu = 0; uu < 4; ++uu) {
            const u32x4 b = wfrag<512>(wb + (4 * c + uu) * 2048);
#pragma unroll
            for (int j = 0; j < 4; ++j) c2[j & 1] = dot2(b[j], ONES, c2[j & 1]);
        }
        PIN(c2[0]); PIN(c2[1]);
    }
    ((LAS float*)(L + P1_EX + 1024))[32 * t + (lane & 31)] = halfsum(c2[0] + c2[1]);
    lds_bar();
    lds_bar();
    if (t) {
        __builtin_amdgcn_s_sleep(16);
        wait_flags(rws, WS_F1 + ((head >> 1) * 32 + (lane & 31)) * 128, tag);
    }
}

__device__ __forceinline__ f32x16 pv(u32x4 v, f32x8 pr, f32x16 o) {
    return __builtin_amdgcn_smfmac_f32_32x32x32_bf16(__builtin_bit_cast(bf8, v), __builtin_bit_cast(bf16v, pr), o, 0xDDDD, 0, 0);
}
__device__ __forceinline__ f32x8 lo8(const f32x16& s) { return __builtin_shufflevector(s, s, 0, 1, 2, 3, 4, 5, 6, 7); }
__device__ __forceinline__ f32x8 hi8(const f32x16& s) { return __builtin_shufflevector(s, s, 8, 9, 10, 11, 12, 13, 14, 15); }
__host__ __device__ constexpr int step_q(int n) { return n < 16 ? (n & 1) : (n >> 3); }
__host__ __device__ constexpr int step_k(int n) { return n < 16 ? (n >> 1) : (n & 7); }
__device__ __forceinline__ unsigned slab_voff(int lane, int pitch) { return (lane >> 3) * pitch + (((lane & 7) ^ (((lane >> 4) & 1) << 2)) << 4); }
__device__ __forceinline__ int slab_rd(int lane, int t) {
    const int q = (lane & 15) >> 2, pp = lane & 3, g1 = (lane >> 4) & 1, hh = lane >> 5;
    return (4 * hh + q) * 128 + (((8 * t + 4 * g1 + pp) ^ ((q >> 1) << 3)) << 3);
}
__device__ __forceinline__ void store_o(rsrc_t rws, f32x4 a, float l, int lane, int head, int rt, int i, bool on) {
    const float il = __builtin_amdgcn_rcpf(l);
    const int r = lane & 31, dg = 2 * i + (lane >> 5), G = 7 * head + dg;
    const u32x2 ov = {cvtpk(a[0] * il, a[1] * il), cvtpk(a[2] * il, a[3] * il)};
    __builtin_amdgcn_raw_buffer_store_b64(ov, rws, dg < 7 && on ? WS_O + rt * 28 * 1024 + (G >> 2) * 1024 + ((G & 1) * 32 + r) * 16 + ((G >> 1) & 1) * 8 : 0x7FFFFFF0u, 0, SC1);
}
__device__ __forceinline__ void redo_o(LAS char* L, rsrc_t rws, int lane, int head, int rt, int qt, int ih) {
    const u32x4 q0 = ldsr128(L + L_QF + qt * 2048 + lane * 16), q1 = ldsr128(L + L_QF + qt * 2048 + 1024 + lane * 16);
    float m = -INFINITY;
#pragma unroll 1
    for (int t = 0; t < 64; ++t) {
        const unsigned b = WS_KV + (head * 64 + t) * 4096;
        f32x16 s = {};
        s = mfma(ldh(rws, lane * 16, b), q0, s);
        s = mfma(ldh(rws, lane * 16 + 1024, b), q1, s);
#pragma unroll
        for (int k = 0; k < 16; ++k) m = fmaxf(m, s[k]);
    }
    auto mm = __builtin_amdgcn_permlane32_swap(__float_as_uint(m), __float_as_uint(m), false, false);
    m = fmaxf(__uint_as_float(mm[0]), __uint_as_float(mm[1]));
    f32x16 o = {};
#pragma unroll 1
    for (int t = 0; t < 64; ++t) {
        const unsigned b = WS_KV + (head * 64 + t) * 4096;
        f32x16 s = {};
        s = mfma(ldh(rws, lane * 16, b), q0, s);
        s = mfma(ldh(rws, lane * 16 + 1024, b), q1, s);
#pragma unroll
        for (int k = 0; k < 16; ++k) s[k] = __builtin_amdgcn_exp2f(s[k] - m);
        o = pv(ldh(rws, lane * 16 + 2048, b), lo8(s), o);
        o = pv(ldh(rws, lane * 16 + 3072, b), hi8(s), o);
    }
    auto ll = __builtin_amdgcn_permlane32_swap(__float_as_uint(o[12]), __float_as_uint(o[12]), false, false);
#pragma unroll
    for (int i = 0; i < 2; ++i) {
        f32x4 a;
#pragma unroll
        for (int k = 0; k < 4; ++k) a[k] = ih ? o[8 + 4 * i + k] : o[4 * i + k];
        store_o(rws, a, __uint_as_float(ll[1]), lane, head, rt, 2 * ih + i, true);
    }
}

#define W1DMA(hf, u, e) do { int uu_ = (u) + 2 * ct1; if (uu_ >= 28) uu_ -= 28; dma16(r1, v1, (uu_ * 16 + (e) * 8) * 3584 + ct1 * 256 + (hf) * 128, L_W1 + (hf) * SLAB + (u) * 2048 + (e) * 1024); } while (0)

__device__ __forceinline__ void attention(const Params& p, LAS char* L, rsrc_t rws, int w, int lane, int rb, int head, int j, unsigned tag ST_ARG) {
    lds_bar();
    ST(5);
    const int G = w >> 2;
    if (G == 0) __builtin_amdgcn_s_setprio(1);
    u32x4 kv[8][4];
    auto kvload = [&](int t) INL {
#pragma unroll
        for (int s = 0; s < 4; ++s) kv[t][s] = ldh(rws, lane * 16 + s * 1024, WS_KV + (head * 64 + w * 8 + ((t + j) & 7)) * 4096);
        SBAR();
    };
    kvload(0); kvload(1);
    u32x4 qf[4][2];
    LAS char* qa = L + L_QF + G * 4096 + lane * 16;
    LAS char* qb = L + L_QF + (1 - G) * 4096 + lane * 16;
    LAS char* pw = L + L_P + w * 8192 + lane * 16;
    LAS char* pp = L + L_P + (w ^ 4) * 8192 + lane * 16;
#pragma unroll
    for (int t = 0; t < 2; ++t)
#pragma unroll
        for (int s = 0; s < 2; ++s) qf[t][s] = ldsr128(qa + (t * 2 + s) * 1024);
    f32x16 o[4];
    f32x16 sc[3];
    const f32x16 zero = {};
    { float z = 0.f; PIN(z);
#pragma unroll
      for (int k = 0; k < 16; ++k) { o[0][k] = z; o[1][k] = z; }
      PIN(o[0]); PIN(o[1]); }
    sc[0] = mfma(kv[0][1], qf[0][1], mfma(kv[0][0], qf[0][0], zero));
    auto put = [&](LAS char* d, const f32x16& v) INL {
#pragma unroll
        for (int i = 0; i < 4; ++i) *(LAS f32x4*)(d + i * 1024) = (f32x4){v[4 * i], v[4 * i + 1], v[4 * i + 2], v[4 * i + 3]};
        SBAR();
    };
    auto get = [&](LAS char* d, f32x16& v) INL {
#pragma unroll
        for (int i = 0; i < 4; ++i) { const f32x4 t = *(LAS f32x4*)(d + i * 1024); v[4 * i] = t[0]; v[4 * i + 1] = t[1]; v[4 * i + 2] = t[2]; v[4 * i + 3] = t[3]; }
        SBAR();
    };
    ST(6);
#define EXP4(X, i) do { X[i] = __builtin_amdgcn_exp2f(X[i]); X[i + 1] = __builtin_amdgcn_exp2f(X[i + 1]); X[i + 2] = __builtin_amdgcn_exp2f(X[i + 2]); X[i + 3] = __builtin_amdgcn_exp2f(X[i + 3]); PIN(X); SBAR(); } while (0)
    sfor<33>([&](auto ic) INL {
        constexpr int n = ic.value, qn = step_q(n + 1), kn = step_k(n + 1), qp = step_q(n > 0 ? n - 1 : 0), kp = step_k(n > 0 ? n - 1 : 0);
        f32x16 &sn = sc[(n + 1) % 3], &s = sc[n % 3], &sp = sc[(n + 2) % 3];
        if constexpr (n == 17) {
            sn = mfma(kv[kn][0], qf[qn][0], zero); PIN(sn); SBAR();
            EXP4(s, 0);
            sn = mfma(kv[kn][1], qf[qn][1], sn); PIN(sn); SBAR();
            EXP4(s, 4);
            put(pw + 4096, o[1]);
            EXP4(s, 8);
            ST(22);
            lds_bar();
            ST(23);
            get(pp, o[2]);
            EXP4(s, 12);
            o[2] = pv(kv[kp][2], lo8(sp), o[2]); o[2] = pv(kv[kp][3], hi8(sp), o[2]); PIN(o[2]); SBAR();
        } else {
            if constexpr (n + 1 < 32) { sn = mfma(kv[kn][0], qf[qn][0], zero); PIN(sn); SBAR(); }
            if constexpr (n < 32) EXP4(s, 0);
            if constexpr (n + 1 < 32) { sn = mfma(kv[kn][1], qf[qn][1], sn); PIN(sn); SBAR(); }
            if constexpr (n < 32) EXP4(s, 4);
            if constexpr (n > 0) { o[qp] = pv(kv[kp][2], lo8(sp), o[qp]); PIN(o[qp]); SBAR(); }
            if constexpr (n < 32) EXP4(s, 8);
            if constexpr (n > 0) { o[qp] = pv(kv[kp][3], hi8(sp), o[qp]); PIN(o[qp]); SBAR(); }
            if constexpr (n < 32) EXP4(s, 12);
        }
        if constexpr (n < 6) kvload(2 + n);
        if constexpr (n == 13 || n == 21) {
            constexpr int t = n == 13 ? 2 : 3;
#pragma unroll
            for (int s2 = 0; s2 < 2; ++s2) qf[t][s2] = ldsr128(qb + ((t & 1) * 2 + s2) * 1024);
            SBAR();
        }
        if constexpr (n == 16) put(pw, o[0]);
        if constexpr (n == 22) get(pp + 4096, o[3]);
        if constexpr (n == 25) put(pp, o[2]);
        if constexpr (n == 32) put(pp + 4096, o[3]);
        if constexpr (n == 18 || n == 21 || n == 24) {
            const rsrc_t rt = n == 18 ? mkrs(p.wo, D * D * 2) : n == 21 ? mkrs(p.w1, D * F * 2) : mkrs(p.w2, F * D * 2);
            constexpr int NL = n == 18 ? 13 : 49;
            dma4(rt, lane < NL ? lane * 128 : 0x7FFFFF00u, (j * 8 + w) * NL * 128, P1_EX + w * 256);
            SBAR();
        }
    });
    ST(7);
    if (G == 0) {
        const rsrc_t rw = mkrs(p.wo, D * D * 2), r1 = mkrs(p.w1, D * F * 2);
        const unsigned vw = slab_voff(lane, 896), v1 = slab_voff(lane, 3584);
        const int ct = j % 7, ct1 = j % 14;
#pragma unroll
        for (int i = 0; i < 13; ++i) {
            const int pos = i * 4 + w, kh = pos >= 28, u = (pos >> 1) - 14 * kh;
            int uu = u + 2 * ct; if (uu >= 14) uu -= 14;
            dma16(rw, vw, ((kh * 14 + uu) * 16 + (pos & 1) * 8) * 896 + ct * 128, L_WO + pos * 1024);
        }
#pragma unroll
        for (int i = 0; i < 8; ++i) W1DMA(0, i * 2 + (w >> 1), w & 1);
        __builtin_amdgcn_s_setprio(0);
    }
    wait_bar<0>();
    ST(8);
    if (G == 0) {
        const int ct = j % 7, pos = 52 + w, u = (pos >> 1) - 14;
        int uu = u + 2 * ct; if (uu >= 14) uu -= 14;
        dma16(mkrs(p.wo, D * D * 2), slab_voff(lane, 896), ((14 + uu) * 16 + (pos & 1) * 8) * 896 + ct * 128, L_WO + pos * 1024);
    }
    {
        const int qt = w >> 1, ih = w & 1;
        LAS char* P = L + L_P + (qt >> 1) * 32768 + (qt & 1) * 4096;
        f32x4 a[2] = {};
        float l = 0.f;
#pragma unroll
        for (int ww = 0; ww < 4; ++ww) {
            a[0] += *(LAS f32x4*)(P + ww * 8192 + ih * 2048 + lane * 16);
            a[1] += *(LAS f32x4*)(P + ww * 8192 + ih * 2048 + 1024 + lane * 16);
            l += *(LAS float*)(P + ww * 8192 + 3 * 1024 + (32 + (lane & 31)) * 16);
        }
        const float asum = (fabsf(a[0][0]) + fabsf(a[0][1])) + (fabsf(a[0][2]) + fabsf(a[0][3])) + (fabsf(a[1][0]) + fabsf(a[1][1])) + (fabsf(a[1][2]) + fabsf(a[1][3]));
        const bool ok = __all(l >= 0x1p-40f && l <= 0x1p40f && asum <= 0x1p120f);
        store_o(rws, a[0], l, lane, head, rb * 4 + qt, 2 * ih, ok);
        store_o(rws, a[1], l, lane, head, rb * 4 + qt, 2 * ih + 1, ok);
        if (__builtin_expect(!ok, 0)) redo_o(L, rws, lane, head, rb * 4 + qt, qt, ih);
    }
    lds_bar();
    vm_wait<0>();
    set_flag(rws, WS_F2 + (rb * 16 + head) * 128 + w * 4, tag, lane == 0);
    ST(9);
}

__device__ __forceinline__ int fc2_kstep(int p, int rot) { return mod7((p % 14) / 2 + rot) * 8 + (p / 14) * 2 + (p & 1); }
__device__ __forceinline__ unsigned w2src(int c, int kh, int pi, int e, int ct) { return ((kh * 56 + fc2_kstep(4 * c + pi, ct)) * 16 + e * 8) * 896 + ct * 128; }
__device__ __forceinline__ unsigned bfsplit(float v, float& rest) { const unsigned h = cvtpk(v, v) & 0xFFFFu; rest = v - bflo(h); return h; }

__device__ __forceinline__ void mlp_helper(const Params& p, LAS char* L, rsrc_t rws, int hw, int lane, int xcd, int j, unsigned tag) {
    const int r = lane & 31, hh = lane >> 5, ct1 = j % 14, ct = j % 7;
    const int b4 = xcd * 4 + j / 7, b2 = xcd * 2 + j / 14;
    const rsrc_t r1 = mkrs(p.w1, D * F * 2), r2 = mkrs(p.w2, F * D * 2);
    const unsigned v1 = slab_voff(lane, 3584), v2 = slab_voff(lane, 896);
    const unsigned short bb = ((const unsigned short*)p.b1)[ct1 * 128 + hw * 32 + r];
    auto w2dma = [&](int c) INL {
#pragma unroll
        for (int i = 0; i < 4; ++i) dma16(r2, v2, (hw & 1) ? w2src(c, hw >> 1, 2 + (i >> 1), i & 1, ct) : w2src(c, hw >> 1, i >> 1, i & 1, ct), w2slot(c) + hw * 4096 + i * 1024);
    };
    u32x4 sg[9][4];
    auto w2ld = [&](int c) INL {
#pragma unroll
        for (int i = 0; i < 4; ++i) sg[c - 5][i] = __builtin_amdgcn_raw_buffer_load_b128(r2, v2, (hw & 1) ? w2src(c, hw >> 1, 2 + (i >> 1), i & 1, ct) : w2src(c, hw >> 1, i >> 1, i & 1, ct), 0);
        SBAR();
    };
    auto w2st = [&](int c) INL {
#pragma unroll
        for (int i = 0; i < 4; ++i) *(LAS u32x4*)(L + w2slot(c) + hw * 4096 + i * 1024 + lane * 16) = sg[c - 5][i];
        SBAR();
    };
    if (hw == 3) wait_flags(rws, WS_F2 + ((b4 >> 1) * 16 + (lane & 15)) * 128 + (b4 & 1) * 16, tag);
    else if (hw < 2) {
#pragma unroll
        for (int u = 16; u < 28; ++u) W1DMA(0, u, hw);
#pragma unroll
        for (int u = 0; u < 12; ++u) W1DMA(1, u, hw);
    }
    bar();
    bar();
    wait_bar<0>();
    if (hw == 3) { const int l = lane < 14 ? lane : 0; __builtin_amdgcn_s_sleep(8); wait_flags(rws, WS_F3 + ((b2 * 2 + (l >= 7)) * 8 + mod7(l)) * 128, tag); }
    else if (hw < 2) {
#pragma unroll
        for (int u = 12; u < 28; ++u) W1DMA(1, u, hw);
    }
    wait_bar<0>();
    w2dma(0); w2dma(1);
    {
        float c2[2] = {};
        LAS char* wb = L + L_W1 + (hw >> 1) * SLAB + slab_rd(lane, hw & 1);
#pragma unroll
        for (int u = 0; u < 28; ++u) {
            const u32x4 f = wfrag<1024>(wb + u * 2048);
#pragma unroll
            for (int k = 0; k < 4; ++k) c2[k & 1] = dot2(f[k], ONES, c2[k & 1]);
        }
        float lo, lo2;
        const unsigned ch = bfsplit(halfsum(c2[0] + c2[1]), lo), cl = bfsplit(lo, lo2), b = bb;
        u32x4 e = {ch | (ch << 16), cl | (b << 16), b | (b << 16), 0u};
        if (hh) e = (u32x4){0u, 0u, 0u, 0u};
        *(LAS u32x4*)(L + L_EXT + hw * 1024 + lane * 16) = e;
    }
    lds_bar();
    w2dma(2); w2dma(3); w2dma(4);
    sfor<9>([&](auto ic) INL { w2ld(5 + ic.value); });
    bar();
    w2st(5); w2st(6); w2st(7); w2st(8);
    const unsigned f4 = WS_F4 + ((b4 >> 1) * 16 + (lane < 14 ? lane : 0)) * 128;
    if (hw == 3) wait_flags(rws, f4, tag);
    wait_bar<0>();
    if (hw == 3) wait_flags(rws, f4 + 16, tag);
    bar();
    w2st(9); w2st(10); w2st(11); w2st(12);
    lds_bar();
    w2st(13);
    lds_bar();
    bar();
}

__device__ __forceinline__ void mlp_compute(const Params& p, LAS char* L, rsrc_t rws, int w, int lane, int xcd, int j, unsigned tag ST_ARG) {
    const int r = lane & 31, hh = lane >> 5;
    const int r4 = j / 7, ct = j % 7, m = w & 1, kh = w >> 1;
    const int rt = xcd * 8 + r4 * 2 + m;
    const int sr[2] = {slab_rd(lane, 0), slab_rd(lane, 1)};
    u32x4 hpk[2];
    u32x4 bf[4][2];
    {
        const rsrc_t rx = mkrs(p.x, S * D * 2);
        u32x2 xr[4];
#pragma unroll
        for (int i = 0; i < 4; ++i) xr[i] = __builtin_amdgcn_raw_buffer_load_b64(rx, r * 896 + hh * 8 + i * 16, rt * 32 * 896 + ct * 128 + kh * 64, 0);
        bar();
        ST(10);
        u32x4 af[14];
#pragma unroll
        for (int u = 0; u < 14; ++u) { int uu = u + 2 * ct; if (uu >= 14) uu -= 14; af[u] = ldh(rws, lane * 16, WS_O + (rt * 28 + kh * 14 + uu) * 1024); }
        auto RD = [&](int u) INL {
#pragma unroll
            for (int t = 0; t < 2; ++t) bf[u & 3][t] = wfrag<1024>(L + L_WO + sr[t] + kh * 14 * 2048 + u * 2048);
            SBAR();
        };
        RD(0); RD(1); RD(2);
        f32x16 acc[2] = {};
        sfor<14>([&](auto ic) INL {
            constexpr int u = ic.value;
            if constexpr (u + 3 < 14) RD(u + 3);
            acc[0] = mfma(bf[u & 3][0], af[u], acc[0]); acc[1] = mfma(bf[u & 3][1], af[u], acc[1]);
            PIN(acc[0]); PIN(acc[1]); SBAR();
        });
        const f32x16 snd = kh ? acc[0] : acc[1];
        f32x16 hv = kh ? acc[1] : acc[0];
#pragma unroll
        for (int i = 0; i < 4; ++i) *(LAS f32x4*)(L + L_RED1 + (w * 4 + i) * 1024 + lane * 16) = (f32x4){snd[4 * i], snd[4 * i + 1], snd[4 * i + 2], snd[4 * i + 3]};
        ST(11);
        lds_bar();
#pragma unroll
        for (int i = 0; i < 4; ++i) {
            const f32x4 o = *(LAS f32x4*)(L + L_RED1 + ((w ^ 2) * 4 + i) * 1024 + lane * 16);
            hv[4 * i] += o[0] + bflo(xr[i][0]); hv[4 * i + 1] += o[1] + bfhi(xr[i][0]);
            hv[4 * i + 2] += o[2] + bflo(xr[i][1]); hv[4 * i + 3] += o[3] + bfhi(xr[i][1]);
        }
#pragma unroll
        for (int s = 0; s < 2; ++s) { hpk[s] = pack8(hv, s); st128<SC1>(hpk[s], rws, lane * 16, WS_H + (rt * 28 + ct * 4 + kh * 2 + s) * 1024); }
        float s1 = 0.f, s2 = 0.f;
#pragma unroll
        for (int k = 0; k < 8; ++k) { const unsigned u = hpk[k >> 2][k & 3]; s1 = dot2(u, ONES, s1); s2 = dot2(u, u, s2); }
        __builtin_amdgcn_raw_buffer_store_b64((u32x2){__float_as_uint(halfsum(s1)), __float_as_uint(halfsum(s2))}, rws,
                                              hh ? 0x7FFFFFF0u : WS_HS + ((rt * 14 + ct * 2 + kh) * 32 + r) * 8, 0, SC1);
        lds_bar();
        vm_wait<0>();
        set_flag(rws, WS_F3 + ((xcd * 4 + r4) * 8 + ct) * 128 + w * 4, tag, lane == 0);
        ST(12);
    }
    {
        const int ct1 = j % 14, rt1 = xcd * 8 + (j / 14) * 4 + w;
        auto RD = [&](int u) INL {
#pragma unroll
            for (int t = 0; t < 2; ++t) bf[u & 3][t] = wfrag<1024>(L + L_W1 + sr[t] + u * 2048);
            SBAR();
        };
        RD(0); RD(1); RD(2);
        bar();
        ST(13);
        u32x4 af[28];
        auto aload = [&](int u) INL { int uu = u + 2 * ct1; if (uu >= 28) uu -= 28; af[u] = ldh(rws, lane * 16, WS_H + (rt1 * 28 + uu) * 1024); };
        constexpr int WIN = 10;
#pragma unroll
        for (int u = 0; u < WIN; ++u) aload(u);
        SBAR();
        f32x16 acc[4] = {};
        u32x2 st[7];
        u32x4 b2f[4];
        auto RD2 = [&](int z) INL { b2f[z & 3] = wfrag<1024>(L + L_W1 + SLAB + sr[z / 28] + (z % 28) * 2048); SBAR(); };
        sfor<28>([&](auto ic) INL {
            constexpr int u = ic.value;
            if constexpr (u + 3 < 28) RD(u + 3); else RD2(u + 3 - 28);
            acc[0] = mfma(bf[u & 3][0], af[u], acc[0]); acc[1] = mfma(bf[u & 3][1], af[u], acc[1]);
            PIN(acc[0]); PIN(acc[1]); SBAR();
            if constexpr (u + WIN < 28) aload(u + WIN);
            else if constexpr (u + WIN < 35) st[u + WIN - 28] = __builtin_amdgcn_raw_buffer_load_b64(rws, (hh * 7 * 32 + r) * 8, WS_HS + (rt1 * 14 + u + WIN - 28) * 256, SC1);
            SBAR();
        });
        ST(14);
        bar();
        u32x4 ext[4];
#pragma unroll
        for (int t = 0; t < 4; ++t) ext[t] = ldsr128(L + L_EXT + t * 1024 + lane * 16);
        float s1 = 0.f, s2 = 0.f;
#pragma unroll
        for (int k = 0; k < 7; ++k) { s1 += __uint_as_float(st[k][0]); s2 += __uint_as_float(st[k][1]); }
        const float mu = halfsum(s1) * (1.0f / D);
        const float var = fmaxf(halfsum(s2) * (1.0f / D) - mu * mu, 0.f);
        const float r1 = __builtin_amdgcn_rsqf(var + EPS);
        const float rs = r1 * __builtin_amdgcn_rsqf(var * r1 * r1 + EPS), irs = __builtin_amdgcn_rcpf(rs);
        float t0, t1, t2;
        const unsigned mh = bfsplit(-mu, t0), ml = bfsplit(t0, t1), ih = bfsplit(irs, t0), il = bfsplit(t0, t1), i3 = bfsplit(t1, t2);
        const u32x4 xe = {mh | (ml << 16), mh | (ih << 16), il | (i3 << 16), 0u};
        acc[0] = mfma(ext[0], xe, acc[0]); acc[1] = mfma(ext[1], xe, acc[1]);
        acc[2] = mfma(ext[2], xe, acc[2]); acc[3] = mfma(ext[3], xe, acc[3]);
        PIN(acc[0]); PIN(acc[1]); PIN(acc[2]); PIN(acc[3]); SBAR();
        constexpr float C0 = -2.0f * 0.7978845608028654f * 1.4426950408889634f, C1 = C0 * 0.044715f;
        const float k0 = C0 * rs, k1 = C1 * rs * rs * rs;
        auto gelu = [&](f32x16& v, int k) INL {
            const float a = v[k];
            const float e = __builtin_amdgcn_exp2f(a * fmaf(a * a, k1, k0));
            v[k] = a * __builtin_amdgcn_rcpf(fmaf(e, irs, irs));
        };
        auto put = [&](int t) INL {
#pragma unroll
            for (int s = 0; s < 2; ++s) st128<SC1>(pack8(acc[t], s), rws, lane * 16, WS_G + (rt1 * 112 + ct1 * 8 + t * 2 + s) * 1024);
        };
        auto flag = [&](int stage) INL { set_flag(rws, WS_F4 + ((xcd * 2 + j / 14) * 16 + ct1) * 128 + stage * 16 + w * 4, tag, lane == 0); };
        sfor<28>([&](auto ic) INL {
            constexpr int g = ic.value, t = 2 + g / 14;
#pragma unroll
            for (int k = 0; k < 2; ++k) {
                const int z = 2 * g + k;
                if (z + 3 < 56) RD2(z + 3);
                acc[t] = mfma(b2f[z & 3], af[z % 28], acc[t]); PIN(acc[t]); SBAR();
            }
            constexpr int e = g < 2 ? -1 : g < 18 ? g - 2 : g < 19 ? -1 : g < 27 ? g - 3 : -1;
            if constexpr (e >= 0) { gelu(acc[e >> 3], 2 * (e & 7)); gelu(acc[e >> 3], 2 * (e & 7) + 1); PIN(acc[e >> 3]); if constexpr ((e & 7) == 7) put(e >> 3); SBAR(); }
            if constexpr (g == 25) { vm_wait<0>(); flag(0); SBAR(); }
        });
        ST(15);
        bar();
#pragma unroll
        for (int k = 0; k < 16; ++k) gelu(acc[3], k);
        put(3);
        ST(16);
        bar();
        ST(17);
        constexpr int WN2 = 12;
        u32x4 ag[WN2];
        const unsigned abase = WS_G + (rt * 112 + kh * 56) * 1024;
#pragma unroll
        for (int u = 0; u < WN2; ++u) ag[u] = ldh(rws, lane * 16, abase + fc2_kstep(u, ct) * 1024);
        SBAR();
        vm_wait<WN2>(); flag(1);
        auto RD3 = [&](int u) INL {
#pragma unroll
            for (int t = 0; t < 2; ++t) bf[u & 3][t] = wfrag<1024>(L + sr[t] + w2slot(u >> 2) + kh * 8192 + (u & 3) * 2048);
            SBAR();
        };
        RD3(0); RD3(1); RD3(2);
        ST(18);
        f32x16 a2[2] = {};
        u32x2 bb[4];
        sfor<56>([&](auto ic) INL {
            constexpr int u = ic.value;
            if constexpr (u == 16 || u == 32 || u == 44) bar();
            if constexpr (u == 16) ST(19);
            if constexpr (u + 3 < 56) RD3(u + 3);
            a2[0] = mfma(bf[u & 3][0], ag[u % WN2], a2[0]); a2[1] = mfma(bf[u & 3][1], ag[u % WN2], a2[1]);
            PIN(a2[0]); PIN(a2[1]); SBAR();
            if constexpr (u + WN2 < 56) ag[u % WN2] = ldh(rws, lane * 16, abase + fc2_kstep(u + WN2, ct) * 1024);
            else if constexpr (u + WN2 < 60) bb[u + WN2 - 56] = __builtin_amdgcn_raw_buffer_load_b64(mkrs(p.b2, D * 2), hh * 8 + (u + WN2 - 56) * 16, ct * 128 + kh * 64, 0);
            SBAR();
        });
        ST(20);
        const f32x16 snd = kh ? a2[0] : a2[1];
        f32x16 v = kh ? a2[1] : a2[0];
#pragma unroll
        for (int i = 0; i < 4; ++i) *(LAS f32x4*)(L + L_RED2 + (w * 4 + i) * 1024 + lane * 16) = (f32x4){snd[4 * i], snd[4 * i + 1], snd[4 * i + 2], snd[4 * i + 3]};
        lds_bar();
        unsigned o[8];
#pragma unroll
        for (int i = 0; i < 4; ++i) {
            const f32x4 t = *(LAS f32x4*)(L + L_RED2 + ((w ^ 2) * 4 + i) * 1024 + lane * 16);
            const unsigned h0 = hpk[i >> 1][2 * (i & 1)], h1 = hpk[i >> 1][2 * (i & 1) + 1];
            o[2 * i] = cvtpk(v[4 * i] + t[0] + bflo(bb[i][0]) + bflo(h0), v[4 * i + 1] + t[1] + bfhi(bb[i][0]) + bfhi(h0));
            o[2 * i + 1] = cvtpk(v[4 * i + 2] + t[2] + bflo(bb[i][1]) + bflo(h1), v[4 * i + 3] + t[3] + bfhi(bb[i][1]) + bfhi(h1));
        }
        const rsrc_t ro = mkrs(p.out, S * D * 2);
#pragma unroll
        for (int k = 0; k < 2; ++k) {
            auto s0 = __builtin_amdgcn_permlane32_swap(o[4 * k], o[4 * k + 2], false, false);
            auto s1_ = __builtin_amdgcn_permlane32_swap(o[4 * k + 1], o[4 * k + 3], false, false);
            st128<0>((u32x4){s0[0], s1_[0], s0[1], s1_[1]}, ro, r * 896 + hh * 16 + k * 32, rt * 32 * 896 + ct * 128 + kh * 64);
        }
        ST(21);
    }
}

__global__ void __launch_bounds__(NT, 2) k_block(Params p) {
    extern __shared__ __attribute__((aligned(16))) char smem[];
    LAS char* L = (LAS char*)smem;
    ST_DECL
    ST(0);
    const int lane = threadIdx.x & 63, w = __builtin_amdgcn_readfirstlane(threadIdx.x >> 6);
    const int b = blockIdx.x, xcd = b & 7, j = b >> 3;
    const int rb = (xcd & 1) * 8 + (j & 7), sub = j >> 3, head = 4 * (xcd >> 1) + sub, pair = 2 * (xcd >> 1) + (sub >> 1), kind = sub & 1;
    const rsrc_t rws = mkrs(p.ws, WS_END);
    const int rot = ((j & 7) + 2 * sub) % 7;
    unsigned tag;
    if (w < 4) {
        tag = load_tag(p);
        if (kind) p1_compute<true>(L, rws, w, lane, rb, pair, head, tag ST_PASS);
        else p1_compute<false>(L, rws, w, lane, rb, pair, head, tag ST_PASS);
    } else if (w < 6) p1_loader(p, w - 4, lane, rb, pair, kind, head, rot, tag);
    else { tag = load_tag(p); p1_colsum(L, rws, w - 6, lane, head, tag); }
    ST(4);
    attention(p, L, rws, w, lane, rb, head, j, tag ST_PASS);
    if (j < 28) {
        if (w < 4) mlp_compute(p, L, rws, w, lane, xcd, j, tag ST_PASS);
        else mlp_helper(p, L, rws, w - 4, lane, xcd, j, tag);
    }
    if (b == 0 && w == 0) set_flag(rws, WS_GEN, tag, lane == 0);
    ST_FLUSH();
}

extern "C" void kernel_launch(void* const* d_in, const int* in_sizes, int n_in, void* d_out, int out_size, void* d_ws, size_t ws_size, hipStream_t stream) {
    Params p;
    p.x = d_in[0]; p.wqkv = d_in[1]; p.wo = d_in[2]; p.w1 = d_in[4]; p.b1 = d_in[5]; p.w2 = d_in[6]; p.b2 = d_in[7];
    p.out = d_out; p.ws = d_ws;
    (void)hipFuncSetAttribute((const void*)k_block, hipFuncAttributeMaxDynamicSharedMemorySize, LDS_BYTES);
    k_block<<<NWG, NT, LDS_BYTES, stream>>>(p);
}
```

```cpp
#include <hip/hip_runtime.h>
#include <stdint.h>
#include <utility>

#define LAS __attribute__((address_space(3)))
#define GAS __attribute__((address_space(1)))
typedef __bf16 bf2 __attribute__((ext_vector_type(2)));
typedef __bf16 bf8 __attribute__((ext_vector_type(8)));
typedef __bf16 bf16v __attribute__((ext_vector_type(16)));
typedef float f32x4 __attribute__((ext_vector_type(4)));
typedef float f32x8 __attribute__((ext_vector_type(8)));
typedef float f32x16 __attribute__((ext_vector_type(16)));
typedef unsigned u32x2 __attribute__((ext_vector_type(2)));
typedef unsigned u32x4 __attribute__((ext_vector_type(4)));
typedef short s16x4 __attribute__((ext_vector_type(4)));
typedef __amdgpu_buffer_rsrc_t rsrc_t;

constexpr int S = 2048, D = 448, H = 16, F = 1792;
constexpr int NT = 512, NWG = 256;
constexpr float EPS = 1e-5f;
constexpr int LDS_BYTES = 160 * 1024;
constexpr int SC1 = 16;
constexpr unsigned CODE_LEN = 52 * 1024;
constexpr unsigned WS_GEN = 0;
constexpr unsigned WS_F1 = 4096;
constexpr unsigned WS_F2 = WS_F1 + 256 * 128;
constexpr unsigned WS_F3 = WS_F2 + 256 * 128;
constexpr unsigned WS_F4 = WS_F3 + 256 * 128;
constexpr unsigned WS_HS = WS_F4 + 768 * 128;
constexpr unsigned WS_KV = 512 * 1024;
constexpr unsigned WS_O = WS_KV + 16 * 64 * 4096;
constexpr unsigned WS_H = WS_O + 64 * 32 * 1024;
constexpr unsigned WS_G = WS_H + 64 * 28 * 1024;
constexpr unsigned WS_END = WS_G + 64 * 112 * 1024;

struct Params {
    const void *x, *wqkv, *wo, *w1, *b1, *w2, *b2;
    void *out, *ws;
};

template <class Fn, int... I> __device__ __forceinline__ void sfor_(Fn&& f, std::integer_sequence<int, I...>) { (f(std::integral_constant<int, I>{}), ...); }
template <int N, class Fn> __device__ __forceinline__ void sfor(Fn&& f) { sfor_(f, std::make_integer_sequence<int, N>{}); }
#define INL __attribute__((always_inline))
__device__ __forceinline__ rsrc_t mkrs(const void* p, unsigned bytes) { return __builtin_amdgcn_make_buffer_rsrc((void*)p, 0, (int)bytes, 0x00020000); }
__device__ __forceinline__ void dma16(rsrc_t rs, unsigned voff, unsigned soff, unsigned lds) {
    asm volatile("s_mov_b32 m0, %0\n\ts_nop 0\n\tbuffer_load_dwordx4 %1, %2, %3 offen lds" ::"s"(lds), "v"(voff), "s"(rs), "s"(soff) : "memory");
}
__device__ __forceinline__ void dma4(rsrc_t rs, unsigned voff, unsigned soff, unsigned lds) {
    asm volatile("s_mov_b32 m0, %0\n\ts_nop 0\n\tbuffer_load_dword %1, %2, %3 offen lds" ::"s"(lds), "v"(voff), "s"(rs), "s"(soff) : "memory");
}
__device__ __forceinline__ void dma4_if0(int skip, rsrc_t rs, unsigned voff, unsigned soff, unsigned lds) {
    asm volatile("s_cmp_lg_u32 %4, 0\n\ts_cbranch_scc1 1f\n\ts_mov_b32 m0, %0\n\ts_nop 0\n\tbuffer_load_dword %1, %2, %3 offen lds\n1:" ::"s"(lds), "v"(voff), "s"(rs), "s"(soff), "s"(skip) : "memory", "scc");
}
template <int N> __device__ __forceinline__ void wait_bar() { asm volatile("s_waitcnt vmcnt(%0) lgkmcnt(0)\n\ts_barrier" ::"i"(N) : "memory"); }
template <int N> __device__ __forceinline__ void vm_wait() { asm volatile("s_waitcnt vmcnt(%0)" ::"i"(N) : "memory"); }
__device__ __forceinline__ void lds_bar() { asm volatile("s_waitcnt lgkmcnt(0)\n\ts_barrier" ::: "memory"); }
__device__ __forceinline__ void bar() { asm volatile("s_barrier" ::: "memory"); }
__device__ __forceinline__ void lds_wait() { asm volatile("s_waitcnt lgkmcnt(0)" ::: "memory"); }
__device__ __forceinline__ f32x16 mfma(u32x4 a, u32x4 b, f32x16 c) {
    return __builtin_amdgcn_mfma_f32_32x32x16_bf16(__builtin_bit_cast(bf8, a), __builtin_bit_cast(bf8, b), c, 0, 0, 0);
}
__device__ __forceinline__ float dot2(unsigned a, unsigned b, float c) {
    return __builtin_amdgcn_fdot2_f32_bf16(__builtin_bit_cast(bf2, a), __builtin_bit_cast(bf2, b), c, false);
}
__device__ __forceinline__ unsigned cvtpk(float lo, float hi) {
    typedef float f2 __attribute__((ext_vector_type(2)));
    return __builtin_bit_cast(unsigned, __builtin_convertvector((f2){lo, hi}, bf2));
}
__device__ __forceinline__ float halfsum(float v) {
    auto r = __builtin_amdgcn_permlane32_swap(__float_as_uint(v), __float_as_uint(v), false, false);
    return __uint_as_float(r[0]) + __uint_as_float(r[1]);
}
template <int AUX> __device__ __forceinline__ void st128(u32x4 v, rsrc_t rs, unsigned voff, unsigned soff) { __builtin_amdgcn_raw_buffer_store_b128(v, rs, voff + soff, 0, AUX); }
__device__ __forceinline__ u32x4 ldh(rsrc_t rs, unsigned voff, unsigned soff) { return __builtin_amdgcn_raw_buffer_load_b128(rs, voff, soff, SC1); }
__device__ __forceinline__ u32x4 ldsr128(LAS char* p) { return *(LAS u32x4*)p; }
__device__ __forceinline__ u32x2 trread(LAS char* p) {
    return __builtin_bit_cast(u32x2, __builtin_amdgcn_ds_read_tr16_b64_v4i16((LAS s16x4*)p));
}
template <int ESTEP> __device__ __forceinline__ u32x4 wfrag(LAS char* p) {
    const u32x2 a = trread(p), b = trread(p + ESTEP);
    return (u32x4){a[0], a[1], b[0], b[1]};
}
__device__ __forceinline__ u32x4 pack8(const f32x16& v, int s) {
    return (u32x4){cvtpk(v[8 * s], v[8 * s + 1]), cvtpk(v[8 * s + 2], v[8 * s + 3]), cvtpk(v[8 * s + 4], v[8 * s + 5]), cvtpk(v[8 * s + 6], v[8 * s + 7])};
}
__device__ __forceinline__ float smul(float a, float b) { float r; asm("v_mul_f32 %0, %1, %2" : "=v"(r) : "v"(a), "v"(b)); return r; }
__device__ __forceinline__ float sadd(float a, float b) { float r; asm("v_add_f32 %0, %1, %2" : "=v"(r) : "v"(a), "v"(b)); return r; }
__device__ __forceinline__ float sfma(float a, float b, float c) { float r; asm("v_fma_f32 %0, %1, %2, %3" : "=v"(r) : "v"(a), "v"(b), "v"(c)); return r; }
__device__ __forceinline__ float bflo(unsigned u) { return __uint_as_float(u << 16); }
__device__ __forceinline__ float bfhi(unsigned u) { return __uint_as_float(u & 0xFFFF0000u); }
constexpr unsigned ONES = 0x3F803F80u;
#define PIN(x) asm volatile("" : "+v"(x))
#define SBAR() __builtin_amdgcn_sched_barrier(0)
#define ST(id)
#define ST_DECL
#define ST_ARG
#define ST_PASS
#define ST_FLUSH()

constexpr int K1 = 1024;
constexpr int P1_XS = 0, P1_WS = 64 * K1, P1_WQ = 120 * K1, P1_EX = 148 * K1;
constexpr int L_SYNC1 = 150 * K1, L_SYNC4 = 144 * K1;
constexpr int L_P = 32 * K1, L_WO = 96 * K1, L_QF = 152 * K1;
constexpr int L_W1 = 0, SLAB = 56 * K1, L_RED1 = 80 * K1, L_EXT = 156 * K1, L_RED2 = 144 * K1;
__host__ __device__ constexpr int w2slot(int c) { return ((c % 9) < 2 ? 112 + 16 * (c % 9) : 16 * ((c % 9) - 2)) * K1; }

__device__ __forceinline__ void set_flag(rsrc_t rws, unsigned off, unsigned tag, bool me) {
    __builtin_amdgcn_raw_buffer_store_b32(tag, rws, me ? off : 0x7FFFFFF0u, 0, SC1);
}
__device__ __forceinline__ void publish(LAS char* cnt, rsrc_t rws, unsigned off, unsigned tag, int lane) {
    unsigned old = 0;
    if (lane == 0) old = __hip_atomic_fetch_add((LAS unsigned*)cnt, 1u, __ATOMIC_RELAXED, __HIP_MEMORY_SCOPE_WORKGROUP);
    set_flag(rws, off, tag, lane == 0 && old == 3u);
}
__device__ __forceinline__ void wait_word(rsrc_t rws, unsigned off, unsigned tag) {
#pragma unroll 1
    for (unsigned spins = 0; spins < (1u << 20); ++spins) {
        asm volatile("" ::: "memory");
        if (__all(__builtin_amdgcn_raw_buffer_load_b32(rws, off, 0, SC1) == tag)) break;
        __builtin_amdgcn_s_sleep(2);
    }
    asm volatile("" ::: "memory");
}
__device__ __forceinline__ unsigned load_tag(const Params& p) {
    unsigned g;
    asm volatile("s_load_dword %0, %1, 0x0\n\ts_waitcnt lgkmcnt(0)" : "=s"(g) : "s"(p.ws) : "memory");
    return g + 1;
}
__device__ __forceinline__ int mod7(int v) { return v >= 7 ? v - 7 : v; }

__device__ __forceinline__ int p1_wrd(int lane, int t) {
    const int q = (lane & 15) >> 2, pp = lane & 3, g1 = (lane >> 4) & 1, hh = lane >> 5;
    return P1_WS + (8 * hh + q) * 128 + (((7 * t + 4 * g1 + pp) ^ ((q >> 1) << 3)) << 3);
}
template <bool VT>
__device__ __forceinline__ void p1_compute(LAS char* L, rsrc_t rws, int w, int lane, int rb, int pair, int head, unsigned tag ST_ARG) {
    const int r = lane & 31, hh = lane >> 5;
    LAS char* xa[4];
#pragma unroll
    for (int uu = 0; uu < 4; ++uu) xa[uu] = L + P1_XS + w * 4096 + r * 128 + (((2 * uu + hh) ^ ((r >> 1) & 7)) << 4);
    LAS char* wb[2] = {L + p1_wrd(lane, 0), L + p1_wrd(lane, 1)};
    PIN(wb[0]); PIN(wb[1]);
    u32x4 ones = {ONES, ONES, ONES, ONES};
    PIN(ones);
    u32x4 xf[28];
    f32x16 acc[2] = {};
    float sx[2] = {}, sxx[2] = {};
    u32x4 bf[8][2];
    auto RD = [&](int u) INL {
        xf[u] = ldsr128(xa[u & 3] + ((u >> 2) & 3) * 16384);
        bf[u & 7][0] = wfrag<512>(wb[0] + u * 2048);
        bf[u & 7][1] = wfrag<512>(wb[1] + u * 2048);
        SBAR();
    };
    auto MM = [&](int u) INL {
        acc[0] = VT ? mfma(xf[u], bf[u & 7][0], acc[0]) : mfma(bf[u & 7][0], xf[u], acc[0]);
        acc[1] = VT ? mfma(xf[u], bf[u & 7][1], acc[1]) : mfma(bf[u & 7][1], xf[u], acc[1]);
        PIN(acc[0]); PIN(acc[1]); SBAR();
    };
    auto SQ = [&](int c) INL {
#pragma unroll
        for (int k = 0; k < 16; ++k) { const unsigned a = xf[4 * c + (k >> 2)][k & 3]; sx[k & 1] = dot2(a, ONES, sx[k & 1]); sxx[k & 1] = dot2(a, a, sxx[k & 1]); }
        PIN(sx[0]); PIN(sx[1]); PIN(sxx[0]); PIN(sxx[1]); SBAR();
    };
    sfor<7>([&](auto ic) INL {
        constexpr int c = ic.value;
        lds_bar();
        if (c == 0) ST(1);
#pragma unroll
        for (int i = 0; i < 4; ++i) { if (c > 0 && c < 6) MM(4 * c - 4 + i); RD(4 * c + i); }
        SQ(c);
        if constexpr (c >= 5) { MM(4 * c); MM(4 * c + 1); MM(4 * c + 2); MM(4 * c + 3); }
    });
    ST(2);
    const float mu = halfsum(sx[0] + sx[1]) * (1.0f / D);
    const float var = fmaxf(halfsum(sxx[0] + sxx[1]) * (1.0f / D) - mu * mu, 0.f);
    const float rstd = __builtin_amdgcn_rsqf(var + EPS);
    LAS float* ex = (LAS float*)(L + P1_EX + w * 256);
    LAS float* cs = (LAS float*)(L + P1_EX + 1024);
    const int tile = rb * 4 + w;
    if (VT) ex[lane] = hh ? rstd : -mu * rstd;
    lds_bar();
    if (VT) {
        f32x4 a4[4], b4[4];
#pragma unroll
        for (int i = 0; i < 4; ++i) { a4[i] = *(LAS f32x4*)(ex + 8 * i + 4 * hh); b4[i] = *(LAS f32x4*)(ex + 32 + 8 * i + 4 * hh); }
#pragma unroll
        for (int t = 0; t < 2; ++t) {
            const float c = cs[32 * t + r];
            f32x16 v;
#pragma unroll
            for (int k = 0; k < 16; ++k) v[k] = fmaf(acc[t][k], b4[k >> 2][k & 3], a4[k >> 2][k & 3] * c);
#pragma unroll
            for (int s = 0; s < 2; ++s) {
                u32x4 o = pack8(v, s);
                if (r >= 28) { const unsigned z = r == 28 ? ONES : 0u; o = (u32x4){z, z, z, z}; }
                st128<SC1>(o, rws, lane * 16, WS_KV + ((2 * pair + t) * 64 + tile) * 4096 + (2 + s) * 1024);
            }
        }
    } else {
        const int rp = (r & 19) | ((r & 4) << 1) | ((r & 8) >> 1);
        const float nm = -mu * rstd;
#pragma unroll
        for (int t = 0; t < 2; ++t) {
            f32x16 v;
#pragma unroll
            for (int i = 0; i < 4; ++i) {
                const f32x4 c4 = *(LAS f32x4*)(cs + 32 * t + 8 * i + 4 * hh);
#pragma unroll
                for (int k = 0; k < 4; ++k) v[4 * i + k] = fmaf(acc[t][4 * i + k], rstd, nm * c4[k]);
            }
#pragma unroll
            for (int s = 0; s < 2; ++s) {
                u32x4 o = pack8(v, s);
                if (s == 1 && hh) { o[2] = 0u; o[3] = 0u; }
                st128<SC1>(o, rws, (rp + 32 * hh) * 16, WS_KV + ((2 * pair + t) * 64 + tile) * 4096 + s * 1024);
            }
        }
    }
    ST(3);
    lds_bar();
    LAS char* wq = L + P1_WQ + (8 * hh + ((lane & 15) >> 2)) * 64 + ((head & 1) + 4 * ((lane >> 4) & 1) + (lane & 3)) * 8;
    PIN(wq);
    f32x16 aq = {}, cq = {};
    u32x4 bq[4];
    bq[0] = wfrag<256>(wq); bq[1] = wfrag<256>(wq + 1024); bq[2] = wfrag<256>(wq + 2048);
    sfor<28>([&](auto ic) INL {
        constexpr int u = ic.value;
        if constexpr (u + 3 < 28) bq[(u + 3) & 3] = wfrag<256>(wq + (u + 3) * 1024);
        SBAR();
        aq = mfma(bq[u & 3], xf[u], aq);
        cq = mfma(bq[u & 3], ones, cq);
        PIN(aq); PIN(cq); SBAR();
        if constexpr (u == 11) { vm_wait<0>(); publish(L + L_SYNC1, rws, WS_F1 + ((pair * 16 + rb) * 2 + (VT ? 1 : 0)) * 128, tag, lane); SBAR(); }
    });
    const float qs = rstd * (0.18898223650461362f * 1.4426950408889634f), nmq = -mu * qs;
    f32x16 v;
#pragma unroll
    for (int k = 0; k < 16; ++k) v[k] = fmaf(aq[k], qs, nmq * cq[k]);
#pragma unroll
    for (int s = 0; s < 2; ++s) {
        u32x4 o = pack8(v, s);
        if (s == 1 && hh) { o[2] = 0u; o[3] = 0u; }
        *(LAS u32x4*)(L + L_QF + (w * 2 + s) * 1024 + lane * 16) = o;
    }
}

__device__ __forceinline__ void p1_loader(const Params& p, int lw, int lane, int rb, int pair, int kind, int head, int rot, unsigned& tag) {
    const rsrc_t rx = mkrs(p.x, S * D * 2), rw = mkrs(p.wqkv, D * 3 * D * 2);
    unsigned vx[2];
#pragma unroll
    for (int i = 0; i < 2; ++i) vx[i] = (lane >> 3) * 896 + (((lane & 7) ^ ((4 * i + (lane >> 4)) & 7)) << 4);
    int piece = (lane & 7) ^ (((lane >> 4) & 1) << 2);
    if (piece == 7) piece = 6;
    const unsigned vw = (lane >> 3) * 2688 + piece * 16;
    const unsigned xb = (rb * 128 + lw * 64) * 896, wb = lw * 32 * 2688 + (448 * (1 + kind) + 56 * pair) * 2;
    auto chunk = [&](int c) INL {
        const int cc = mod7(c + rot);
#pragma unroll
        for (int i = 0; i < 4; ++i) dma16(rw, vw, wb + (cc * 64 + i * 8) * 2688, P1_WS + (c * 64 + lw * 32 + i * 8) * 128);
#pragma unroll
        for (int i = 0; i < 8; ++i) dma16(rx, vx[i & 1], xb + i * 8 * 896 + cc * 128, P1_XS + (c & 3) * 16384 + lw * 8192 + i * 1024);
    };
    const unsigned vq = (lane >> 2) * 2688 + (lane & 3) * 16;
    const unsigned qb = 56 * head - 8 * (head & 1) + lw * 32 * 2688;
    auto wq = [&](int c) INL {
#pragma unroll
        for (int i = 0; i < 2; ++i) dma16(rw, vq, (mod7(c + rot) * 64 + i * 16) * 2688 + qb, P1_WQ + (c * 64 + lw * 32 + i * 16) * 64);
    };
    chunk(0); chunk(1);
#pragma unroll
    for (int c = 0; c < 5; ++c) { chunk(c + 2); wait_bar<24>(); }
    wq(0); wq(1); wq(2);
    wait_bar<18>();
    wq(3); wq(4);
    wait_bar<10>();
    wq(5); wq(6);
    tag = load_tag(p);
    bar();
    wait_bar<0>();
}

__device__ __forceinline__ void p1_colsum(LAS char* L, int t, int lane) {
    LAS char* wb = L + p1_wrd(lane, t);
    PIN(wb);
    ((LAS unsigned*)(L + L_SYNC1))[t] = 0u;
    float c2[4] = {};
#pragma unroll
    for (int c = 0; c < 7; ++c) {
        lds_bar();
#pragma unroll
        for (int uu = 0; uu < 4; ++uu) {
            const u32x4 b = wfrag<512>(wb + (4 * c + uu) * 2048);
#pragma unroll
            for (int j = 0; j < 4; ++j) { c2[2 * (j & 1)] = sadd(c2[2 * (j & 1)], bflo(b[j])); c2[2 * (j & 1) + 1] = sadd(c2[2 * (j & 1) + 1], bfhi(b[j])); }
        }
        PIN(c2[0]); PIN(c2[1]); PIN(c2[2]); PIN(c2[3]);
    }
    ((LAS float*)(L + P1_EX + 1024))[32 * t + (lane & 31)] = halfsum((c2[0] + c2[1]) + (c2[2] + c2[3]));
    lds_bar();
    lds_bar();
}

__device__ __forceinline__ f32x16 pv(u32x4 v, f32x8 pr, f32x16 o) {
    return __builtin_amdgcn_smfmac_f32_32x32x32_bf16(__builtin_bit_cast(bf8, v), __builtin_bit_cast(bf16v, pr), o, 0xDDDD, 0, 0);
}
__device__ __forceinline__ void mfma0(f32x16& s, u32x4 a, u32x4 b) { asm volatile("v_mfma_f32_32x32x16_bf16 %0, %1, %2, 0" : "+v"(s) : "v"(a), "v"(b)); }
__device__ __forceinline__ f32x8 lo8(const f32x16& s) { return __builtin_shufflevector(s, s, 0, 1, 2, 3, 4, 5, 6, 7); }
__device__ __forceinline__ f32x8 hi8(const f32x16& s) { return __builtin_shufflevector(s, s, 8, 9, 10, 11, 12, 13, 14, 15); }
__device__ __forceinline__ unsigned slab_voff(int lane, int pitch) { return (lane >> 3) * pitch + (((lane & 7) ^ (((lane >> 4) & 1) << 2)) << 4); }
__device__ __forceinline__ int slab_rd(int lane, int t) {
    const int q = (lane & 15) >> 2, pp = lane & 3, g1 = (lane >> 4) & 1, hh = lane >> 5;
    return (4 * hh + q) * 128 + (((8 * t + 4 * g1 + pp) ^ ((q >> 1) << 3)) << 3);
}
__device__ __forceinline__ f32x16 redo_o(rsrc_t rws, int lane, int head, const u32x4 (&qf)[2]) {
    float m = -INFINITY;
#pragma unroll 1
    for (int t = 0; t < 64; ++t) {
        const unsigned b = WS_KV + (head * 64 + t) * 4096;
        f32x16 s = {};
        s = mfma(ldh(rws, lane * 16, b), qf[0], s);
        s = mfma(ldh(rws, lane * 16 + 1024, b), qf[1], s);
#pragma unroll
        for (int k = 0; k < 16; ++k) m = fmaxf(m, s[k]);
    }
    auto mm = __builtin_amdgcn_permlane32_swap(__float_as_uint(m), __float_as_uint(m), false, false);
    m = fmaxf(__uint_as_float(mm[0]), __uint_as_float(mm[1]));
    f32x16 o = {};
#pragma unroll 1
    for (int t = 0; t < 64; ++t) {
        const unsigned b = WS_KV + (head * 64 + t) * 4096;
        f32x16 s = {};
        s = mfma(ldh(rws, lane * 16, b), qf[0], s);
        s = mfma(ldh(rws, lane * 16 + 1024, b), qf[1], s);
#pragma unroll
        for (int k = 0; k < 16; ++k) s[k] = __builtin_amdgcn_exp2f(s[k] - m);
        o = pv(ldh(rws, lane * 16 + 2048, b), lo8(s), o);
        o = pv(ldh(rws, lane * 16 + 3072, b), hi8(s), o);
    }
    return o;
}

#define W1DMA(hf, u, e) do { int uu_ = (u) + 2 * ct1; if (uu_ >= 28) uu_ -= 28; dma16(r1, v1, (uu_ * 16 + (e) * 8) * 3584 + ct1 * 256 + (hf) * 128, L_W1 + (hf) * SLAB + (u) * 2048 + (e) * 1024); } while (0)

__host__ __device__ constexpr int step_q(int n) { return n < 52 ? (n & 3) : (n - 52) / 3; }
__host__ __device__ constexpr int step_k(int n) { return n < 52 ? (n >> 2) : 13 + (n - 52) % 3; }

__device__ __forceinline__ void attn_compute(LAS char* L, rsrc_t rws, int w, int lane, int head ST_ARG) {
    u32x4 kf[5][2], vf[5][2];
    const unsigned kvb = WS_KV + (head * 64 + w * 16) * 4096;
    auto ld = [&](int t, int s) INL { (s < 2 ? kf : vf)[t % 5][s & 1] = ldh(rws, lane * 16 + s * 1024, kvb + t * 4096); SBAR(); };
    f32x16 o[4], sc[3];
    { float z = 0.f; PIN(z);
#pragma unroll
      for (int k = 0; k < 64; ++k) o[k >> 4][k & 15] = z;
      sc[0] = o[0]; sc[1] = o[0]; sc[2] = o[0];
      PIN(o[0]); PIN(o[1]); PIN(o[2]); PIN(o[3]); PIN(sc[0]); PIN(sc[1]); PIN(sc[2]); }
    __builtin_amdgcn_s_setprio(1);
    lds_bar();
    ST(5);
#pragma unroll
    for (int k = 0; k < 10; ++k) ld(k >> 2, k & 3);
    u32x4 qf[4][2];
#pragma unroll
    for (int k = 0; k < 8; ++k) qf[k >> 1][k & 1] = ldsr128(L + L_QF + k * 1024 + lane * 16);
    mfma0(sc[0], kf[0][0], qf[0][0]);
    sc[0] = mfma(kf[0][1], qf[0][1], sc[0]);
    LAS char* pw = L + L_P + w * 4096 + lane * 16;
    ST(6);
#define EXP4(X, i) do { X[i] = __builtin_amdgcn_exp2f(X[i]); X[i + 1] = __builtin_amdgcn_exp2f(X[i + 1]); X[i + 2] = __builtin_amdgcn_exp2f(X[i + 2]); X[i + 3] = __builtin_amdgcn_exp2f(X[i + 3]); PIN(X); SBAR(); } while (0)
    auto step = [&](auto ic) INL {
        constexpr int n = ic.value, qn = step_q(n + 1), kn = step_k(n + 1) % 5, qp = step_q(n > 0 ? n - 1 : 0), kp = step_k(n > 0 ? n - 1 : 0) % 5;
        f32x16 &sn = sc[(n + 1) % 3], &s = sc[n % 3], &sp = sc[(n + 2) % 3];
        if constexpr (n + 1 < 64) { mfma0(sn, kf[kn][0], qf[qn][0]); SBAR(); }
        if constexpr (n < 64) EXP4(s, 0);
        if constexpr (n + 1 < 64) { sn = mfma(kf[kn][1], qf[qn][1], sn); PIN(sn); SBAR(); }
        if constexpr (n < 64) EXP4(s, 4);
        if constexpr (n > 0) { o[qp] = pv(vf[kp][0], lo8(sp), o[qp]); PIN(o[qp]); SBAR(); }
        if constexpr (n < 64) EXP4(s, 8);
        if constexpr (n > 0) { o[qp] = pv(vf[kp][1], hi8(sp), o[qp]); PIN(o[qp]); SBAR(); }
        constexpr int issued = 10 + n + (n < 8 ? n : 8) < 64 ? 10 + n + (n < 8 ? n : 8) : 64;
        constexpr int nk = 4 * step_k(n + 2 < 64 ? n + 2 : 63) + 1, nv = 4 * step_k(n < 64 ? n : 63) + 3, need = nk > nv ? nk : nv, left = issued - 1 - need;
        if constexpr (n < 55) { __builtin_amdgcn_s_waitcnt(0x0F70 | (left & 15) | ((left >> 4) << 14)); SBAR(); }
        if constexpr (n < 64) { s[12] = __builtin_amdgcn_exp2f(s[12]); PIN(s); SBAR(); }
        if constexpr (issued < 64) ld(issued >> 2, issued & 3);
        if constexpr (n < 8) ld((issued + 1) >> 2, (issued + 1) & 3);
        if constexpr (n < 64) { s[13] = __builtin_amdgcn_exp2f(s[13]); s[14] = __builtin_amdgcn_exp2f(s[14]); s[15] = __builtin_amdgcn_exp2f(s[15]); PIN(s); SBAR(); }
        if constexpr (n == 56 || n == 59 || n == 62 || n == 64) {
            constexpr int qt = n == 64 ? 3 : (n - 56) / 3;
#pragma unroll
            for (int i = 0; i < 4; ++i) *(LAS f32x4*)(pw + qt * 16384 + i * 1024) = (f32x4){o[qt][4 * i], o[qt][4 * i + 1], o[qt][4 * i + 2], o[qt][4 * i + 3]};
            SBAR();
        }
    };
    sfor<65>(step);
    __builtin_amdgcn_s_setprio(0);
}

__device__ __forceinline__ int fc2_kstep(int p, int rot) { return mod7((p % 14) / 2 + rot) * 8 + (p / 14) * 2 + (p & 1); }
__device__ __forceinline__ int fc2_rot(int j) { return (j % 7 + 2 * (j / 7)) % 7; }
__device__ __forceinline__ unsigned w2src(int c, int hw, int i, int j) {
    const int ks = (hw & 1) ? fc2_kstep(4 * c + 2 + (i >> 1), fc2_rot(j)) : fc2_kstep(4 * c + (i >> 1), fc2_rot(j));
    return (((hw >> 1) * 56 + ks) * 16 + (i & 1) * 8) * 896 + (j % 7) * 128;
}
__device__ __forceinline__ void attn_helper(const Params& p, rsrc_t rws, int hw, int lane, int head, int j, unsigned tag, u32x4 (&sg)[9][4]) {
    const rsrc_t rw = mkrs(p.wo, D * D * 2), r1 = mkrs(p.w1, D * F * 2), r2 = mkrs(p.w2, F * D * 2);
    const unsigned vt = lane < 49 ? lane * 128 : 0x7FFFFF00u, land = L_W1 + hw * 1024;
    dma4(r1, vt, (j * 4 + hw) * 49 * 128, land);
    if (hw == 3) { __builtin_amdgcn_s_sleep(22); wait_word(rws, WS_F1 + ((head >> 1) * 32 + (lane & 31)) * 128, tag); }
    bar();
    __builtin_amdgcn_s_sleep(32);
    dma4(r1, vt, (128 + j * 4 + hw) * 49 * 128, land);
    vm_wait<0>();
    const unsigned v2 = slab_voff(lane, 896), v1 = slab_voff(lane, 3584);
    const int ct = j % 7, ct1 = j % 14;
#pragma unroll
    for (int i = 0; i < 14; ++i) {
        int ii = i + 4 * (j / 7); if (ii >= 14) ii -= 14; if (ii >= 14) ii -= 14;
        const int pos = ii * 4 + hw, kh = pos >= 28;
        int pp = pos - 28 * kh + 7 * (ct & 3); if (pp >= 28) pp -= 28;
        dma16(rw, v2, (kh * 28 + pp) * 8 * 896 + ct * 128, L_WO + pos * 1024);
        vm_wait<4>();
    }
#pragma unroll
    for (int k = 0; k < 36; ++k) { sg[k >> 2][k & 3] = __builtin_amdgcn_raw_buffer_load_b128(r2, v2, w2src(5 + (k >> 2), hw, k & 3, j), 0); vm_wait<4>(); }
#pragma unroll
    for (int i = 0; i < 8; ++i) { W1DMA(0, i * 2 + (hw >> 1), hw & 1); vm_wait<4>(); }
}

__device__ __forceinline__ void attn_finish(LAS char* L, rsrc_t rws, int w, int lane, int rb, int head, unsigned tag ST_ARG) {
    const int qt = w >> 1, s = w & 1;
    LAS char* P = L + L_P + qt * 16384 + s * 2048 + lane * 16;
    LAS char* PL = L + L_P + qt * 16384 + 3 * 1024 + (32 + (lane & 31)) * 16;
    unsigned dst = WS_O + (((rb * 4 + qt) * 16 + head) * 2 + s) * 1024 + lane * 16;
    PIN(P); PIN(PL); PIN(dst);
    ST(7);
    wait_bar<0>();
    ST(8);
    f32x4 a[4][2];
    float lp[4];
#pragma unroll
    for (int ww = 0; ww < 4; ++ww) lp[ww] = *(LAS float*)(PL + ww * 4096);
#pragma unroll
    for (int ww = 0; ww < 4; ++ww) { a[ww][0] = *(LAS f32x4*)(P + ww * 4096); a[ww][1] = *(LAS f32x4*)(P + ww * 4096 + 1024); }
    SBAR();
    float l = (lp[0] + lp[1]) + (lp[2] + lp[3]);
    f32x4 x = (a[0][0] + a[1][0]) + (a[2][0] + a[3][0]), y = (a[0][1] + a[1][1]) + (a[2][1] + a[3][1]);
    const float asum = ((fabsf(x[0]) + fabsf(x[1])) + (fabsf(x[2]) + fabsf(x[3]))) + ((fabsf(y[0]) + fabsf(y[1])) + (fabsf(y[2]) + fabsf(y[3])));
    if (__builtin_expect(!__all(l >= 0x1p-40f && l <= 0x1p40f && asum <= 0x1p120f), 0)) {
        const u32x4 qf[2] = {ldsr128(L + L_QF + qt * 2048 + lane * 16), ldsr128(L + L_QF + qt * 2048 + 1024 + lane * 16)};
        const f32x16 o = redo_o(rws, lane, head, qf);
        l = __uint_as_float(__builtin_amdgcn_permlane32_swap(__float_as_uint(o[12]), __float_as_uint(o[12]), false, false)[1]);
#pragma unroll
        for (int k = 0; k < 4; ++k) { x[k] = s ? o[8 + k] : o[k]; y[k] = s ? o[12 + k] : o[4 + k]; }
    }
    const float il = __builtin_amdgcn_rcpf(l);
    u32x4 v = {cvtpk(x[0] * il, x[1] * il), cvtpk(x[2] * il, x[3] * il), cvtpk(y[0] * il, y[1] * il), cvtpk(y[2] * il, y[3] * il)};
    if (s == 1 && lane >= 32) { v[2] = 0u; v[3] = 0u; }
    st128<SC1>(v, rws, dst, 0);
    ST(24);
}

__device__ __forceinline__ unsigned bfsplit(float v, float& rest) { const unsigned h = cvtpk(v, v) & 0xFFFFu; rest = v - bflo(h); return h; }

__device__ __forceinline__ void mlp_helper(const Params& p, LAS char* L, rsrc_t rws, int hw, int lane, int xcd, int j, unsigned tag, const u32x4 (&sg)[9][4]) {
    const int r = lane & 31, hh = lane >> 5, ct1 = j % 14, ct = j % 7;
    const int b4 = xcd * 4 + j / 7, b2 = xcd * 2 + j / 14;
    const rsrc_t r1 = mkrs(p.w1, D * F * 2), r2 = mkrs(p.w2, F * D * 2);
    const unsigned v1 = slab_voff(lane, 3584), v2 = slab_voff(lane, 896);
    unsigned bb[2];
#pragma unroll
    for (int hf = 0; hf < 2; ++hf) bb[hf] = hh ? 0u : ((const unsigned short*)p.b1)[ct1 * 128 + hf * 64 + (hw & 1) * 32 + r];
    auto colsum = [&](int hf) INL {
        float c2[4] = {};
        LAS char* wb = L + L_W1 + hf * SLAB + slab_rd(lane, hw & 1) + (hw >> 1) * 14 * 2048;
        PIN(wb);
#pragma unroll
        for (int u = 0; u < 14; ++u) {
            const u32x4 f = wfrag<1024>(wb + u * 2048);
#pragma unroll
            for (int k = 0; k < 4; ++k) {
                if (hf == 0) c2[k] = dot2(f[k], ONES, c2[k]);
                else { c2[2 * (k & 1)] = sadd(c2[2 * (k & 1)], bflo(f[k])); c2[2 * (k & 1) + 1] = sadd(c2[2 * (k & 1) + 1], bfhi(f[k])); }
            }
        }
        float lo, lo2;
        const unsigned ch = bfsplit((c2[0] + c2[1]) + (c2[2] + c2[3]), lo), cl = bfsplit(lo, lo2);
        *(LAS u32x2*)(L + L_EXT + (2 * hf + (hw & 1)) * 1024 + lane * 16 + (hw >> 1) * 8) = (u32x2){ch | (ch << 16), cl | (bb[hf] << 16)};
    };
    auto w2dma = [&](int c) INL {
#pragma unroll
        for (int i = 0; i < 4; ++i) dma16(r2, v2, w2src(c, hw, i, j), w2slot(c) + hw * 4096 + i * 1024);
    };
    auto w2st = [&](int c) INL {
#pragma unroll
        for (int i = 0; i < 4; ++i) *(LAS u32x4*)(L + w2slot(c) + hw * 4096 + i * 1024 + lane * 16) = sg[c - 5][i];
        SBAR();
    };
    if (hw == 3) { __builtin_amdgcn_s_sleep(8); wait_word(rws, WS_F2 + ((b4 >> 1) * 16 + (lane & 15)) * 128, tag); }
    else if (hw < 2) {
#pragma unroll
        for (int u = 16; u < 28; ++u) W1DMA(0, u, hw);
#pragma unroll
        for (int u = 0; u < 12; ++u) W1DMA(1, u, hw);
    }
    bar();
    bar();
    wait_bar<0>();
    if (hw == 3) set_flag(rws, WS_F3 + (b4 * 8 + ct) * 128, tag, lane == 0);
    if (hw == 2) ((LAS unsigned*)(L + L_SYNC4))[lane] = 0u;
    if (hw < 2) {
#pragma unroll
        for (int u = 12; u < 28; ++u) W1DMA(1, u, hw);
    }
    colsum(0);
    if (hw == 3) { const int l = lane < 14 ? lane : 0; wait_word(rws, WS_F3 + ((b2 * 2 + (l >= 7)) * 8 + mod7(l)) * 128, tag); }
    wait_bar<0>();
    colsum(1);
    lds_bar();
    w2dma(0); w2dma(1); w2dma(2); w2dma(3); w2dma(4);
    bar();
    w2st(5); w2st(6); w2st(7); w2st(8);
    const unsigned f4 = WS_F4 + ((b4 >> 1) * 16 + (lane < 14 ? lane : 0)) * 128;
    if (hw == 3) wait_word(rws, f4, tag);
    wait_bar<0>();
    if (hw == 3) wait_word(rws, f4 + 256 * 128, tag);
    bar();
    w2st(9); w2st(10); w2st(11); w2st(12);
    if (hw == 3) wait_word(rws, f4 + 512 * 128, tag);
    lds_bar();
    w2st(13);
    lds_bar();
    bar();
}

__device__ __forceinline__ void mlp_compute(const Params& p, LAS char* L, rsrc_t rws, int w, int lane, int xcd, int j, unsigned tag ST_ARG) {
    const int r = lane & 31, hh = lane >> 5;
    const int r4 = j / 7, ct = j % 7, m = w & 1, kh = w >> 1;
    const int rt = xcd * 8 + r4 * 2 + m;
    const int sr[2] = {slab_rd(lane, 0), slab_rd(lane, 1)};
    u32x4 hpk[2];
    u32x4 bf[4][2];
    {
        u32x4 wf[16][2];
        LAS char* wo[2][2];
#pragma unroll
        for (int t = 0; t < 2; ++t) { wo[t][0] = L + L_WO + sr[t] + kh * 224 * 128; wo[t][1] = wo[t][0] - hh * 512; PIN(wo[t][0]); PIN(wo[t][1]); }
#pragma unroll
        for (int u = 0; u < 16; ++u)
#pragma unroll
            for (int t = 0; t < 2; ++t) {
                const int row = (u >> 1) * 28 + (u & 1) * 16;
                const u32x2 a = trread(wo[t][0] + row * 128), b = trread(wo[t][u & 1] + (row + 8) * 128);
                wf[u][t] = (u32x4){a[0], a[1], b[0], b[1]};
            }
        wait_bar<0>();
        ST(9);
        const rsrc_t rx = mkrs(p.x, S * D * 2);
        u32x2 xr[4];
#pragma unroll
        for (int i = 0; i < 4; ++i) xr[i] = __builtin_amdgcn_raw_buffer_load_b64(rx, r * 896 + hh * 8 + i * 16, rt * 32 * 896 + ct * 128 + kh * 64, 0);
        bar();
        ST(10);
        u32x4 af[16];
#pragma unroll
        for (int u = 0; u < 16; ++u) af[u] = ldh(rws, lane * 16, WS_O + (rt * 32 + kh * 16 + ((u + 4 * ct) & 15)) * 1024);
        SBAR();
        f32x16 acc[2] = {};
#pragma unroll
        for (int u = 0; u < 16; ++u) { acc[0] = mfma(wf[u][0], af[u], acc[0]); acc[1] = mfma(wf[u][1], af[u], acc[1]); }
        const f32x16 snd = kh ? acc[0] : acc[1];
        f32x16 hv = kh ? acc[1] : acc[0];
#pragma unroll
        for (int i = 0; i < 4; ++i) *(LAS f32x4*)(L + L_RED1 + (w * 4 + i) * 1024 + lane * 16) = (f32x4){snd[4 * i], snd[4 * i + 1], snd[4 * i + 2], snd[4 * i + 3]};
        ST(11);
        lds_bar();
#pragma unroll
        for (int i = 0; i < 4; ++i) {
            const f32x4 o = *(LAS f32x4*)(L + L_RED1 + ((w ^ 2) * 4 + i) * 1024 + lane * 16);
            hv[4 * i] += o[0] + bflo(xr[i][0]); hv[4 * i + 1] += o[1] + bfhi(xr[i][0]);
            hv[4 * i + 2] += o[2] + bflo(xr[i][1]); hv[4 * i + 3] += o[3] + bfhi(xr[i][1]);
        }
#pragma unroll
        for (int s = 0; s < 2; ++s) { hpk[s] = pack8(hv, s); st128<SC1>(hpk[s], rws, lane * 16, WS_H + (rt * 28 + ct * 4 + kh * 2 + s) * 1024); }
        float s1 = 0.f, s2 = 0.f;
#pragma unroll
        for (int k = 0; k < 8; ++k) { const unsigned u = hpk[k >> 2][k & 3]; s1 = dot2(u, ONES, s1); s2 = dot2(u, u, s2); }
        __builtin_amdgcn_raw_buffer_store_b64((u32x2){__float_as_uint(halfsum(s1)), __float_as_uint(halfsum(s2))}, rws,
                                              hh ? 0x7FFFFFF0u : WS_HS + ((rt * 14 + ct * 2 + kh) * 32 + r) * 8, 0, SC1);
        wait_bar<0>();
        ST(12);
    }
    {
        const int ct1 = j % 14, rt1 = xcd * 8 + (j / 14) * 4 + w;
        auto RD = [&](int u) INL {
#pragma unroll
            for (int t = 0; t < 2; ++t) bf[u & 3][t] = wfrag<1024>(L + L_W1 + sr[t] + u * 2048);
            SBAR();
        };
        RD(0); RD(1); RD(2);
        bar();
        ST(13);
        u32x4 af[28];
        auto aload = [&](int u) INL { int uu = u + 2 * ct1; if (uu >= 28) uu -= 28; af[u] = ldh(rws, lane * 16, WS_H + (rt1 * 28 + uu) * 1024); };
        constexpr int WIN = 10;
#pragma unroll
        for (int u = 0; u < WIN; ++u) aload(u);
        SBAR();
        f32x16 acc[4] = {};
        u32x2 st[7];
        u32x4 b2f[4];
        LAS char* w1b[2] = {L + L_W1 + SLAB + sr[0], L + L_W1 + SLAB + sr[1]};
        PIN(w1b[0]); PIN(w1b[1]);
        auto RD2 = [&](int z) INL { b2f[z & 3] = wfrag<1024>(w1b[z / 28] + (z % 28) * 2048); SBAR(); };
        sfor<28>([&](auto ic) INL {
            constexpr int u = ic.value;
            if constexpr (u + 3 < 28) RD(u + 3); else RD2(u + 3 - 28);
            acc[0] = mfma(bf[u & 3][0], af[u], acc[0]); acc[1] = mfma(bf[u & 3][1], af[u], acc[1]);
            PIN(acc[0]); PIN(acc[1]); SBAR();
            if constexpr (u + WIN < 28) aload(u + WIN);
            else if constexpr (u + WIN < 35) st[u + WIN - 28] = __builtin_amdgcn_raw_buffer_load_b64(rws, (hh * 7 * 32 + r) * 8, WS_HS + (rt1 * 14 + u + WIN - 28) * 256, SC1);
            SBAR();
        });
        ST(14);
        bar();
        u32x4 ext[4];
#pragma unroll
        for (int t = 0; t < 4; ++t) ext[t] = ldsr128(L + L_EXT + t * 1024 + lane * 16);
        float s1 = 0.f, s2 = 0.f;
#pragma unroll
        for (int k = 0; k < 7; ++k) { s1 += __uint_as_float(st[k][0]); s2 += __uint_as_float(st[k][1]); }
        const float mu = halfsum(s1) * (1.0f / D);
        const float var = fmaxf(halfsum(s2) * (1.0f / D) - mu * mu, 0.f);
        const float r1 = __builtin_amdgcn_rsqf(var + EPS);
        const float rs = r1 * __builtin_amdgcn_rsqf(var * r1 * r1 + EPS), irs = __builtin_amdgcn_rcpf(rs);
        float t0, t1;
        const unsigned mh = bfsplit(-mu, t0), ml = bfsplit(t0, t1), ih = bfsplit(irs, t0), il = bfsplit(t0, t1);
        const u32x4 xe = {mh | (ml << 16), mh | (ih << 16), mh | (ml << 16), mh | (il << 16)};
        acc[0] = mfma(ext[0], xe, acc[0]); acc[1] = mfma(ext[1], xe, acc[1]);
        acc[2] = mfma(ext[2], xe, acc[2]); acc[3] = mfma(ext[3], xe, acc[3]);
        PIN(acc[0]); PIN(acc[1]); PIN(acc[2]); PIN(acc[3]); SBAR();
        constexpr float C0 = -2.0f * 0.7978845608028654f * 1.4426950408889634f, C1 = C0 * 0.044715f;
        const float k0 = C0 * rs, k1 = C1 * rs * rs * rs;
        f32x4 ga;
        auto gelu = [&](f32x16& v, int i, int stage) INL {
#pragma unroll
            for (int k = 0; k < 4; ++k) {
                const float a = v[4 * i + k];
                if (stage == 0) ga[k] = smul(a, sfma(smul(a, a), k1, k0));
                if (stage == 1) ga[k] = __builtin_amdgcn_exp2f(ga[k]);
                if (stage == 2) ga[k] = __builtin_amdgcn_rcpf(sfma(ga[k], irs, irs));
                if (stage == 3) v[4 * i + k] = smul(a, ga[k]);
            }
            if (stage < 3) PIN(ga); else PIN(v);
            SBAR();
        };
        auto put = [&](int t) INL {
#pragma unroll
            for (int s = 0; s < 2; ++s) st128<SC1>(pack8(acc[t], s), rws, lane * 16, WS_G + (rt1 * 112 + ct1 * 8 + t * 2 + s) * 1024);
        };
        auto flag = [&](int stage) INL { publish(L + L_SYNC4 + stage * 4, rws, WS_F4 + (stage * 256 + (xcd * 2 + j / 14) * 16 + ct1) * 128, tag, lane); };
        sfor<56>([&](auto ic) INL {
            constexpr int z = ic.value, t = 2 + z / 28;
            if constexpr (z + 3 < 56) RD2(z + 3);
            acc[t] = mfma(b2f[z & 3], af[z % 28], acc[t]); PIN(acc[t]); SBAR();
            constexpr int e = z - 4;
            if constexpr (e >= 0 && e < 48) { gelu(acc[e >> 4], (e >> 2) & 3, e & 3); if constexpr ((e & 15) == 15) { put(e >> 4); SBAR(); } }
            if constexpr (z == 47) { vm_wait<0>(); flag(0); SBAR(); }
        });
        ST(15);
        bar();
#pragma unroll
        for (int k = 0; k < 16; ++k) {
            const float a = acc[3][k];
            acc[3][k] = a * __builtin_amdgcn_rcpf(fmaf(__builtin_amdgcn_exp2f(a * fmaf(a * a, k1, k0)), irs, irs));
        }
        put(3);
        vm_wait<2>(); flag(1);
        ST(16);
        bar();
        ST(17);
        constexpr int WN2 = 10;
        u32x4 ag[WN2];
        const int rot2 = fc2_rot(j);
        const unsigned abase = WS_G + (rt * 112 + kh * 56) * 1024;
#pragma unroll
        for (int u = 0; u < WN2; ++u) ag[u] = ldh(rws, lane * 16, abase + fc2_kstep(u, rot2) * 1024);
        SBAR();
        vm_wait<WN2>(); flag(2);
        LAS char* w2b[3][2];
#pragma unroll
        for (int k = 0; k < 6; ++k) { w2b[k >> 1][k & 1] = L + sr[k & 1] + kh * 8192 + (k >> 1) * 48 * K1; PIN(w2b[k >> 1][k & 1]); }
        auto RD3 = [&](int u) INL {
#pragma unroll
            for (int t = 0; t < 2; ++t) bf[u & 3][t] = wfrag<1024>(w2b[w2slot(u >> 2) / (48 * K1)][t] + w2slot(u >> 2) % (48 * K1) + (u & 3) * 2048);
            SBAR();
        };
        RD3(0); RD3(1); RD3(2);
        ST(18);
        f32x16 a2[2] = {};
        u32x2 bb[4];
        sfor<56>([&](auto ic) INL {
            constexpr int u = ic.value;
            if constexpr (u == 16 || u == 32 || u == 44) bar();
            if constexpr (u == 16) ST(19);
            if constexpr (u + 3 < 56) RD3(u + 3);
            a2[0] = mfma(bf[u & 3][0], ag[u % WN2], a2[0]); a2[1] = mfma(bf[u & 3][1], ag[u % WN2], a2[1]);
            PIN(a2[0]); PIN(a2[1]); SBAR();
            if constexpr (u + WN2 < 56) ag[u % WN2] = ldh(rws, lane * 16, abase + fc2_kstep(u + WN2, rot2) * 1024);
            else if constexpr (u + WN2 < 60) bb[u + WN2 - 56] = __builtin_amdgcn_raw_buffer_load_b64(mkrs(p.b2, D * 2), hh * 8 + (u + WN2 - 56) * 16, ct * 128 + kh * 64, 0);
            SBAR();
        });
        ST(20);
        const f32x16 snd = kh ? a2[0] : a2[1];
        f32x16 v = kh ? a2[1] : a2[0];
#pragma unroll
        for (int i = 0; i < 4; ++i) *(LAS f32x4*)(L + L_RED2 + (w * 4 + i) * 1024 + lane * 16) = (f32x4){snd[4 * i], snd[4 * i + 1], snd[4 * i + 2], snd[4 * i + 3]};
        lds_bar();
        unsigned o[8];
#pragma unroll
        for (int i = 0; i < 4; ++i) {
            const f32x4 t = *(LAS f32x4*)(L + L_RED2 + ((w ^ 2) * 4 + i) * 1024 + lane * 16);
            const unsigned h0 = hpk[i >> 1][2 * (i & 1)], h1 = hpk[i >> 1][2 * (i & 1) + 1];
            o[2 * i] = cvtpk(v[4 * i] + t[0] + bflo(bb[i][0]) + bflo(h0), v[4 * i + 1] + t[1] + bfhi(bb[i][0]) + bfhi(h0));
            o[2 * i + 1] = cvtpk(v[4 * i + 2] + t[2] + bflo(bb[i][1]) + bflo(h1), v[4 * i + 3] + t[3] + bfhi(bb[i][1]) + bfhi(h1));
        }
        const rsrc_t ro = mkrs(p.out, S * D * 2);
#pragma unroll
        for (int k = 0; k < 2; ++k) {
            auto s0 = __builtin_amdgcn_permlane32_swap(o[4 * k], o[4 * k + 2], false, false);
            auto s1_ = __builtin_amdgcn_permlane32_swap(o[4 * k + 1], o[4 * k + 3], false, false);
            st128<0>((u32x4){s0[0], s1_[0], s0[1], s1_[1]}, ro, r * 896 + hh * 16 + k * 32, rt * 32 * 896 + ct * 128 + kh * 64);
        }
        ST(21);
    }
}

__global__ void __launch_bounds__(NT, 2) k_block(Params p) {
    extern __shared__ __attribute__((aligned(16))) char smem[];
    LAS char* L = (LAS char*)smem;
    ST_DECL
    ST(0);
    const int lane = threadIdx.x & 63, w = __builtin_amdgcn_readfirstlane(threadIdx.x >> 6);
    const int b = blockIdx.x, xcd = b & 7, j = b >> 3;
    const int rb = (xcd & 1) * 8 + (j & 7), sub = j >> 3, head = 4 * (xcd >> 1) + sub, pair = 2 * (xcd >> 1) + (sub >> 1), kind = sub & 1;
    const rsrc_t rws = mkrs(p.ws, WS_END);
    const int rot = ((j & 7) + 2 * sub) % 7;
    if (w == 7) {
        unsigned long long pc;
        asm volatile("s_getpc_b64 %0" : "=s"(pc));
        dma4(mkrs((const void*)pc, CODE_LEN), (lane * 8 + (j & 7)) * 128, 0, P1_EX + 1536);
    }
    unsigned tag;
    if (w < 4) {
        tag = load_tag(p);
        if (kind) p1_compute<true>(L, rws, w, lane, rb, pair, head, tag ST_PASS);
        else p1_compute<false>(L, rws, w, lane, rb, pair, head, tag ST_PASS);
        ST(4);
        attn_compute(L, rws, w, lane, head ST_PASS);
        attn_finish(L, rws, w, lane, rb, head, tag ST_PASS);
        if (j < 28) mlp_compute(p, L, rws, w, lane, xcd, j, tag ST_PASS);
        else wait_bar<0>();
    } else {
        if (w < 6) p1_loader(p, w - 4, lane, rb, pair, kind, head, rot, tag);
        else { tag = load_tag(p); p1_colsum(L, w - 6, lane); }
        ST(4);
        u32x4 sg[9][4];
        attn_helper(p, rws, w - 4, lane, head, j, tag, sg);
        attn_finish(L, rws, w, lane, rb, head, tag ST_PASS);
        wait_bar<0>();
        if (w == 7) set_flag(rws, WS_F2 + (rb * 16 + head) * 128, tag, lane == 0);
        ST(9);
        if (j < 28) mlp_helper(p, L, rws, w - 4, lane, xcd, j, tag, sg);
    }
    if (b == 0 && w == 0) set_flag(rws, WS_GEN, tag, lane == 0);
    ST_FLUSH();
}

extern "C" void kernel_launch(void* const* d_in, const int* in_sizes, int n_in, void* d_out, int out_size, void* d_ws, size_t ws_size, hipStream_t stream) {
    Params p;
    p.x = d_in[0]; p.wqkv = d_in[1]; p.wo = d_in[2]; p.w1 = d_in[4]; p.b1 = d_in[5]; p.w2 = d_in[6]; p.b2 = d_in[7];
    p.out = d_out; p.ws = d_ws;
    (void)hipFuncSetAttribute((const void*)k_block, hipFuncAttributeMaxDynamicSharedMemorySize, LDS_BYTES);
    k_block<<<NWG, NT, LDS_BYTES, stream>>>(p);
}
```

```cpp
#define STAMPS
#include <hip/hip_runtime.h>
#include <stdint.h>
#include <utility>
#include <cstdio>

#define LAS __attribute__((address_space(3)))
#define GAS __attribute__((address_space(1)))
typedef __bf16 bf2 __attribute__((ext_vector_type(2)));
typedef __bf16 bf8 __attribute__((ext_vector_type(8)));
typedef __bf16 bf16v __attribute__((ext_vector_type(16)));
typedef float f32x4 __attribute__((ext_vector_type(4)));
typedef float f32x8 __attribute__((ext_vector_type(8)));
typedef float f32x16 __attribute__((ext_vector_type(16)));
typedef unsigned u32x2 __attribute__((ext_vector_type(2)));
typedef unsigned u32x4 __attribute__((ext_vector_type(4)));
typedef short s16x4 __attribute__((ext_vector_type(4)));
typedef __amdgpu_buffer_rsrc_t rsrc_t;

constexpr int S = 2048, D = 448, H = 16, F = 1792;
constexpr int NT = 512, NWG = 256;
constexpr float EPS = 1e-5f;
constexpr int LDS_BYTES = 160 * 1024;
constexpr int SC1 = 16;
constexpr unsigned CODE_LEN = 52 * 1024;
constexpr unsigned WS_GEN = 0;
constexpr unsigned WS_F1 = 4096;
constexpr unsigned WS_F2 = WS_F1 + 256 * 128;
constexpr unsigned WS_F3 = WS_F2 + 256 * 128;
constexpr unsigned WS_F4 = WS_F3 + 256 * 128;
constexpr unsigned WS_HS = WS_F4 + 768 * 128;
constexpr unsigned WS_KV = 512 * 1024;
constexpr unsigned WS_O = WS_KV + 16 * 64 * 4096;
constexpr unsigned WS_H = WS_O + 64 * 32 * 1024;
constexpr unsigned WS_G = WS_H + 64 * 28 * 1024;
constexpr unsigned WS_END = WS_G + 64 * 112 * 1024;

struct Params {
    const void *x, *wqkv, *wo, *w1, *b1, *w2, *b2;
    void *out, *ws;
};

template <class Fn, int... I> __device__ __forceinline__ void sfor_(Fn&& f, std::integer_sequence<int, I...>) { (f(std::integral_constant<int, I>{}), ...); }
template <int N, class Fn> __device__ __forceinline__ void sfor(Fn&& f) { sfor_(f, std::make_integer_sequence<int, N>{}); }
#define INL __attribute__((always_inline))
__device__ __forceinline__ rsrc_t mkrs(const void* p, unsigned bytes) { return __builtin_amdgcn_make_buffer_rsrc((void*)p, 0, (int)bytes, 0x00020000); }
__device__ __forceinline__ void dma16(rsrc_t rs, unsigned voff, unsigned soff, unsigned lds) {
    asm volatile("s_mov_b32 m0, %0\n\ts_nop 0\n\tbuffer_load_dwordx4 %1, %2, %3 offen lds" ::"s"(lds), "v"(voff), "s"(rs), "s"(soff) : "memory");
}
__device__ __forceinline__ void dma4(rsrc_t rs, unsigned voff, unsigned soff, unsigned lds) {
    asm volatile("s_mov_b32 m0, %0\n\ts_nop 0\n\tbuffer_load_dword %1, %2, %3 offen lds" ::"s"(lds), "v"(voff), "s"(rs), "s"(soff) : "memory");
}
__device__ __forceinline__ void dma4_if0(int skip, rsrc_t rs, unsigned voff, unsigned soff, unsigned lds) {
    asm volatile("s_cmp_lg_u32 %4, 0\n\ts_cbranch_scc1 1f\n\ts_mov_b32 m0, %0\n\ts_nop 0\n\tbuffer_load_dword %1, %2, %3 offen lds\n1:" ::"s"(lds), "v"(voff), "s"(rs), "s"(soff), "s"(skip) : "memory", "scc");
}
template <int N> __device__ __forceinline__ void wait_bar() { asm volatile("s_waitcnt vmcnt(%0) lgkmcnt(0)\n\ts_barrier" ::"i"(N) : "memory"); }
template <int N> __device__ __forceinline__ void vm_wait() { asm volatile("s_waitcnt vmcnt(%0)" ::"i"(N) : "memory"); }
__device__ __forceinline__ void lds_bar() { asm volatile("s_waitcnt lgkmcnt(0)\n\ts_barrier" ::: "memory"); }
__device__ __forceinline__ void bar() { asm volatile("s_barrier" ::: "memory"); }
__device__ __forceinline__ void lds_wait() { asm volatile("s_waitcnt lgkmcnt(0)" ::: "memory"); }
__device__ __forceinline__ f32x16 mfma(u32x4 a, u32x4 b, f32x16 c) {
    return __builtin_amdgcn_mfma_f32_32x32x16_bf16(__builtin_bit_cast(bf8, a), __builtin_bit_cast(bf8, b), c, 0, 0, 0);
}
__device__ __forceinline__ float dot2(unsigned a, unsigned b, float c) {
    return __builtin_amdgcn_fdot2_f32_bf16(__builtin_bit_cast(bf2, a), __builtin_bit_cast(bf2, b), c, false);
}
__device__ __forceinline__ unsigned cvtpk(float lo, float hi) {
    typedef float f2 __attribute__((ext_vector_type(2)));
    return __builtin_bit_cast(unsigned, __builtin_convertvector((f2){lo, hi}, bf2));
}
__device__ __forceinline__ float halfsum(float v) {
    auto r = __builtin_amdgcn_permlane32_swap(__float_as_uint(v), __float_as_uint(v), false, false);
    return __uint_as_float(r[0]) + __uint_as_float(r[1]);
}
template <int AUX> __device__ __forceinline__ void st128(u32x4 v, rsrc_t rs, unsigned voff, unsigned soff) { __builtin_amdgcn_raw_buffer_store_b128(v, rs, voff + soff, 0, AUX); }
__device__ __forceinline__ u32x4 ldh(rsrc_t rs, unsigned voff, unsigned soff) { return __builtin_amdgcn_raw_buffer_load_b128(rs, voff, soff, SC1); }
__device__ __forceinline__ u32x4 ldsr128(LAS char* p) { return *(LAS u32x4*)p; }
__device__ __forceinline__ u32x2 trread(LAS char* p) {
    return __builtin_bit_cast(u32x2, __builtin_amdgcn_ds_read_tr16_b64_v4i16((LAS s16x4*)p));
}
template <int ESTEP> __device__ __forceinline__ u32x4 wfrag(LAS char* p) {
    const u32x2 a = trread(p), b = trread(p + ESTEP);
    return (u32x4){a[0], a[1], b[0], b[1]};
}
__device__ __forceinline__ u32x4 pack8(const f32x16& v, int s) {
    return (u32x4){cvtpk(v[8 * s], v[8 * s + 1]), cvtpk(v[8 * s + 2], v[8 * s + 3]), cvtpk(v[8 * s + 4], v[8 * s + 5]), cvtpk(v[8 * s + 6], v[8 * s + 7])};
}
__device__ __forceinline__ float smul(float a, float b) { float r; asm("v_mul_f32 %0, %1, %2" : "=v"(r) : "v"(a), "v"(b)); return r; }
__device__ __forceinline__ float sadd(float a, float b) { float r; asm("v_add_f32 %0, %1, %2" : "=v"(r) : "v"(a), "v"(b)); return r; }
__device__ __forceinline__ float sfma(float a, float b, float c) { float r; asm("v_fma_f32 %0, %1, %2, %3" : "=v"(r) : "v"(a), "v"(b), "v"(c)); return r; }
__device__ __forceinline__ float bflo(unsigned u) { return __uint_as_float(u << 16); }
__device__ __forceinline__ float bfhi(unsigned u) { return __uint_as_float(u & 0xFFFF0000u); }
constexpr unsigned ONES = 0x3F803F80u;
#define PIN(x) asm volatile("" : "+v"(x))
#define SBAR() __builtin_amdgcn_sched_barrier(0)
constexpr unsigned WS_ST = 32u << 20;
#define ST(id) do { unsigned long long t_, c_; asm volatile("s_memrealtime %0\n\ts_memtime %1\n\ts_waitcnt lgkmcnt(0)" : "=s"(t_), "=s"(c_) :: "memory"); stt[id] = t_; stc[id] = (unsigned)c_; } while (0)
#define ST_DECL unsigned stt[32] = {}, stc[32] = {};
#define ST_ARG , unsigned (&stt)[32], unsigned (&stc)[32]
#define ST_PASS , stt, stc
#define ST_FLUSH() do { if ((threadIdx.x & 63) == 0) { _Pragma("unroll") for (int i_ = 0; i_ < 32; ++i_) { ((unsigned*)((char*)p.ws + WS_ST))[((i_) * 256 + blockIdx.x) * 8 + (threadIdx.x >> 6)] = stt[i_]; ((unsigned*)((char*)p.ws + WS_ST))[((i_ + 32) * 256 + blockIdx.x) * 8 + (threadIdx.x >> 6)] = stc[i_]; } } } while (0)

constexpr int K1 = 1024;
constexpr int P1_XS = 0, P1_WS = 64 * K1, P1_WQ = 120 * K1, P1_EX = 148 * K1;
constexpr int L_SYNC1 = 150 * K1, L_SYNC4 = 144 * K1;
constexpr int L_P = 32 * K1, L_WO = 96 * K1, L_QF = 152 * K1;
constexpr int L_W1 = 0, SLAB = 56 * K1, L_RED1 = 80 * K1, L_EXT = 156 * K1, L_RED2 = 144 * K1;
__host__ __device__ constexpr int w2slot(int c) { return ((c % 9) < 2 ? 112 + 16 * (c % 9) : 16 * ((c % 9) - 2)) * K1; }

__device__ __forceinline__ void set_flag(rsrc_t rws, unsigned off, unsigned tag, bool me) {
    __builtin_amdgcn_raw_buffer_store_b32(tag, rws, me ? off : 0x7FFFFFF0u, 0, SC1);
}
__device__ __forceinline__ void publish(LAS char* cnt, rsrc_t rws, unsigned off, unsigned tag, int lane) {
    unsigned old = 0;
    if (lane == 0) old = __hip_atomic_fetch_add((LAS unsigned*)cnt, 1u, __ATOMIC_RELAXED, __HIP_MEMORY_SCOPE_WORKGROUP);
    set_flag(rws, off, tag, lane == 0 && old == 3u);
}
__device__ __forceinline__ void wait_word(rsrc_t rws, unsigned off, unsigned tag) {
#pragma unroll 1
    for (unsigned spins = 0; spins < (1u << 20); ++spins) {
        asm volatile("" ::: "memory");
        if (__all(__builtin_amdgcn_raw_buffer_load_b32(rws, off, 0, SC1) == tag)) break;
        __builtin_amdgcn_s_sleep(2);
    }
    asm volatile("" ::: "memory");
}
__device__ __forceinline__ unsigned load_tag(const Params& p) {
    unsigned g;
    asm volatile("s_load_dword %0, %1, 0x0\n\ts_waitcnt lgkmcnt(0)" : "=s"(g) : "s"(p.ws) : "memory");
    return g + 1;
}
__device__ __forceinline__ int mod7(int v) { return v >= 7 ? v - 7 : v; }

__device__ __forceinline__ int p1_wrd(int lane, int t) {
    const int q = (lane & 15) >> 2, pp = lane & 3, g1 = (lane >> 4) & 1, hh = lane >> 5;
    return P1_WS + (8 * hh + q) * 128 + (((7 * t + 4 * g1 + pp) ^ ((q >> 1) << 3)) << 3);
}
template <bool VT>
__device__ __forceinline__ void p1_compute(LAS char* L, rsrc_t rws, int w, int lane, int rb, int pair, int head, unsigned tag ST_ARG) {
    const int r = lane & 31, hh = lane >> 5;
    LAS char* xa[4];
#pragma unroll
    for (int uu = 0; uu < 4; ++uu) xa[uu] = L + P1_XS + w * 4096 + r * 128 + (((2 * uu + hh) ^ ((r >> 1) & 7)) << 4);
    LAS char* wb[2] = {L + p1_wrd(lane, 0), L + p1_wrd(lane, 1)};
    PIN(wb[0]); PIN(wb[1]);
    u32x4 ones = {ONES, ONES, ONES, ONES};
    PIN(ones);
    u32x4 xf[28];
    f32x16 acc[2] = {};
    float sx[2] = {}, sxx[2] = {};
    u32x4 bf[8][2];
    auto RD = [&](int u) INL {
        xf[u] = ldsr128(xa[u & 3] + ((u >> 2) & 3) * 16384);
        bf[u & 7][0] = wfrag<512>(wb[0] + u * 2048);
        bf[u & 7][1] = wfrag<512>(wb[1] + u * 2048);
        SBAR();
    };
    auto MM = [&](int u) INL {
        acc[0] = VT ? mfma(xf[u], bf[u & 7][0], acc[0]) : mfma(bf[u & 7][0], xf[u], acc[0]);
        acc[1] = VT ? mfma(xf[u], bf[u & 7][1], acc[1]) : mfma(bf[u & 7][1], xf[u], acc[1]);
        PIN(acc[0]); PIN(acc[1]); SBAR();
    };
    auto SQ = [&](int c) INL {
#pragma unroll
        for (int k = 0; k < 16; ++k) { const unsigned a = xf[4 * c + (k >> 2)][k & 3]; sx[k & 1] = dot2(a, ONES, sx[k & 1]); sxx[k & 1] = dot2(a, a, sxx[k & 1]); }
        PIN(sx[0]); PIN(sx[1]); PIN(sxx[0]); PIN(sxx[1]); SBAR();
    };
    sfor<7>([&](auto ic) INL {
        constexpr int c = ic.value;
        lds_bar();
        if (c == 0) ST(1);
#pragma unroll
        for (int i = 0; i < 4; ++i) { if (c > 0 && c < 6) MM(4 * c - 4 + i); RD(4 * c + i); }
        SQ(c);
        if constexpr (c >= 5) { MM(4 * c); MM(4 * c + 1); MM(4 * c + 2); MM(4 * c + 3); }
    });
    ST(2);
    const float mu = halfsum(sx[0] + sx[1]) * (1.0f / D);
    const float var = fmaxf(halfsum(sxx[0] + sxx[1]) * (1.0f / D) - mu * mu, 0.f);
    const float rstd = __builtin_amdgcn_rsqf(var + EPS);
    LAS float* ex = (LAS float*)(L + P1_EX + w * 256);
    LAS float* cs = (LAS float*)(L + P1_EX + 1024);
    const int tile = rb * 4 + w;
    if (VT) ex[lane] = hh ? rstd : -mu * rstd;
    lds_bar();
    if (VT) {
        f32x4 a4[4], b4[4];
#pragma unroll
        for (int i = 0; i < 4; ++i) { a4[i] = *(LAS f32x4*)(ex + 8 * i + 4 * hh); b4[i] = *(LAS f32x4*)(ex + 32 + 8 * i + 4 * hh); }
#pragma unroll
        for (int t = 0; t < 2; ++t) {
            const float c = cs[32 * t + r];
            f32x16 v;
#pragma unroll
            for (int k = 0; k < 16; ++k) v[k] = fmaf(acc[t][k], b4[k >> 2][k & 3], a4[k >> 2][k & 3] * c);
#pragma unroll
            for (int s = 0; s < 2; ++s) {
                u32x4 o = pack8(v, s);
                if (r >= 28) { const unsigned z = r == 28 ? ONES : 0u; o = (u32x4){z, z, z, z}; }
                st128<SC1>(o, rws, lane * 16, WS_KV + ((2 * pair + t) * 64 + tile) * 4096 + (2 + s) * 1024);
            }
        }
    } else {
        const int rp = (r & 19) | ((r & 4) << 1) | ((r & 8) >> 1);
        const float nm = -mu * rstd;
#pragma unroll
        for (int t = 0; t < 2; ++t) {
            f32x16 v;
#pragma unroll
            for (int i = 0; i < 4; ++i) {
                const f32x4 c4 = *(LAS f32x4*)(cs + 32 * t + 8 * i + 4 * hh);
#pragma unroll
                for (int k = 0; k < 4; ++k) v[4 * i + k] = fmaf(acc[t][4 * i + k], rstd, nm * c4[k]);
            }
#pragma unroll
            for (int s = 0; s < 2; ++s) {
                u32x4 o = pack8(v, s);
                if (s == 1 && hh) { o[2] = 0u; o[3] = 0u; }
                st128<SC1>(o, rws, (rp + 32 * hh) * 16, WS_KV + ((2 * pair + t) * 64 + tile) * 4096 + s * 1024);
            }
        }
    }
    ST(3);
    lds_bar();
    LAS char* wq = L + P1_WQ + (8 * hh + ((lane & 15) >> 2)) * 64 + ((head & 1) + 4 * ((lane >> 4) & 1) + (lane & 3)) * 8;
    PIN(wq);
    f32x16 aq = {}, cq = {};
    u32x4 bq[4];
    bq[0] = wfrag<256>(wq); bq[1] = wfrag<256>(wq + 1024); bq[2] = wfrag<256>(wq + 2048);
    sfor<28>([&](auto ic) INL {
        constexpr int u = ic.value;
        if constexpr (u + 3 < 28) bq[(u + 3) & 3] = wfrag<256>(wq + (u + 3) * 1024);
        SBAR();
        aq = mfma(bq[u & 3], xf[u], aq);
        cq = mfma(bq[u & 3], ones, cq);
        PIN(aq); PIN(cq); SBAR();
        if constexpr (u == 11) { vm_wait<0>(); publish(L + L_SYNC1, rws, WS_F1 + ((pair * 16 + rb) * 2 + (VT ? 1 : 0)) * 128, tag, lane); SBAR(); }
    });
    const float qs = rstd * (0.18898223650461362f * 1.4426950408889634f), nmq = -mu * qs;
    f32x16 v;
#pragma unroll
    for (int k = 0; k < 16; ++k) v[k] = fmaf(aq[k], qs, nmq * cq[k]);
#pragma unroll
    for (int s = 0; s < 2; ++s) {
        u32x4 o = pack8(v, s);
        if (s == 1 && hh) { o[2] = 0u; o[3] = 0u; }
        *(LAS u32x4*)(L + L_QF + (w * 2 + s) * 1024 + lane * 16) = o;
    }
}

__device__ __forceinline__ void p1_loader(const Params& p, int lw, int lane, int rb, int pair, int kind, int head, int rot, unsigned& tag) {
    const rsrc_t rx = mkrs(p.x, S * D * 2), rw = mkrs(p.wqkv, D * 3 * D * 2);
    unsigned vx[2];
#pragma unroll
    for (int i = 0; i < 2; ++i) vx[i] = (lane >> 3) * 896 + (((lane & 7) ^ ((4 * i + (lane >> 4)) & 7)) << 4);
    int piece = (lane & 7) ^ (((lane >> 4) & 1) << 2);
    if (piece == 7) piece = 6;
    const unsigned vw = (lane >> 3) * 2688 + piece * 16;
    const unsigned xb = (rb * 128 + lw * 64) * 896, wb = lw * 32 * 2688 + (448 * (1 + kind) + 56 * pair) * 2;
    auto chunk = [&](int c) INL {
        const int cc = mod7(c + rot);
#pragma unroll
        for (int i = 0; i < 4; ++i) dma16(rw, vw, wb + (cc * 64 + i * 8) * 2688, P1_WS + (c * 64 + lw * 32 + i * 8) * 128);
#pragma unroll
        for (int i = 0; i < 8; ++i) dma16(rx, vx[i & 1], xb + i * 8 * 896 + cc * 128, P1_XS + (c & 3) * 16384 + lw * 8192 + i * 1024);
    };
    const unsigned vq = (lane >> 2) * 2688 + (lane & 3) * 16;
    const unsigned qb = 56 * head - 8 * (head & 1) + lw * 32 * 2688;
    auto wq = [&](int c) INL {
#pragma unroll
        for (int i = 0; i < 2; ++i) dma16(rw, vq, (mod7(c + rot) * 64 + i * 16) * 2688 + qb, P1_WQ + (c * 64 + lw * 32 + i * 16) * 64);
    };
    chunk(0); chunk(1);
#pragma unroll
    for (int c = 0; c < 5; ++c) { chunk(c + 2); wait_bar<24>(); }
    wq(0); wq(1); wq(2);
    wait_bar<18>();
    wq(3); wq(4);
    wait_bar<10>();
    wq(5); wq(6);
    tag = load_tag(p);
    bar();
    wait_bar<0>();
}

__device__ __forceinline__ void p1_colsum(LAS char* L, int t, int lane) {
    LAS char* wb = L + p1_wrd(lane, t);
    PIN(wb);
    ((LAS unsigned*)(L + L_SYNC1))[t] = 0u;
    float c2[4] = {};
#pragma unroll
    for (int c = 0; c < 7; ++c) {
        lds_bar();
#pragma unroll
        for (int uu = 0; uu < 4; ++uu) {
            const u32x4 b = wfrag<512>(wb + (4 * c + uu) * 2048);
#pragma unroll
            for (int j = 0; j < 4; ++j) { c2[2 * (j & 1)] = sadd(c2[2 * (j & 1)], bflo(b[j])); c2[2 * (j & 1) + 1] = sadd(c2[2 * (j & 1) + 1], bfhi(b[j])); }
        }
        PIN(c2[0]); PIN(c2[1]); PIN(c2[2]); PIN(c2[3]);
    }
    ((LAS float*)(L + P1_EX + 1024))[32 * t + (lane & 31)] = halfsum((c2[0] + c2[1]) + (c2[2] + c2[3]));
    lds_bar();
    lds_bar();
}

__device__ __forceinline__ f32x16 pv(u32x4 v, f32x8 pr, f32x16 o) {
    return __builtin_amdgcn_smfmac_f32_32x32x32_bf16(__builtin_bit_cast(bf8, v), __builtin_bit_cast(bf16v, pr), o, 0xDDDD, 0, 0);
}
__device__ __forceinline__ void mfma0(f32x16& s, u32x4 a, u32x4 b) { asm volatile("v_mfma_f32_32x32x16_bf16 %0, %1, %2, 0" : "+v"(s) : "v"(a), "v"(b)); }
__device__ __forceinline__ f32x8 lo8(const f32x16& s) { return __builtin_shufflevector(s, s, 0, 1, 2, 3, 4, 5, 6, 7); }
__device__ __forceinline__ f32x8 hi8(const f32x16& s) { return __builtin_shufflevector(s, s, 8, 9, 10, 11, 12, 13, 14, 15); }
__device__ __forceinline__ unsigned slab_voff(int lane, int pitch) { return (lane >> 3) * pitch + (((lane & 7) ^ (((lane >> 4) & 1) << 2)) << 4); }
__device__ __forceinline__ int slab_rd(int lane, int t) {
    const int q = (lane & 15) >> 2, pp = lane & 3, g1 = (lane >> 4) & 1, hh = lane >> 5;
    return (4 * hh + q) * 128 + (((8 * t + 4 * g1 + pp) ^ ((q >> 1) << 3)) << 3);
}
__device__ __forceinline__ f32x16 redo_o(rsrc_t rws, int lane, int head, const u32x4 (&qf)[2]) {
    float m = -INFINITY;
#pragma unroll 1
    for (int t = 0; t < 64; ++t) {
        const unsigned b = WS_KV + (head * 64 + t) * 4096;
        f32x16 s = {};
        s = mfma(ldh(rws, lane * 16, b), qf[0], s);
        s = mfma(ldh(rws, lane * 16 + 1024, b), qf[1], s);
#pragma unroll
        for (int k = 0; k < 16; ++k) m = fmaxf(m, s[k]);
    }
    auto mm = __builtin_amdgcn_permlane32_swap(__float_as_uint(m), __float_as_uint(m), false, false);
    m = fmaxf(__uint_as_float(mm[0]), __uint_as_float(mm[1]));
    f32x16 o = {};
#pragma unroll 1
    for (int t = 0; t < 64; ++t) {
        const unsigned b = WS_KV + (head * 64 + t) * 4096;
        f32x16 s = {};
        s = mfma(ldh(rws, lane * 16, b), qf[0], s);
        s = mfma(ldh(rws, lane * 16 + 1024, b), qf[1], s);
#pragma unroll
        for (int k = 0; k < 16; ++k) s[k] = __builtin_amdgcn_exp2f(s[k] - m);
        o = pv(ldh(rws, lane * 16 + 2048, b), lo8(s), o);
        o = pv(ldh(rws, lane * 16 + 3072, b), hi8(s), o);
    }
    return o;
}

#define W1DMA(hf, u, e) do { int uu_ = (u) + 2 * ct1; if (uu_ >= 28) uu_ -= 28; dma16(r1, v1, (uu_ * 16 + (e) * 8) * 3584 + ct1 * 256 + (hf) * 128, L_W1 + (hf) * SLAB + (u) * 2048 + (e) * 1024); } while (0)

__host__ __device__ constexpr int step_q(int n) { return n < 52 ? (n & 3) : (n - 52) / 3; }
__host__ __device__ constexpr int step_k(int n) { return n < 52 ? (n >> 2) : 13 + (n - 52) % 3; }

__device__ __forceinline__ void attn_compute(LAS char* L, rsrc_t rws, int w, int lane, int head ST_ARG) {
    u32x4 kf[5][2], vf[5][2];
    const unsigned kvb = WS_KV + (head * 64 + w * 16) * 4096;
    auto ld = [&](int t, int s) INL { (s < 2 ? kf : vf)[t % 5][s & 1] = ldh(rws, lane * 16 + s * 1024, kvb + t * 4096); SBAR(); };
    f32x16 o[4], sc[3];
    { float z = 0.f; PIN(z);
#pragma unroll
      for (int k = 0; k < 64; ++k) o[k >> 4][k & 15] = z;
      sc[0] = o[0]; sc[1] = o[0]; sc[2] = o[0];
      PIN(o[0]); PIN(o[1]); PIN(o[2]); PIN(o[3]); PIN(sc[0]); PIN(sc[1]); PIN(sc[2]); }
    __builtin_amdgcn_s_setprio(1);
    lds_bar();
    ST(5);
#pragma unroll
    for (int k = 0; k < 10; ++k) ld(k >> 2, k & 3);
    u32x4 qf[4][2];
#pragma unroll
    for (int k = 0; k < 8; ++k) qf[k >> 1][k & 1] = ldsr128(L + L_QF + k * 1024 + lane * 16);
    mfma0(sc[0], kf[0][0], qf[0][0]);
    sc[0] = mfma(kf[0][1], qf[0][1], sc[0]);
    LAS char* pw = L + L_P + w * 4096 + lane * 16;
    ST(6);
#define EXP4(X, i) do { X[i] = __builtin_amdgcn_exp2f(X[i]); X[i + 1] = __builtin_amdgcn_exp2f(X[i + 1]); X[i + 2] = __builtin_amdgcn_exp2f(X[i + 2]); X[i + 3] = __builtin_amdgcn_exp2f(X[i + 3]); PIN(X); SBAR(); } while (0)
    auto step = [&](auto ic) INL {
        constexpr int n = ic.value, qn = step_q(n + 1), kn = step_k(n + 1) % 5, qp = step_q(n > 0 ? n - 1 : 0), kp = step_k(n > 0 ? n - 1 : 0) % 5;
        f32x16 &sn = sc[(n + 1) % 3], &s = sc[n % 3], &sp = sc[(n + 2) % 3];
        if constexpr (n + 1 < 64) { mfma0(sn, kf[kn][0], qf[qn][0]); SBAR(); }
        if constexpr (n < 64) EXP4(s, 0);
        if constexpr (n + 1 < 64) { sn = mfma(kf[kn][1], qf[qn][1], sn); PIN(sn); SBAR(); }
        if constexpr (n < 64) EXP4(s, 4);
        if constexpr (n > 0) { o[qp] = pv(vf[kp][0], lo8(sp), o[qp]); PIN(o[qp]); SBAR(); }
        if constexpr (n < 64) EXP4(s, 8);
        if constexpr (n > 0) { o[qp] = pv(vf[kp][1], hi8(sp), o[qp]); PIN(o[qp]); SBAR(); }
        constexpr int issued = 10 + n + (n < 8 ? n : 8) < 64 ? 10 + n + (n < 8 ? n : 8) : 64;
        constexpr int nk = 4 * step_k(n + 2 < 64 ? n + 2 : 63) + 1, nv = 4 * step_k(n < 64 ? n : 63) + 3, need = nk > nv ? nk : nv, left = issued - 1 - need;
        if constexpr (n < 55) { __builtin_amdgcn_s_waitcnt(0x0F70 | (left & 15) | ((left >> 4) << 14)); SBAR(); }
        if constexpr (n < 64) { s[12] = __builtin_amdgcn_exp2f(s[12]); PIN(s); SBAR(); }
        if constexpr (issued < 64) ld(issued >> 2, issued & 3);
        if constexpr (n < 8) ld((issued + 1) >> 2, (issued + 1) & 3);
        if constexpr (n < 64) { s[13] = __builtin_amdgcn_exp2f(s[13]); s[14] = __builtin_amdgcn_exp2f(s[14]); s[15] = __builtin_amdgcn_exp2f(s[15]); PIN(s); SBAR(); }
        if constexpr (n == 56 || n == 59 || n == 62 || n == 64) {
            constexpr int qt = n == 64 ? 3 : (n - 56) / 3;
#pragma unroll
            for (int i = 0; i < 4; ++i) *(LAS f32x4*)(pw + qt * 16384 + i * 1024) = (f32x4){o[qt][4 * i], o[qt][4 * i + 1], o[qt][4 * i + 2], o[qt][4 * i + 3]};
            SBAR();
        }
    };
    sfor<65>(step);
    __builtin_amdgcn_s_setprio(0);
}

__device__ __forceinline__ int fc2_kstep(int p, int rot) { return mod7((p % 14) / 2 + rot) * 8 + (p / 14) * 2 + (p & 1); }
__device__ __forceinline__ int fc2_rot(int j) { return (j % 7 + 2 * (j / 7)) % 7; }
__device__ __forceinline__ unsigned w2src(int c, int hw, int i, int j) {
    const int ks = (hw & 1) ? fc2_kstep(4 * c + 2 + (i >> 1), fc2_rot(j)) : fc2_kstep(4 * c + (i >> 1), fc2_rot(j));
    return (((hw >> 1) * 56 + ks) * 16 + (i & 1) * 8) * 896 + (j % 7) * 128;
}
__device__ __forceinline__ void attn_helper(const Params& p, rsrc_t rws, int hw, int lane, int head, int j, unsigned tag, u32x4 (&sg)[9][4]) {
    const rsrc_t rw = mkrs(p.wo, D * D * 2), r1 = mkrs(p.w1, D * F * 2), r2 = mkrs(p.w2, F * D * 2);
    const unsigned vt = lane < 49 ? lane * 128 : 0x7FFFFF00u, land = L_W1 + hw * 1024;
    dma4(r1, vt, (j * 4 + hw) * 49 * 128, land);
    if (hw == 3) { __builtin_amdgcn_s_sleep(22); wait_word(rws, WS_F1 + ((head >> 1) * 32 + (lane & 31)) * 128, tag); }
    bar();
    __builtin_amdgcn_s_sleep(32);
    dma4(r1, vt, (128 + j * 4 + hw) * 49 * 128, land);
    vm_wait<0>();
    const unsigned v2 = slab_voff(lane, 896), v1 = slab_voff(lane, 3584);
    const int ct = j % 7, ct1 = j % 14;
#pragma unroll
    for (int i = 0; i < 14; ++i) {
        int ii = i + 4 * (j / 7); if (ii >= 14) ii -= 14; if (ii >= 14) ii -= 14;
        const int pos = ii * 4 + hw, kh = pos >= 28;
        int pp = pos - 28 * kh + 7 * (ct & 3); if (pp >= 28) pp -= 28;
        dma16(rw, v2, (kh * 28 + pp) * 8 * 896 + ct * 128, L_WO + pos * 1024);
        vm_wait<4>();
    }
#pragma unroll
    for (int k = 0; k < 36; ++k) { sg[k >> 2][k & 3] = __builtin_amdgcn_raw_buffer_load_b128(r2, v2, w2src(5 + (k >> 2), hw, k & 3, j), 0); vm_wait<4>(); }
#pragma unroll
    for (int i = 0; i < 8; ++i) { W1DMA(0, i * 2 + (hw >> 1), hw & 1); vm_wait<4>(); }
}

__device__ __forceinline__ void attn_finish(LAS char* L, rsrc_t rws, int w, int lane, int rb, int head, unsigned tag ST_ARG) {
    const int qt = w >> 1, s = w & 1;
    LAS char* P = L + L_P + qt * 16384 + s * 2048 + lane * 16;
    LAS char* PL = L + L_P + qt * 16384 + 3 * 1024 + (32 + (lane & 31)) * 16;
    unsigned dst = WS_O + (((rb * 4 + qt) * 16 + head) * 2 + s) * 1024 + lane * 16;
    PIN(P); PIN(PL); PIN(dst);
    ST(7);
    wait_bar<0>();
    ST(8);
    f32x4 a[4][2];
    float lp[4];
#pragma unroll
    for (int ww = 0; ww < 4; ++ww) lp[ww] = *(LAS float*)(PL + ww * 4096);
#pragma unroll
    for (int ww = 0; ww < 4; ++ww) { a[ww][0] = *(LAS f32x4*)(P + ww * 4096); a[ww][1] = *(LAS f32x4*)(P + ww * 4096 + 1024); }
    SBAR();
    float l = (lp[0] + lp[1]) + (lp[2] + lp[3]);
    f32x4 x = (a[0][0] + a[1][0]) + (a[2][0] + a[3][0]), y = (a[0][1] + a[1][1]) + (a[2][1] + a[3][1]);
    const float asum = ((fabsf(x[0]) + fabsf(x[1])) + (fabsf(x[2]) + fabsf(x[3]))) + ((fabsf(y[0]) + fabsf(y[1])) + (fabsf(y[2]) + fabsf(y[3])));
    if (__builtin_expect(!__all(l >= 0x1p-40f && l <= 0x1p40f && asum <= 0x1p120f), 0)) {
        const u32x4 qf[2] = {ldsr128(L + L_QF + qt * 2048 + lane * 16), ldsr128(L + L_QF + qt * 2048 + 1024 + lane * 16)};
        const f32x16 o = redo_o(rws, lane, head, qf);
        l = __uint_as_float(__builtin_amdgcn_permlane32_swap(__float_as_uint(o[12]), __float_as_uint(o[12]), false, false)[1]);
#pragma unroll
        for (int k = 0; k < 4; ++k) { x[k] = s ? o[8 + k] : o[k]; y[k] = s ? o[12 + k] : o[4 + k]; }
    }
    const float il = __builtin_amdgcn_rcpf(l);
    u32x4 v = {cvtpk(x[0] * il, x[1] * il), cvtpk(x[2] * il, x[3] * il), cvtpk(y[0] * il, y[1] * il), cvtpk(y[2] * il, y[3] * il)};
    if (s == 1 && lane >= 32) { v[2] = 0u; v[3] = 0u; }
    st128<SC1>(v, rws, dst, 0);
    ST(24);
}

__device__ __forceinline__ unsigned bfsplit(float v, float& rest) { const unsigned h = cvtpk(v, v) & 0xFFFFu; rest = v - bflo(h); return h; }

__device__ __forceinline__ void mlp_helper(const Params& p, LAS char* L, rsrc_t rws, int hw, int lane, int xcd, int j, unsigned tag, const u32x4 (&sg)[9][4]) {
    const int r = lane & 31, hh = lane >> 5, ct1 = j % 14, ct = j % 7;
    const int b4 = xcd * 4 + j / 7, b2 = xcd * 2 + j / 14;
    const rsrc_t r1 = mkrs(p.w1, D * F * 2), r2 = mkrs(p.w2, F * D * 2);
    const unsigned v1 = slab_voff(lane, 3584), v2 = slab_voff(lane, 896);
    unsigned bb[2];
#pragma unroll
    for (int hf = 0; hf < 2; ++hf) bb[hf] = hh ? 0u : ((const unsigned short*)p.b1)[ct1 * 128 + hf * 64 + (hw & 1) * 32 + r];
    auto colsum = [&](int hf) INL {
        float c2[4] = {};
        LAS char* wb = L + L_W1 + hf * SLAB + slab_rd(lane, hw & 1) + (hw >> 1) * 14 * 2048;
        PIN(wb);
#pragma unroll
        for (int u = 0; u < 14; ++u) {
            const u32x4 f = wfrag<1024>(wb + u * 2048);
#pragma unroll
            for (int k = 0; k < 4; ++k) {
                if (hf == 0) c2[k] = dot2(f[k], ONES, c2[k]);
                else { c2[2 * (k & 1)] = sadd(c2[2 * (k & 1)], bflo(f[k])); c2[2 * (k & 1) + 1] = sadd(c2[2 * (k & 1) + 1], bfhi(f[k])); }
            }
        }
        float lo, lo2;
        const unsigned ch = bfsplit((c2[0] + c2[1]) + (c2[2] + c2[3]), lo), cl = bfsplit(lo, lo2);
        *(LAS u32x2*)(L + L_EXT + (2 * hf + (hw & 1)) * 1024 + lane * 16 + (hw >> 1) * 8) = (u32x2){ch | (ch << 16), cl | (bb[hf] << 16)};
    };
    auto w2dma = [&](int c) INL {
#pragma unroll
        for (int i = 0; i < 4; ++i) dma16(r2, v2, w2src(c, hw, i, j), w2slot(c) + hw * 4096 + i * 1024);
    };
    auto w2st = [&](int c) INL {
#pragma unroll
        for (int i = 0; i < 4; ++i) *(LAS u32x4*)(L + w2slot(c) + hw * 4096 + i * 1024 + lane * 16) = sg[c - 5][i];
        SBAR();
    };
    if (hw == 3) { __builtin_amdgcn_s_sleep(8); wait_word(rws, WS_F2 + ((b4 >> 1) * 16 + (lane & 15)) * 128, tag); }
    else if (hw < 2) {
#pragma unroll
        for (int u = 16; u < 28; ++u) W1DMA(0, u, hw);
#pragma unroll
        for (int u = 0; u < 12; ++u) W1DMA(1, u, hw);
    }
    bar();
    bar();
    wait_bar<0>();
    if (hw == 3) set_flag(rws, WS_F3 + (b4 * 8 + ct) * 128, tag, lane == 0);
    if (hw == 2) ((LAS unsigned*)(L + L_SYNC4))[lane] = 0u;
    if (hw < 2) {
#pragma unroll
        for (int u = 12; u < 28; ++u) W1DMA(1, u, hw);
    }
    colsum(0);
    if (hw == 3) { const int l = lane < 14 ? lane : 0; wait_word(rws, WS_F3 + ((b2 * 2 + (l >= 7)) * 8 + mod7(l)) * 128, tag); }
    wait_bar<0>();
    colsum(1);
    lds_bar();
    w2dma(0); w2dma(1); w2dma(2); w2dma(3); w2dma(4);
    bar();
    w2st(5); w2st(6); w2st(7); w2st(8);
    const unsigned f4 = WS_F4 + ((b4 >> 1) * 16 + (lane < 14 ? lane : 0)) * 128;
    if (hw == 3) wait_word(rws, f4, tag);
    wait_bar<0>();
    if (hw == 3) wait_word(rws, f4 + 256 * 128, tag);
    bar();
    w2st(9); w2st(10); w2st(11); w2st(12);
    if (hw == 3) wait_word(rws, f4 + 512 * 128, tag);
    lds_bar();
    w2st(13);
    lds_bar();
    bar();
}

__device__ __forceinline__ void mlp_compute(const Params& p, LAS char* L, rsrc_t rws, int w, int lane, int xcd, int j, unsigned tag ST_ARG) {
    const int r = lane & 31, hh = lane >> 5;
    const int r4 = j / 7, ct = j % 7, m = w & 1, kh = w >> 1;
    const int rt = xcd * 8 + r4 * 2 + m;
    const int sr[2] = {slab_rd(lane, 0), slab_rd(lane, 1)};
    u32x4 hpk[2];
    u32x4 bf[4][2];
    {
        u32x4 wf[16][2];
        LAS char* wo[2][2];
#pragma unroll
        for (int t = 0; t < 2; ++t) { wo[t][0] = L + L_WO + sr[t] + kh * 224 * 128; wo[t][1] = wo[t][0] - hh * 512; PIN(wo[t][0]); PIN(wo[t][1]); }
#pragma unroll
        for (int u = 0; u < 16; ++u)
#pragma unroll
            for (int t = 0; t < 2; ++t) {
                const int row = (u >> 1) * 28 + (u & 1) * 16;
                const u32x2 a = trread(wo[t][0] + row * 128), b = trread(wo[t][u & 1] + (row + 8) * 128);
                wf[u][t] = (u32x4){a[0], a[1], b[0], b[1]};
            }
        wait_bar<0>();
        ST(9);
        const rsrc_t rx = mkrs(p.x, S * D * 2);
        u32x2 xr[4];
#pragma unroll
        for (int i = 0; i < 4; ++i) xr[i] = __builtin_amdgcn_raw_buffer_load_b64(rx, r * 896 + hh * 8 + i * 16, rt * 32 * 896 + ct * 128 + kh * 64, 0);
        bar();
        ST(10);
        u32x4 af[16];
#pragma unroll
        for (int u = 0; u < 16; ++u) af[u] = ldh(rws, lane * 16, WS_O + (rt * 32 + kh * 16 + ((u + 4 * ct) & 15)) * 1024);
        SBAR();
        f32x16 acc[2] = {};
#pragma unroll
        for (int u = 0; u < 16; ++u) { acc[0] = mfma(wf[u][0], af[u], acc[0]); acc[1] = mfma(wf[u][1], af[u], acc[1]); }
        const f32x16 snd = kh ? acc[0] : acc[1];
        f32x16 hv = kh ? acc[1] : acc[0];
#pragma unroll
        for (int i = 0; i < 4; ++i) *(LAS f32x4*)(L + L_RED1 + (w * 4 + i) * 1024 + lane * 16) = (f32x4){snd[4 * i], snd[4 * i + 1], snd[4 * i + 2], snd[4 * i + 3]};
        ST(11);
        lds_bar();
#pragma unroll
        for (int i = 0; i < 4; ++i) {
            const f32x4 o = *(LAS f32x4*)(L + L_RED1 + ((w ^ 2) * 4 + i) * 1024 + lane * 16);
            hv[4 * i] += o[0] + bflo(xr[i][0]); hv[4 * i + 1] += o[1] + bfhi(xr[i][0]);
            hv[4 * i + 2] += o[2] + bflo(xr[i][1]); hv[4 * i + 3] += o[3] + bfhi(xr[i][1]);
        }
#pragma unroll
        for (int s = 0; s < 2; ++s) { hpk[s] = pack8(hv, s); st128<SC1>(hpk[s], rws, lane * 16, WS_H + (rt * 28 + ct * 4 + kh * 2 + s) * 1024); }
        float s1 = 0.f, s2 = 0.f;
#pragma unroll
        for (int k = 0; k < 8; ++k) { const unsigned u = hpk[k >> 2][k & 3]; s1 = dot2(u, ONES, s1); s2 = dot2(u, u, s2); }
        __builtin_amdgcn_raw_buffer_store_b64((u32x2){__float_as_uint(halfsum(s1)), __float_as_uint(halfsum(s2))}, rws,
                                              hh ? 0x7FFFFFF0u : WS_HS + ((rt * 14 + ct * 2 + kh) * 32 + r) * 8, 0, SC1);
        wait_bar<0>();
        ST(12);
    }
    {
        const int ct1 = j % 14, rt1 = xcd * 8 + (j / 14) * 4 + w;
        auto RD = [&](int u) INL {
#pragma unroll
            for (int t = 0; t < 2; ++t) bf[u & 3][t] = wfrag<1024>(L + L_W1 + sr[t] + u * 2048);
            SBAR();
        };
        RD(0); RD(1); RD(2);
        bar();
        ST(13);
        u32x4 af[28];
        auto aload = [&](int u) INL { int uu = u + 2 * ct1; if (uu >= 28) uu -= 28; af[u] = ldh(rws, lane * 16, WS_H + (rt1 * 28 + uu) * 1024); };
        constexpr int WIN = 10;
#pragma unroll
        for (int u = 0; u < WIN; ++u) aload(u);
        SBAR();
        f32x16 acc[4] = {};
        u32x2 st[7];
        u32x4 b2f[4];
        LAS char* w1b[2] = {L + L_W1 + SLAB + sr[0], L + L_W1 + SLAB + sr[1]};
        PIN(w1b[0]); PIN(w1b[1]);
        auto RD2 = [&](int z) INL { b2f[z & 3] = wfrag<1024>(w1b[z / 28] + (z % 28) * 2048); SBAR(); };
        sfor<28>([&](auto ic) INL {
            constexpr int u = ic.value;
            if constexpr (u + 3 < 28) RD(u + 3); else RD2(u + 3 - 28);
            acc[0] = mfma(bf[u & 3][0], af[u], acc[0]); acc[1] = mfma(bf[u & 3][1], af[u], acc[1]);
            PIN(acc[0]); PIN(acc[1]); SBAR();
            if constexpr (u + WIN < 28) aload(u + WIN);
            else if constexpr (u + WIN < 35) st[u + WIN - 28] = __builtin_amdgcn_raw_buffer_load_b64(rws, (hh * 7 * 32 + r) * 8, WS_HS + (rt1 * 14 + u + WIN - 28) * 256, SC1);
            SBAR();
        });
        ST(14);
        bar();
        u32x4 ext[4];
#pragma unroll
        for (int t = 0; t < 4; ++t) ext[t] = ldsr128(L + L_EXT + t * 1024 + lane * 16);
        float s1 = 0.f, s2 = 0.f;
#pragma unroll
        for (int k = 0; k < 7; ++k) { s1 += __uint_as_float(st[k][0]); s2 += __uint_as_float(st[k][1]); }
        const float mu = halfsum(s1) * (1.0f / D);
        const float var = fmaxf(halfsum(s2) * (1.0f / D) - mu * mu, 0.f);
        const float r1 = __builtin_amdgcn_rsqf(var + EPS);
        const float rs = r1 * __builtin_amdgcn_rsqf(var * r1 * r1 + EPS), irs = __builtin_amdgcn_rcpf(rs);
        float t0, t1;
        const unsigned mh = bfsplit(-mu, t0), ml = bfsplit(t0, t1), ih = bfsplit(irs, t0), il = bfsplit(t0, t1);
        const u32x4 xe = {mh | (ml << 16), mh | (ih << 16), mh | (ml << 16), mh | (il << 16)};
        acc[0] = mfma(ext[0], xe, acc[0]); acc[1] = mfma(ext[1], xe, acc[1]);
        acc[2] = mfma(ext[2], xe, acc[2]); acc[3] = mfma(ext[3], xe, acc[3]);
        PIN(acc[0]); PIN(acc[1]); PIN(acc[2]); PIN(acc[3]); SBAR();
        constexpr float C0 = -2.0f * 0.7978845608028654f * 1.4426950408889634f, C1 = C0 * 0.044715f;
        const float k0 = C0 * rs, k1 = C1 * rs * rs * rs;
        f32x4 ga;
        auto gelu = [&](f32x16& v, int i, int stage) INL {
#pragma unroll
            for (int k = 0; k < 4; ++k) {
                const float a = v[4 * i + k];
                if (stage == 0) ga[k] = smul(a, sfma(smul(a, a), k1, k0));
                if (stage == 1) ga[k] = __builtin_amdgcn_exp2f(ga[k]);
                if (stage == 2) ga[k] = __builtin_amdgcn_rcpf(sfma(ga[k], irs, irs));
                if (stage == 3) v[4 * i + k] = smul(a, ga[k]);
            }
            if (stage < 3) PIN(ga); else PIN(v);
            SBAR();
        };
        auto put = [&](int t) INL {
#pragma unroll
            for (int s = 0; s < 2; ++s) st128<SC1>(pack8(acc[t], s), rws, lane * 16, WS_G + (rt1 * 112 + ct1 * 8 + t * 2 + s) * 1024);
        };
        auto flag = [&](int stage) INL { publish(L + L_SYNC4 + stage * 4, rws, WS_F4 + (stage * 256 + (xcd * 2 + j / 14) * 16 + ct1) * 128, tag, lane); };
        sfor<56>([&](auto ic) INL {
            constexpr int z = ic.value, t = 2 + z / 28;
            if constexpr (z + 3 < 56) RD2(z + 3);
            acc[t] = mfma(b2f[z & 3], af[z % 28], acc[t]); PIN(acc[t]); SBAR();
            constexpr int e = z - 4;
            if constexpr (e >= 0 && e < 48) { gelu(acc[e >> 4], (e >> 2) & 3, e & 3); if constexpr ((e & 15) == 15) { put(e >> 4); SBAR(); } }
            if constexpr (z == 47) { vm_wait<0>(); flag(0); SBAR(); }
        });
        ST(15);
        bar();
#pragma unroll
        for (int k = 0; k < 16; ++k) {
            const float a = acc[3][k];
            acc[3][k] = a * __builtin_amdgcn_rcpf(fmaf(__builtin_amdgcn_exp2f(a * fmaf(a * a, k1, k0)), irs, irs));
        }
        put(3);
        vm_wait<2>(); flag(1);
        ST(16);
        bar();
        ST(17);
        constexpr int WN2 = 10;
        u32x4 ag[WN2];
        const int rot2 = fc2_rot(j);
        const unsigned abase = WS_G + (rt * 112 + kh * 56) * 1024;
#pragma unroll
        for (int u = 0; u < WN2; ++u) ag[u] = ldh(rws, lane * 16, abase + fc2_kstep(u, rot2) * 1024);
        SBAR();
        vm_wait<WN2>(); flag(2);
        LAS char* w2b[3][2];
#pragma unroll
        for (int k = 0; k < 6; ++k) { w2b[k >> 1][k & 1] = L + sr[k & 1] + kh * 8192 + (k >> 1) * 48 * K1; PIN(w2b[k >> 1][k & 1]); }
        auto RD3 = [&](int u) INL {
#pragma unroll
            for (int t = 0; t < 2; ++t) bf[u & 3][t] = wfrag<1024>(w2b[w2slot(u >> 2) / (48 * K1)][t] + w2slot(u >> 2) % (48 * K1) + (u & 3) * 2048);
            SBAR();
        };
        RD3(0); RD3(1); RD3(2);
        ST(18);
        f32x16 a2[2] = {};
        u32x2 bb[4];
        sfor<56>([&](auto ic) INL {
            constexpr int u = ic.value;
            if constexpr (u == 16 || u == 32 || u == 44) bar();
            if constexpr (u == 16) ST(19);
            if constexpr (u + 3 < 56) RD3(u + 3);
            a2[0] = mfma(bf[u & 3][0], ag[u % WN2], a2[0]); a2[1] = mfma(bf[u & 3][1], ag[u % WN2], a2[1]);
            PIN(a2[0]); PIN(a2[1]); SBAR();
            if constexpr (u + WN2 < 56) ag[u % WN2] = ldh(rws, lane * 16, abase + fc2_kstep(u + WN2, rot2) * 1024);
            else if constexpr (u + WN2 < 60) bb[u + WN2 - 56] = __builtin_amdgcn_raw_buffer_load_b64(mkrs(p.b2, D * 2), hh * 8 + (u + WN2 - 56) * 16, ct * 128 + kh * 64, 0);
            SBAR();
        });
        ST(20);
        const f32x16 snd = kh ? a2[0] : a2[1];
        f32x16 v = kh ? a2[1] : a2[0];
#pragma unroll
        for (int i = 0; i < 4; ++i) *(LAS f32x4*)(L + L_RED2 + (w * 4 + i) * 1024 + lane * 16) = (f32x4){snd[4 * i], snd[4 * i + 1], snd[4 * i + 2], snd[4 * i + 3]};
        lds_bar();
        unsigned o[8];
#pragma unroll
        for (int i = 0; i < 4; ++i) {
            const f32x4 t = *(LAS f32x4*)(L + L_RED2 + ((w ^ 2) * 4 + i) * 1024 + lane * 16);
            const unsigned h0 = hpk[i >> 1][2 * (i & 1)], h1 = hpk[i >> 1][2 * (i & 1) + 1];
            o[2 * i] = cvtpk(v[4 * i] + t[0] + bflo(bb[i][0]) + bflo(h0), v[4 * i + 1] + t[1] + bfhi(bb[i][0]) + bfhi(h0));
            o[2 * i + 1] = cvtpk(v[4 * i + 2] + t[2] + bflo(bb[i][1]) + bflo(h1), v[4 * i + 3] + t[3] + bfhi(bb[i][1]) + bfhi(h1));
        }
        const rsrc_t ro = mkrs(p.out, S * D * 2);
#pragma unroll
        for (int k = 0; k < 2; ++k) {
            auto s0 = __builtin_amdgcn_permlane32_swap(o[4 * k], o[4 * k + 2], false, false);
            auto s1_ = __builtin_amdgcn_permlane32_swap(o[4 * k + 1], o[4 * k + 3], false, false);
            st128<0>((u32x4){s0[0], s1_[0], s0[1], s1_[1]}, ro, r * 896 + hh * 16 + k * 32, rt * 32 * 896 + ct * 128 + kh * 64);
        }
        ST(21);
    }
}

__global__ void __launch_bounds__(NT, 2) k_block(Params p) {
    extern __shared__ __attribute__((aligned(16))) char smem[];
    LAS char* L = (LAS char*)smem;
    ST_DECL
    ST(0);
    const int lane = threadIdx.x & 63, w = __builtin_amdgcn_readfirstlane(threadIdx.x >> 6);
    const int b = blockIdx.x, xcd = b & 7, j = b >> 3;
    const int rb = (xcd & 1) * 8 + (j & 7), sub = j >> 3, head = 4 * (xcd >> 1) + sub, pair = 2 * (xcd >> 1) + (sub >> 1), kind = sub & 1;
    const rsrc_t rws = mkrs(p.ws, WS_END);
    const int rot = ((j & 7) + 2 * sub) % 7;
    if (w == 7) {
        unsigned long long pc;
        asm volatile("s_getpc_b64 %0" : "=s"(pc));
        dma4(mkrs((const void*)pc, CODE_LEN), (lane * 8 + (j & 7)) * 128, 0, P1_EX + 1536);
    }
    unsigned tag;
    if (w < 4) {
        tag = load_tag(p);
        if (kind) p1_compute<true>(L, rws, w, lane, rb, pair, head, tag ST_PASS);
        else p1_compute<false>(L, rws, w, lane, rb, pair, head, tag ST_PASS);
        ST(4);
        attn_compute(L, rws, w, lane, head ST_PASS);
        attn_finish(L, rws, w, lane, rb, head, tag ST_PASS);
        if (j < 28) mlp_compute(p, L, rws, w, lane, xcd, j, tag ST_PASS);
        else wait_bar<0>();
    } else {
        if (w < 6) p1_loader(p, w - 4, lane, rb, pair, kind, head, rot, tag);
        else { tag = load_tag(p); p1_colsum(L, w - 6, lane); }
        ST(4);
        u32x4 sg[9][4];
        attn_helper(p, rws, w - 4, lane, head, j, tag, sg);
        attn_finish(L, rws, w, lane, rb, head, tag ST_PASS);
        wait_bar<0>();
        if (w == 7) set_flag(rws, WS_F2 + (rb * 16 + head) * 128, tag, lane == 0);
        ST(9);
        if (j < 28) mlp_helper(p, L, rws, w - 4, lane, xcd, j, tag, sg);
    }
    if (b == 0 && w == 0) set_flag(rws, WS_GEN, tag, lane == 0);
    ST_FLUSH();
}

static void dump_stamps(void* ws, const char* tag);
extern "C" void kernel_launch(void* const* d_in, const int* in_sizes, int n_in, void* d_out, int out_size, void* d_ws, size_t ws_size, hipStream_t stream) {
    Params p;
    p.x = d_in[0]; p.wqkv = d_in[1]; p.wo = d_in[2]; p.w1 = d_in[4]; p.b1 = d_in[5]; p.w2 = d_in[6]; p.b2 = d_in[7];
    p.out = d_out; p.ws = d_ws;
    (void)hipFuncSetAttribute((const void*)k_block, hipFuncAttributeMaxDynamicSharedMemorySize, LDS_BYTES);
    k_block<<<NWG, NT, LDS_BYTES, stream>>>(p);
    hipStreamCaptureStatus cs; (void)hipStreamIsCapturing(stream, &cs);
    if (cs == hipStreamCaptureStatusNone) { (void)hipStreamSynchronize(stream); dump_stamps(d_ws, "harness first launch");
        fprintf(stderr, "ptrs x %p wqkv %p wo %p w1 %p b1 %p w2 %p b2 %p out %p ws %p\n", p.x, p.wqkv, p.wo, p.w1, p.b1, p.w2, p.b2, p.out, p.ws); }
}

#include <vector>
#include <algorithm>
#include <cstdio>
static void dump_stamps(void* ws, const char* tag) {
    std::vector<unsigned> h(64 * 256 * 8);
    (void)hipMemcpy(h.data(), (char*)ws + WS_ST, h.size() * 4, hipMemcpyDeviceToHost);
    unsigned t0 = ~0u; for (int b = 0; b < 256; ++b) t0 = std::min(t0, h[b * 8]);
    fprintf(stderr, "stamps[%s] (us since first WG start; min / median / max over WGs) for wave 0 | wave 3 | wave 4 | wave 7;  WG0 wave0 cycles since its start, clock since prev\n", tag);
    int prev = -1;
    for (int id = 0; id < 32; ++id) { if (!h[id * 256 * 8] && !h[id * 256 * 8 + 4]) continue; fprintf(stderr, "  id %2d:", id);
        for (int w : {0, 3, 4, 7}) {
            std::vector<double> v; for (int b = 0; b < 256; ++b) { unsigned t = h[(id * 256 + b) * 8 + w]; if (t) v.push_back((t - t0) / 100.0); }
            if (v.size() < 100) { fprintf(stderr, "   -      -      -    |"); continue; }
            std::sort(v.begin(), v.end()); fprintf(stderr, " %6.2f %6.2f %6.2f |", v[0], v[v.size() / 2], v.back()); }
        fprintf(stderr, " cyc %6d", (int)(h[((id + 32) * 256) * 8] - h[(32 * 256) * 8]));
        if (prev >= 0) fprintf(stderr, "  clk %.2f", (double)(h[((id + 32) * 256) * 8] - h[((prev + 32) * 256) * 8]) / ((double)(h[(id * 256) * 8] - h[(prev * 256) * 8]) * 10.0));
        fprintf(stderr, "\n"); prev = id; }
}
```

```cpp
#include <hip/hip_runtime.h>
#include <stdint.h>
#include <utility>

#define LAS __attribute__((address_space(3)))
#define GAS __attribute__((address_space(1)))
typedef __bf16 bf2 __attribute__((ext_vector_type(2)));
typedef __bf16 bf8 __attribute__((ext_vector_type(8)));
typedef __bf16 bf16v __attribute__((ext_vector_type(16)));
typedef float f32x4 __attribute__((ext_vector_type(4)));
typedef float f32x8 __attribute__((ext_vector_type(8)));
typedef float f32x16 __attribute__((ext_vector_type(16)));
typedef unsigned u32x2 __attribute__((ext_vector_type(2)));
typedef unsigned u32x4 __attribute__((ext_vector_type(4)));
typedef short s16x4 __attribute__((ext_vector_type(4)));
typedef __amdgpu_buffer_rsrc_t rsrc_t;

constexpr int S = 2048, D = 448, H = 16, F = 1792;
constexpr int NT = 512, NWG = 256;
constexpr float EPS = 1e-5f;
constexpr int LDS_BYTES = 160 * 1024;
constexpr int SC1 = 16;
constexpr unsigned CODE_LEN = 52 * 1024;
constexpr unsigned WS_GEN = 0;
constexpr unsigned WS_F1 = 4096;
constexpr unsigned WS_F2 = WS_F1 + 256 * 128;
constexpr unsigned WS_F3 = WS_F2 + 256 * 128;
constexpr unsigned WS_F4 = WS_F3 + 256 * 128;
constexpr unsigned WS_HS = WS_F4 + 768 * 128;
constexpr unsigned WS_KV = 512 * 1024;
constexpr unsigned WS_O = WS_KV + 16 * 64 * 4096;
constexpr unsigned WS_H = WS_O + 64 * 32 * 1024;
constexpr unsigned WS_G = WS_H + 64 * 28 * 1024;
constexpr unsigned WS_END = WS_G + 64 * 112 * 1024;

struct Params {
    const void *x, *wqkv, *wo, *w1, *b1, *w2, *b2;
    void *out, *ws;
};

template <class Fn, int... I> __device__ __forceinline__ void sfor_(Fn&& f, std::integer_sequence<int, I...>) { (f(std::integral_constant<int, I>{}), ...); }
template <int N, class Fn> __device__ __forceinline__ void sfor(Fn&& f) { sfor_(f, std::make_integer_sequence<int, N>{}); }
#define INL __attribute__((always_inline))
__device__ __forceinline__ rsrc_t mkrs(const void* p, unsigned bytes) { return __builtin_amdgcn_make_buffer_rsrc((void*)p, 0, (int)bytes, 0x00020000); }
__device__ __forceinline__ void dma16(rsrc_t rs, unsigned voff, unsigned soff, unsigned lds) {
    asm volatile("s_mov_b32 m0, %0\n\ts_nop 0\n\tbuffer_load_dwordx4 %1, %2, %3 offen lds" ::"s"(lds), "v"(voff), "s"(rs), "s"(soff) : "memory");
}
__device__ __forceinline__ void dma4(rsrc_t rs, unsigned voff, unsigned soff, unsigned lds) {
    asm volatile("s_mov_b32 m0, %0\n\ts_nop 0\n\tbuffer_load_dword %1, %2, %3 offen lds" ::"s"(lds), "v"(voff), "s"(rs), "s"(soff) : "memory");
}
__device__ __forceinline__ void dma4_if0(int skip, rsrc_t rs, unsigned voff, unsigned soff, unsigned lds) {
    asm volatile("s_cmp_lg_u32 %4, 0\n\ts_cbranch_scc1 1f\n\ts_mov_b32 m0, %0\n\ts_nop 0\n\tbuffer_load_dword %1, %2, %3 offen lds\n1:" ::"s"(lds), "v"(voff), "s"(rs), "s"(soff), "s"(skip) : "memory", "scc");
}
template <int N> __device__ __forceinline__ void wait_bar() { asm volatile("s_waitcnt vmcnt(%0) lgkmcnt(0)\n\ts_barrier" ::"i"(N) : "memory"); }
template <int N> __device__ __forceinline__ void vm_wait() { asm volatile("s_waitcnt vmcnt(%0)" ::"i"(N) : "memory"); }
__device__ __forceinline__ void lds_bar() { asm volatile("s_waitcnt lgkmcnt(0)\n\ts_barrier" ::: "memory"); }
__device__ __forceinline__ void bar() { asm volatile("s_barrier" ::: "memory"); }
__device__ __forceinline__ void lds_wait() { asm volatile("s_waitcnt lgkmcnt(0)" ::: "memory"); }
__device__ __forceinline__ f32x16 mfma(u32x4 a, u32x4 b, f32x16 c) {
    return __builtin_amdgcn_mfma_f32_32x32x16_bf16(__builtin_bit_cast(bf8, a), __builtin_bit_cast(bf8, b), c, 0, 0, 0);
}
__device__ __forceinline__ float dot2(unsigned a, unsigned b, float c) {
    return __builtin_amdgcn_fdot2_f32_bf16(__builtin_bit_cast(bf2, a), __builtin_bit_cast(bf2, b), c, false);
}
__device__ __forceinline__ unsigned cvtpk(float lo, float hi) {
    typedef float f2 __attribute__((ext_vector_type(2)));
    return __builtin_bit_cast(unsigned, __builtin_convertvector((f2){lo, hi}, bf2));
}
__device__ __forceinline__ float halfsum(float v) {
    auto r = __builtin_amdgcn_permlane32_swap(__float_as_uint(v), __float_as_uint(v), false, false);
    return __uint_as_float(r[0]) + __uint_as_float(r[1]);
}
template <int AUX> __device__ __forceinline__ void st128(u32x4 v, rsrc_t rs, unsigned voff, unsigned soff) { __builtin_amdgcn_raw_buffer_store_b128(v, rs, voff + soff, 0, AUX); }
__device__ __forceinline__ u32x4 ldh(rsrc_t rs, unsigned voff, unsigned soff) { return __builtin_amdgcn_raw_buffer_load_b128(rs, voff, soff, SC1); }
__device__ __forceinline__ u32x4 ldsr128(LAS char* p) { return *(LAS u32x4*)p; }
__device__ __forceinline__ u32x2 trread(LAS char* p) {
    return __builtin_bit_cast(u32x2, __builtin_amdgcn_ds_read_tr16_b64_v4i16((LAS s16x4*)p));
}
template <int ESTEP> __device__ __forceinline__ u32x4 wfrag(LAS char* p) {
    const u32x2 a = trread(p), b = trread(p + ESTEP);
    return (u32x4){a[0], a[1], b[0], b[1]};
}
__device__ __forceinline__ u32x4 pack8(const f32x16& v, int s) {
    return (u32x4){cvtpk(v[8 * s], v[8 * s + 1]), cvtpk(v[8 * s + 2], v[8 * s + 3]), cvtpk(v[8 * s + 4], v[8 * s + 5]), cvtpk(v[8 * s + 6], v[8 * s + 7])};
}
__device__ __forceinline__ float smul(float a, float b) { float r; asm("v_mul_f32 %0, %1, %2" : "=v"(r) : "v"(a), "v"(b)); return r; }
__device__ __forceinline__ float sadd(float a, float b) { float r; asm("v_add_f32 %0, %1, %2" : "=v"(r) : "v"(a), "v"(b)); return r; }
__device__ __forceinline__ float sfma(float a, float b, float c) { float r; asm("v_fma_f32 %0, %1, %2, %3" : "=v"(r) : "v"(a), "v"(b), "v"(c)); return r; }
__device__ __forceinline__ float bflo(unsigned u) { return __uint_as_float(u << 16); }
__device__ __forceinline__ float bfhi(unsigned u) { return __uint_as_float(u & 0xFFFF0000u); }
constexpr unsigned ONES = 0x3F803F80u;
#define PIN(x) asm volatile("" : "+v"(x))
#define SBAR() __builtin_amdgcn_sched_barrier(0)
#define PADWAIT() do { while (__builtin_amdgcn_s_memrealtime() - t_start < 5000ull) __builtin_amdgcn_s_sleep(1); } while (0)
#define ST(id) do { if ((id) == 99) PADWAIT(); } while (0)
#define ST_DECL const unsigned long long t_start = __builtin_amdgcn_s_memrealtime();
#define ST_ARG , unsigned long long t_start
#define ST_PASS , t_start
#define ST_FLUSH() ST(99)

constexpr int K1 = 1024;
constexpr int P1_XS = 0, P1_WS = 64 * K1, P1_WQ = 120 * K1, P1_EX = 148 * K1;
constexpr int L_SYNC1 = 150 * K1, L_SYNC4 = 144 * K1;
constexpr int L_P = 32 * K1, L_WO = 96 * K1, L_QF = 152 * K1;
constexpr int L_W1 = 0, SLAB = 56 * K1, L_RED1 = 80 * K1, L_EXT = 156 * K1, L_RED2 = 144 * K1;
__host__ __device__ constexpr int w2slot(int c) { return ((c % 9) < 2 ? 112 + 16 * (c % 9) : 16 * ((c % 9) - 2)) * K1; }

__device__ __forceinline__ void set_flag(rsrc_t rws, unsigned off, unsigned tag, bool me) {
    __builtin_amdgcn_raw_buffer_store_b32(tag, rws, me ? off : 0x7FFFFFF0u, 0, SC1);
}
__device__ __forceinline__ void publish(LAS char* cnt, rsrc_t rws, unsigned off, unsigned tag, int lane) {
    unsigned old = 0;
    if (lane == 0) old = __hip_atomic_fetch_add((LAS unsigned*)cnt, 1u, __ATOMIC_RELAXED, __HIP_MEMORY_SCOPE_WORKGROUP);
    set_flag(rws, off, tag, lane == 0 && old == 3u);
}
__device__ __forceinline__ void wait_word(rsrc_t rws, unsigned off, unsigned tag) {
#pragma unroll 1
    for (unsigned spins = 0; spins < (1u << 20); ++spins) {
        asm volatile("" ::: "memory");
        if (__all(__builtin_amdgcn_raw_buffer_load_b32(rws, off, 0, SC1) == tag)) break;
        __builtin_amdgcn_s_sleep(2);
    }
    asm volatile("" ::: "memory");
}
__device__ __forceinline__ unsigned load_tag(const Params& p) {
    unsigned g;
    asm volatile("s_load_dword %0, %1, 0x0\n\ts_waitcnt lgkmcnt(0)" : "=s"(g) : "s"(p.ws) : "memory");
    return g + 1;
}
__device__ __forceinline__ int mod7(int v) { return v >= 7 ? v - 7 : v; }

__device__ __forceinline__ int p1_wrd(int lane, int t) {
    const int q = (lane & 15) >> 2, pp = lane & 3, g1 = (lane >> 4) & 1, hh = lane >> 5;
    return P1_WS + (8 * hh + q) * 128 + (((7 * t + 4 * g1 + pp) ^ ((q >> 1) << 3)) << 3);
}
template <bool VT>
__device__ __forceinline__ void p1_compute(LAS char* L, rsrc_t rws, int w, int lane, int rb, int pair, int head, unsigned tag ST_ARG) {
    const int r = lane & 31, hh = lane >> 5;
    LAS char* xa[4];
#pragma unroll
    for (int uu = 0; uu < 4; ++uu) xa[uu] = L + P1_XS + w * 4096 + r * 128 + (((2 * uu + hh) ^ ((r >> 1) & 7)) << 4);
    LAS char* wb[2] = {L + p1_wrd(lane, 0), L + p1_wrd(lane, 1)};
    PIN(wb[0]); PIN(wb[1]);
    u32x4 ones = {ONES, ONES, ONES, ONES};
    PIN(ones);
    u32x4 xf[28];
    f32x16 acc[2] = {};
    float sx[2] = {}, sxx[2] = {};
    u32x4 bf[8][2];
    auto RD = [&](int u) INL {
        xf[u] = ldsr128(xa[u & 3] + ((u >> 2) & 3) * 16384);
        bf[u & 7][0] = wfrag<512>(wb[0] + u * 2048);
        bf[u & 7][1] = wfrag<512>(wb[1] + u * 2048);
        SBAR();
    };
    auto MM = [&](int u) INL {
        acc[0] = VT ? mfma(xf[u], bf[u & 7][0], acc[0]) : mfma(bf[u & 7][0], xf[u], acc[0]);
        acc[1] = VT ? mfma(xf[u], bf[u & 7][1], acc[1]) : mfma(bf[u & 7][1], xf[u], acc[1]);
        PIN(acc[0]); PIN(acc[1]); SBAR();
    };
    auto SQ = [&](int c) INL {
#pragma unroll
        for (int k = 0; k < 16; ++k) { const unsigned a = xf[4 * c + (k >> 2)][k & 3]; sx[k & 1] = dot2(a, ONES, sx[k & 1]); sxx[k & 1] = dot2(a, a, sxx[k & 1]); }
        PIN(sx[0]); PIN(sx[1]); PIN(sxx[0]); PIN(sxx[1]); SBAR();
    };
    sfor<7>([&](auto ic) INL {
        constexpr int c = ic.value;
        lds_bar();
        if (c == 0) ST(1);
#pragma unroll
        for (int i = 0; i < 4; ++i) { if (c > 0 && c < 6) MM(4 * c - 4 + i); RD(4 * c + i); }
        SQ(c);
        if constexpr (c >= 5) { MM(4 * c); MM(4 * c + 1); MM(4 * c + 2); MM(4 * c + 3); }
    });
    ST(2);
    const float mu = halfsum(sx[0] + sx[1]) * (1.0f / D);
    const float var = fmaxf(halfsum(sxx[0] + sxx[1]) * (1.0f / D) - mu * mu, 0.f);
    const float rstd = __builtin_amdgcn_rsqf(var + EPS);
    LAS float* ex = (LAS float*)(L + P1_EX + w * 256);
    LAS float* cs = (LAS float*)(L + P1_EX + 1024);
    const int tile = rb * 4 + w;
    if (VT) ex[lane] = hh ? rstd : -mu * rstd;
    lds_bar();
    if (VT) {
        f32x4 a4[4], b4[4];
#pragma unroll
        for (int i = 0; i < 4; ++i) { a4[i] = *(LAS f32x4*)(ex + 8 * i + 4 * hh); b4[i] = *(LAS f32x4*)(ex + 32 + 8 * i + 4 * hh); }
#pragma unroll
        for (int t = 0; t < 2; ++t) {
            const float c = cs[32 * t + r];
            f32x16 v;
#pragma unroll
            for (int k = 0; k < 16; ++k) v[k] = fmaf(acc[t][k], b4[k >> 2][k & 3], a4[k >> 2][k & 3] * c);
#pragma unroll
            for (int s = 0; s < 2; ++s) {
                u32x4 o = pack8(v, s);
                if (r >= 28) { const unsigned z = r == 28 ? ONES : 0u; o = (u32x4){z, z, z, z}; }
                st128<SC1>(o, rws, lane * 16, WS_KV + ((2 * pair + t) * 64 + tile) * 4096 + (2 + s) * 1024);
            }
        }
    } else {
        const int rp = (r & 19) | ((r & 4) << 1) | ((r & 8) >> 1);
        const float nm = -mu * rstd;
#pragma unroll
        for (int t = 0; t < 2; ++t) {
            f32x16 v;
#pragma unroll
            for (int i = 0; i < 4; ++i) {
                const f32x4 c4 = *(LAS f32x4*)(cs + 32 * t + 8 * i + 4 * hh);
#pragma unroll
                for (int k = 0; k < 4; ++k) v[4 * i + k] = fmaf(acc[t][4 * i + k], rstd, nm * c4[k]);
            }
#pragma unroll
            for (int s = 0; s < 2; ++s) {
                u32x4 o = pack8(v, s);
                if (s == 1 && hh) { o[2] = 0u; o[3] = 0u; }
                st128<SC1>(o, rws, (rp + 32 * hh) * 16, WS_KV + ((2 * pair + t) * 64 + tile) * 4096 + s * 1024);
            }
        }
    }
    ST(3);
    lds_bar();
    LAS char* wq = L + P1_WQ + (8 * hh + ((lane & 15) >> 2)) * 64 + ((head & 1) + 4 * ((lane >> 4) & 1) + (lane & 3)) * 8;
    PIN(wq);
    f32x16 aq = {}, cq = {};
    u32x4 bq[4];
    bq[0] = wfrag<256>(wq); bq[1] = wfrag<256>(wq + 1024); bq[2] = wfrag<256>(wq + 2048);
    sfor<28>([&](auto ic) INL {
        constexpr int u = ic.value;
        if constexpr (u + 3 < 28) bq[(u + 3) & 3] = wfrag<256>(wq + (u + 3) * 1024);
        SBAR();
        aq = mfma(bq[u & 3], xf[u], aq);
        cq = mfma(bq[u & 3], ones, cq);
        PIN(aq); PIN(cq); SBAR();
        if constexpr (u == 11) { vm_wait<0>(); publish(L + L_SYNC1, rws, WS_F1 + ((pair * 16 + rb) * 2 + (VT ? 1 : 0)) * 128, tag, lane); SBAR(); }
    });
    const float qs = rstd * (0.18898223650461362f * 1.4426950408889634f), nmq = -mu * qs;
    f32x16 v;
#pragma unroll
    for (int k = 0; k < 16; ++k) v[k] = fmaf(aq[k], qs, nmq * cq[k]);
#pragma unroll
    for (int s = 0; s < 2; ++s) {
        u32x4 o = pack8(v, s);
        if (s == 1 && hh) { o[2] = 0u; o[3] = 0u; }
        *(LAS u32x4*)(L + L_QF + (w * 2 + s) * 1024 + lane * 16) = o;
    }
}

__device__ __forceinline__ void p1_loader(const Params& p, int lw, int lane, int rb, int pair, int kind, int head, int rot, unsigned& tag) {
    const rsrc_t rx = mkrs(p.x, S * D * 2), rw = mkrs(p.wqkv, D * 3 * D * 2);
    unsigned vx[2];
#pragma unroll
    for (int i = 0; i < 2; ++i) vx[i] = (lane >> 3) * 896 + (((lane & 7) ^ ((4 * i + (lane >> 4)) & 7)) << 4);
    int piece = (lane & 7) ^ (((lane >> 4) & 1) << 2);
    if (piece == 7) piece = 6;
    const unsigned vw = (lane >> 3) * 2688 + piece * 16;
    const unsigned xb = (rb * 128 + lw * 64) * 896, wb = lw * 32 * 2688 + (448 * (1 + kind) + 56 * pair) * 2;
    auto chunk = [&](int c) INL {
        const int cc = mod7(c + rot);
#pragma unroll
        for (int i = 0; i < 4; ++i) dma16(rw, vw, wb + (cc * 64 + i * 8) * 2688, P1_WS + (c * 64 + lw * 32 + i * 8) * 128);
#pragma unroll
        for (int i = 0; i < 8; ++i) dma16(rx, vx[i & 1], xb + i * 8 * 896 + cc * 128, P1_XS + (c & 3) * 16384 + lw * 8192 + i * 1024);
    };
    const unsigned vq = (lane >> 2) * 2688 + (lane & 3) * 16;
    const unsigned qb = 56 * head - 8 * (head & 1) + lw * 32 * 2688;
    auto wq = [&](int c) INL {
#pragma unroll
        for (int i = 0; i < 2; ++i) dma16(rw, vq, (mod7(c + rot) * 64 + i * 16) * 2688 + qb, P1_WQ + (c * 64 + lw * 32 + i * 16) * 64);
    };
    chunk(0); chunk(1);
#pragma unroll
    for (int c = 0; c < 5; ++c) { chunk(c + 2); wait_bar<24>(); }
    wq(0); wq(1); wq(2);
    wait_bar<18>();
    wq(3); wq(4);
    wait_bar<10>();
    wq(5); wq(6);
    tag = load_tag(p);
    bar();
    wait_bar<0>();
}

__device__ __forceinline__ void p1_colsum(LAS char* L, int t, int lane) {
    LAS char* wb = L + p1_wrd(lane, t);
    PIN(wb);
    ((LAS unsigned*)(L + L_SYNC1))[t] = 0u;
    float c2[4] = {};
#pragma unroll
    for (int c = 0; c < 7; ++c) {
        lds_bar();
#pragma unroll
        for (int uu = 0; uu < 4; ++uu) {
            const u32x4 b = wfrag<512>(wb + (4 * c + uu) * 2048);
#pragma unroll
            for (int j = 0; j < 4; ++j) { c2[2 * (j & 1)] = sadd(c2[2 * (j & 1)], bflo(b[j])); c2[2 * (j & 1) + 1] = sadd(c2[2 * (j & 1) + 1], bfhi(b[j])); }
        }
        PIN(c2[0]); PIN(c2[1]); PIN(c2[2]); PIN(c2[3]);
    }
    ((LAS float*)(L + P1_EX + 1024))[32 * t + (lane & 31)] = halfsum((c2[0] + c2[1]) + (c2[2] + c2[3]));
    lds_bar();
    lds_bar();
}

__device__ __forceinline__ f32x16 pv(u32x4 v, f32x8 pr, f32x16 o) {
    return __builtin_amdgcn_smfmac_f32_32x32x32_bf16(__builtin_bit_cast(bf8, v), __builtin_bit_cast(bf16v, pr), o, 0xDDDD, 0, 0);
}
__device__ __forceinline__ void mfma0(f32x16& s, u32x4 a, u32x4 b) { asm volatile("v_mfma_f32_32x32x16_bf16 %0, %1, %2, 0" : "+v"(s) : "v"(a), "v"(b)); }
__device__ __forceinline__ f32x8 lo8(const f32x16& s) { return __builtin_shufflevector(s, s, 0, 1, 2, 3, 4, 5, 6, 7); }
__device__ __forceinline__ f32x8 hi8(const f32x16& s) { return __builtin_shufflevector(s, s, 8, 9, 10, 11, 12, 13, 14, 15); }
__device__ __forceinline__ unsigned slab_voff(int lane, int pitch) { return (lane >> 3) * pitch + (((lane & 7) ^ (((lane >> 4) & 1) << 2)) << 4); }
__device__ __forceinline__ int slab_rd(int lane, int t) {
    const int q = (lane & 15) >> 2, pp = lane & 3, g1 = (lane >> 4) & 1, hh = lane >> 5;
    return (4 * hh + q) * 128 + (((8 * t + 4 * g1 + pp) ^ ((q >> 1) << 3)) << 3);
}
__device__ __forceinline__ f32x16 redo_o(rsrc_t rws, int lane, int head, const u32x4 (&qf)[2]) {
    float m = -INFINITY;
#pragma unroll 1
    for (int t = 0; t < 64; ++t) {
        const unsigned b = WS_KV + (head * 64 + t) * 4096;
        f32x16 s = {};
        s = mfma(ldh(rws, lane * 16, b), qf[0], s);
        s = mfma(ldh(rws, lane * 16 + 1024, b), qf[1], s);
#pragma unroll
        for (int k = 0; k < 16; ++k) m = fmaxf(m, s[k]);
    }
    auto mm = __builtin_amdgcn_permlane32_swap(__float_as_uint(m), __float_as_uint(m), false, false);
    m = fmaxf(__uint_as_float(mm[0]), __uint_as_float(mm[1]));
    f32x16 o = {};
#pragma unroll 1
    for (int t = 0; t < 64; ++t) {
        const unsigned b = WS_KV + (head * 64 + t) * 4096;
        f32x16 s = {};
        s = mfma(ldh(rws, lane * 16, b), qf[0], s);
        s = mfma(ldh(rws, lane * 16 + 1024, b), qf[1], s);
#pragma unroll
        for (int k = 0; k < 16; ++k) s[k] = __builtin_amdgcn_exp2f(s[k] - m);
        o = pv(ldh(rws, lane * 16 + 2048, b), lo8(s), o);
        o = pv(ldh(rws, lane * 16 + 3072, b), hi8(s), o);
    }
    return o;
}

#define W1DMA(hf, u, e) do { int uu_ = (u) + 2 * ct1; if (uu_ >= 28) uu_ -= 28; dma16(r1, v1, (uu_ * 16 + (e) * 8) * 3584 + ct1 * 256 + (hf) * 128, L_W1 + (hf) * SLAB + (u) * 2048 + (e) * 1024); } while (0)

__host__ __device__ constexpr int step_q(int n) { return n < 52 ? (n & 3) : (n - 52) / 3; }
__host__ __device__ constexpr int step_k(int n) { return n < 52 ? (n >> 2) : 13 + (n - 52) % 3; }

__device__ __forceinline__ void attn_compute(LAS char* L, rsrc_t rws, int w, int lane, int head ST_ARG) {
    u32x4 kf[5][2], vf[5][2];
    const unsigned kvb = WS_KV + (head * 64 + w * 16) * 4096;
    auto ld = [&](int t, int s) INL { (s < 2 ? kf : vf)[t % 5][s & 1] = ldh(rws, lane * 16 + s * 1024, kvb + t * 4096); SBAR(); };
    f32x16 o[4], sc[3];
    { float z = 0.f; PIN(z);
#pragma unroll
      for (int k = 0; k < 64; ++k) o[k >> 4][k & 15] = z;
      sc[0] = o[0]; sc[1] = o[0]; sc[2] = o[0];
      PIN(o[0]); PIN(o[1]); PIN(o[2]); PIN(o[3]); PIN(sc[0]); PIN(sc[1]); PIN(sc[2]); }
    __builtin_amdgcn_s_setprio(1);
    lds_bar();
    ST(5);
#pragma unroll
    for (int k = 0; k < 10; ++k) ld(k >> 2, k & 3);
    u32x4 qf[4][2];
#pragma unroll
    for (int k = 0; k < 8; ++k) qf[k >> 1][k & 1] = ldsr128(L + L_QF + k * 1024 + lane * 16);
    mfma0(sc[0], kf[0][0], qf[0][0]);
    sc[0] = mfma(kf[0][1], qf[0][1], sc[0]);
    LAS char* pw = L + L_P + w * 4096 + lane * 16;
    ST(6);
#define EXP4(X, i) do { X[i] = __builtin_amdgcn_exp2f(X[i]); X[i + 1] = __builtin_amdgcn_exp2f(X[i + 1]); X[i + 2] = __builtin_amdgcn_exp2f(X[i + 2]); X[i + 3] = __builtin_amdgcn_exp2f(X[i + 3]); PIN(X); SBAR(); } while (0)
    auto step = [&](auto ic) INL {
        constexpr int n = ic.value, qn = step_q(n + 1), kn = step_k(n + 1) % 5, qp = step_q(n > 0 ? n - 1 : 0), kp = step_k(n > 0 ? n - 1 : 0) % 5;
        f32x16 &sn = sc[(n + 1) % 3], &s = sc[n % 3], &sp = sc[(n + 2) % 3];
        if constexpr (n + 1 < 64) { mfma0(sn, kf[kn][0], qf[qn][0]); SBAR(); }
        if constexpr (n < 64) EXP4(s, 0);
        if constexpr (n + 1 < 64) { sn = mfma(kf[kn][1], qf[qn][1], sn); PIN(sn); SBAR(); }
        if constexpr (n < 64) EXP4(s, 4);
        if constexpr (n > 0) { o[qp] = pv(vf[kp][0], lo8(sp), o[qp]); PIN(o[qp]); SBAR(); }
        if constexpr (n < 64) EXP4(s, 8);
        if constexpr (n > 0) { o[qp] = pv(vf[kp][1], hi8(sp), o[qp]); PIN(o[qp]); SBAR(); }
        constexpr int issued = 10 + n + (n < 8 ? n : 8) < 64 ? 10 + n + (n < 8 ? n : 8) : 64;
        constexpr int nk = 4 * step_k(n + 2 < 64 ? n + 2 : 63) + 1, nv = 4 * step_k(n < 64 ? n : 63) + 3, need = nk > nv ? nk : nv, left = issued - 1 - need;
        if constexpr (n < 55) { __builtin_amdgcn_s_waitcnt(0x0F70 | (left & 15) | ((left >> 4) << 14)); SBAR(); }
        if constexpr (n < 64) { s[12] = __builtin_amdgcn_exp2f(s[12]); PIN(s); SBAR(); }
        if constexpr (issued < 64) ld(issued >> 2, issued & 3);
        if constexpr (n < 8) ld((issued + 1) >> 2, (issued + 1) & 3);
        if constexpr (n < 64) { s[13] = __builtin_amdgcn_exp2f(s[13]); s[14] = __builtin_amdgcn_exp2f(s[14]); s[15] = __builtin_amdgcn_exp2f(s[15]); PIN(s); SBAR(); }
        if constexpr (n == 56 || n == 59 || n == 62 || n == 64) {
            constexpr int qt = n == 64 ? 3 : (n - 56) / 3;
#pragma unroll
            for (int i = 0; i < 4; ++i) *(LAS f32x4*)(pw + qt * 16384 + i * 1024) = (f32x4){o[qt][4 * i], o[qt][4 * i + 1], o[qt][4 * i + 2], o[qt][4 * i + 3]};
            SBAR();
        }
    };
    sfor<65>(step);
    __builtin_amdgcn_s_setprio(0);
}

__device__ __forceinline__ int fc2_kstep(int p, int rot) { return mod7((p % 14) / 2 + rot) * 8 + (p / 14) * 2 + (p & 1); }
__device__ __forceinline__ int fc2_rot(int j) { return (j % 7 + 2 * (j / 7)) % 7; }
__device__ __forceinline__ unsigned w2src(int c, int hw, int i, int j) {
    const int ks = (hw & 1) ? fc2_kstep(4 * c + 2 + (i >> 1), fc2_rot(j)) : fc2_kstep(4 * c + (i >> 1), fc2_rot(j));
    return (((hw >> 1) * 56 + ks) * 16 + (i & 1) * 8) * 896 + (j % 7) * 128;
}
__device__ __forceinline__ void attn_helper(const Params& p, rsrc_t rws, int hw, int lane, int head, int j, unsigned tag, u32x4 (&sg)[9][4]) {
    const rsrc_t rw = mkrs(p.wo, D * D * 2), r1 = mkrs(p.w1, D * F * 2), r2 = mkrs(p.w2, F * D * 2);
    const unsigned vt = lane < 49 ? lane * 128 : 0x7FFFFF00u, land = L_W1 + hw * 1024;
    dma4(r1, vt, (j * 4 + hw) * 49 * 128, land);
    if (hw == 3) { __builtin_amdgcn_s_sleep(22); wait_word(rws, WS_F1 + ((head >> 1) * 32 + (lane & 31)) * 128, tag); }
    bar();
    __builtin_amdgcn_s_sleep(32);
    dma4(r1, vt, (128 + j * 4 + hw) * 49 * 128, land);
    vm_wait<0>();
    const unsigned v2 = slab_voff(lane, 896), v1 = slab_voff(lane, 3584);
    const int ct = j % 7, ct1 = j % 14;
#pragma unroll
    for (int i = 0; i < 14; ++i) {
        int ii = i + 4 * (j / 7); if (ii >= 14) ii -= 14; if (ii >= 14) ii -= 14;
        const int pos = ii * 4 + hw, kh = pos >= 28;
        int pp = pos - 28 * kh + 7 * (ct & 3); if (pp >= 28) pp -= 28;
        dma16(rw, v2, (kh * 28 + pp) * 8 * 896 + ct * 128, L_WO + pos * 1024);
        vm_wait<4>();
    }
#pragma unroll
    for (int k = 0; k < 36; ++k) { sg[k >> 2][k & 3] = __builtin_amdgcn_raw_buffer_load_b128(r2, v2, w2src(5 + (k >> 2), hw, k & 3, j), 0); vm_wait<4>(); }
#pragma unroll
    for (int i = 0; i < 8; ++i) { W1DMA(0, i * 2 + (hw >> 1), hw & 1); vm_wait<4>(); }
}

__device__ __forceinline__ void attn_finish(LAS char* L, rsrc_t rws, int w, int lane, int rb, int head, unsigned tag ST_ARG) {
    const int qt = w >> 1, s = w & 1;
    LAS char* P = L + L_P + qt * 16384 + s * 2048 + lane * 16;
    LAS char* PL = L + L_P + qt * 16384 + 3 * 1024 + (32 + (lane & 31)) * 16;
    unsigned dst = WS_O + (((rb * 4 + qt) * 16 + head) * 2 + s) * 1024 + lane * 16;
    PIN(P); PIN(PL); PIN(dst);
    ST(7);
    wait_bar<0>();
    ST(8);
    f32x4 a[4][2];
    float lp[4];
#pragma unroll
    for (int ww = 0; ww < 4; ++ww) lp[ww] = *(LAS float*)(PL + ww * 4096);
#pragma unroll
    for (int ww = 0; ww < 4; ++ww) { a[ww][0] = *(LAS f32x4*)(P + ww * 4096); a[ww][1] = *(LAS f32x4*)(P + ww * 4096 + 1024); }
    SBAR();
    float l = (lp[0] + lp[1]) + (lp[2] + lp[3]);
    f32x4 x = (a[0][0] + a[1][0]) + (a[2][0] + a[3][0]), y = (a[0][1] + a[1][1]) + (a[2][1] + a[3][1]);
    const float asum = ((fabsf(x[0]) + fabsf(x[1])) + (fabsf(x[2]) + fabsf(x[3]))) + ((fabsf(y[0]) + fabsf(y[1])) + (fabsf(y[2]) + fabsf(y[3])));
    if (__builtin_expect(!__all(l >= 0x1p-40f && l <= 0x1p40f && asum <= 0x1p120f), 0)) {
        const u32x4 qf[2] = {ldsr128(L + L_QF + qt * 2048 + lane * 16), ldsr128(L + L_QF + qt * 2048 + 1024 + lane * 16)};
        const f32x16 o = redo_o(rws, lane, head, qf);
        l = __uint_as_float(__builtin_amdgcn_permlane32_swap(__float_as_uint(o[12]), __float_as_uint(o[12]), false, false)[1]);
#pragma unroll
        for (int k = 0; k < 4; ++k) { x[k] = s ? o[8 + k] : o[k]; y[k] = s ? o[12 + k] : o[4 + k]; }
    }
    const float il = __builtin_amdgcn_rcpf(l);
    u32x4 v = {cvtpk(x[0] * il, x[1] * il), cvtpk(x[2] * il, x[3] * il), cvtpk(y[0] * il, y[1] * il), cvtpk(y[2] * il, y[3] * il)};
    if (s == 1 && lane >= 32) { v[2] = 0u; v[3] = 0u; }
    st128<SC1>(v, rws, dst, 0);
    ST(24);
}

__device__ __forceinline__ unsigned bfsplit(float v, float& rest) { const unsigned h = cvtpk(v, v) & 0xFFFFu; rest = v - bflo(h); return h; }

__device__ __forceinline__ void mlp_helper(const Params& p, LAS char* L, rsrc_t rws, int hw, int lane, int xcd, int j, unsigned tag, const u32x4 (&sg)[9][4]) {
    const int r = lane & 31, hh = lane >> 5, ct1 = j % 14, ct = j % 7;
    const int b4 = xcd * 4 + j / 7, b2 = xcd * 2 + j / 14;
    const rsrc_t r1 = mkrs(p.w1, D * F * 2), r2 = mkrs(p.w2, F * D * 2);
    const unsigned v1 = slab_voff(lane, 3584), v2 = slab_voff(lane, 896);
    unsigned bb[2];
#pragma unroll
    for (int hf = 0; hf < 2; ++hf) bb[hf] = hh ? 0u : ((const unsigned short*)p.b1)[ct1 * 128 + hf * 64 + (hw & 1) * 32 + r];
    auto colsum = [&](int hf) INL {
        float c2[4] = {};
        LAS char* wb = L + L_W1 + hf * SLAB + slab_rd(lane, hw & 1) + (hw >> 1) * 14 * 2048;
        PIN(wb);
#pragma unroll
        for (int u = 0; u < 14; ++u) {
            const u32x4 f = wfrag<1024>(wb + u * 2048);
#pragma unroll
            for (int k = 0; k < 4; ++k) {
                if (hf == 0) c2[k] = dot2(f[k], ONES, c2[k]);
                else { c2[2 * (k & 1)] = sadd(c2[2 * (k & 1)], bflo(f[k])); c2[2 * (k & 1) + 1] = sadd(c2[2 * (k & 1) + 1], bfhi(f[k])); }
            }
        }
        float lo, lo2;
        const unsigned ch = bfsplit((c2[0] + c2[1]) + (c2[2] + c2[3]), lo), cl = bfsplit(lo, lo2);
        *(LAS u32x2*)(L + L_EXT + (2 * hf + (hw & 1)) * 1024 + lane * 16 + (hw >> 1) * 8) = (u32x2){ch | (ch << 16), cl | (bb[hf] << 16)};
    };
    auto w2dma = [&](int c) INL {
#pragma unroll
        for (int i = 0; i < 4; ++i) dma16(r2, v2, w2src(c, hw, i, j), w2slot(c) + hw * 4096 + i * 1024);
    };
    auto w2st = [&](int c) INL {
#pragma unroll
        for (int i = 0; i < 4; ++i) *(LAS u32x4*)(L + w2slot(c) + hw * 4096 + i * 1024 + lane * 16) = sg[c - 5][i];
        SBAR();
    };
    if (hw == 3) { __builtin_amdgcn_s_sleep(8); wait_word(rws, WS_F2 + ((b4 >> 1) * 16 + (lane & 15)) * 128, tag); }
    else if (hw < 2) {
#pragma unroll
        for (int u = 16; u < 28; ++u) W1DMA(0, u, hw);
#pragma unroll
        for (int u = 0; u < 12; ++u) W1DMA(1, u, hw);
    }
    bar();
    bar();
    wait_bar<0>();
    if (hw == 3) set_flag(rws, WS_F3 + (b4 * 8 + ct) * 128, tag, lane == 0);
    if (hw == 2) ((LAS unsigned*)(L + L_SYNC4))[lane] = 0u;
    if (hw < 2) {
#pragma unroll
        for (int u = 12; u < 28; ++u) W1DMA(1, u, hw);
    }
    colsum(0);
    if (hw == 3) { const int l = lane < 14 ? lane : 0; wait_word(rws, WS_F3 + ((b2 * 2 + (l >= 7)) * 8 + mod7(l)) * 128, tag); }
    wait_bar<0>();
    colsum(1);
    lds_bar();
    w2dma(0); w2dma(1); w2dma(2); w2dma(3); w2dma(4);
    bar();
    w2st(5); w2st(6); w2st(7); w2st(8);
    const unsigned f4 = WS_F4 + ((b4 >> 1) * 16 + (lane < 14 ? lane : 0)) * 128;
    if (hw == 3) wait_word(rws, f4, tag);
    wait_bar<0>();
    if (hw == 3) wait_word(rws, f4 + 256 * 128, tag);
    bar();
    w2st(9); w2st(10); w2st(11); w2st(12);
    if (hw == 3) wait_word(rws, f4 + 512 * 128, tag);
    lds_bar();
    w2st(13);
    lds_bar();
    bar();
}

__device__ __forceinline__ void mlp_compute(const Params& p, LAS char* L, rsrc_t rws, int w, int lane, int xcd, int j, unsigned tag ST_ARG) {
    const int r = lane & 31, hh = lane >> 5;
    const int r4 = j / 7, ct = j % 7, m = w & 1, kh = w >> 1;
    const int rt = xcd * 8 + r4 * 2 + m;
    const int sr[2] = {slab_rd(lane, 0), slab_rd(lane, 1)};
    u32x4 hpk[2];
    u32x4 bf[4][2];
    {
        u32x4 wf[16][2];
        LAS char* wo[2][2];
#pragma unroll
        for (int t = 0; t < 2; ++t) { wo[t][0] = L + L_WO + sr[t] + kh * 224 * 128; wo[t][1] = wo[t][0] - hh * 512; PIN(wo[t][0]); PIN(wo[t][1]); }
#pragma unroll
        for (int u = 0; u < 16; ++u)
#pragma unroll
            for (int t = 0; t < 2; ++t) {
                const int row = (u >> 1) * 28 + (u & 1) * 16;
                const u32x2 a = trread(wo[t][0] + row * 128), b = trread(wo[t][u & 1] + (row + 8) * 128);
                wf[u][t] = (u32x4){a[0], a[1], b[0], b[1]};
            }
        wait_bar<0>();
        ST(9);
        const rsrc_t rx = mkrs(p.x, S * D * 2);
        u32x2 xr[4];
#pragma unroll
        for (int i = 0; i < 4; ++i) xr[i] = __builtin_amdgcn_raw_buffer_load_b64(rx, r * 896 + hh * 8 + i * 16, rt * 32 * 896 + ct * 128 + kh * 64, 0);
        bar();
        ST(10);
        u32x4 af[16];
#pragma unroll
        for (int u = 0; u < 16; ++u) af[u] = ldh(rws, lane * 16, WS_O + (rt * 32 + kh * 16 + ((u + 4 * ct) & 15)) * 1024);
        SBAR();
        f32x16 acc[2] = {};
#pragma unroll
        for (int u = 0; u < 16; ++u) { acc[0] = mfma(wf[u][0], af[u], acc[0]); acc[1] = mfma(wf[u][1], af[u], acc[1]); }
        const f32x16 snd = kh ? acc[0] : acc[1];
        f32x16 hv = kh ? acc[1] : acc[0];
#pragma unroll
        for (int i = 0; i < 4; ++i) *(LAS f32x4*)(L + L_RED1 + (w * 4 + i) * 1024 + lane * 16) = (f32x4){snd[4 * i], snd[4 * i + 1], snd[4 * i + 2], snd[4 * i + 3]};
        ST(11);
        lds_bar();
#pragma unroll
        for (int i = 0; i < 4; ++i) {
            const f32x4 o = *(LAS f32x4*)(L + L_RED1 + ((w ^ 2) * 4 + i) * 1024 + lane * 16);
            hv[4 * i] += o[0] + bflo(xr[i][0]); hv[4 * i + 1] += o[1] + bfhi(xr[i][0]);
            hv[4 * i + 2] += o[2] + bflo(xr[i][1]); hv[4 * i + 3] += o[3] + bfhi(xr[i][1]);
        }
#pragma unroll
        for (int s = 0; s < 2; ++s) { hpk[s] = pack8(hv, s); st128<SC1>(hpk[s], rws, lane * 16, WS_H + (rt * 28 + ct * 4 + kh * 2 + s) * 1024); }
        float s1 = 0.f, s2 = 0.f;
#pragma unroll
        for (int k = 0; k < 8; ++k) { const unsigned u = hpk[k >> 2][k & 3]; s1 = dot2(u, ONES, s1); s2 = dot2(u, u, s2); }
        __builtin_amdgcn_raw_buffer_store_b64((u32x2){__float_as_uint(halfsum(s1)), __float_as_uint(halfsum(s2))}, rws,
                                              hh ? 0x7FFFFFF0u : WS_HS + ((rt * 14 + ct * 2 + kh) * 32 + r) * 8, 0, SC1);
        wait_bar<0>();
        ST(12);
    }
    {
        const int ct1 = j % 14, rt1 = xcd * 8 + (j / 14) * 4 + w;
        auto RD = [&](int u) INL {
#pragma unroll
            for (int t = 0; t < 2; ++t) bf[u & 3][t] = wfrag<1024>(L + L_W1 + sr[t] + u * 2048);
            SBAR();
        };
        RD(0); RD(1); RD(2);
        bar();
        ST(13);
        u32x4 af[28];
        auto aload = [&](int u) INL { int uu = u + 2 * ct1; if (uu >= 28) uu -= 28; af[u] = ldh(rws, lane * 16, WS_H + (rt1 * 28 + uu) * 1024); };
        constexpr int WIN = 10;
#pragma unroll
        for (int u = 0; u < WIN; ++u) aload(u);
        SBAR();
        f32x16 acc[4] = {};
        u32x2 st[7];
        u32x4 b2f[4];
        LAS char* w1b[2] = {L + L_W1 + SLAB + sr[0], L + L_W1 + SLAB + sr[1]};
        PIN(w1b[0]); PIN(w1b[1]);
        auto RD2 = [&](int z) INL { b2f[z & 3] = wfrag<1024>(w1b[z / 28] + (z % 28) * 2048); SBAR(); };
        sfor<28>([&](auto ic) INL {
            constexpr int u = ic.value;
            if constexpr (u + 3 < 28) RD(u + 3); else RD2(u + 3 - 28);
            acc[0] = mfma(bf[u & 3][0], af[u], acc[0]); acc[1] = mfma(bf[u & 3][1], af[u], acc[1]);
            PIN(acc[0]); PIN(acc[1]); SBAR();
            if constexpr (u + WIN < 28) aload(u + WIN);
            else if constexpr (u + WIN < 35) st[u + WIN - 28] = __builtin_amdgcn_raw_buffer_load_b64(rws, (hh * 7 * 32 + r) * 8, WS_HS + (rt1 * 14 + u + WIN - 28) * 256, SC1);
            SBAR();
        });
        ST(14);
        bar();
        u32x4 ext[4];
#pragma unroll
        for (int t = 0; t < 4; ++t) ext[t] = ldsr128(L + L_EXT + t * 1024 + lane * 16);
        float s1 = 0.f, s2 = 0.f;
#pragma unroll
        for (int k = 0; k < 7; ++k) { s1 += __uint_as_float(st[k][0]); s2 += __uint_as_float(st[k][1]); }
        const float mu = halfsum(s1) * (1.0f / D);
        const float var = fmaxf(halfsum(s2) * (1.0f / D) - mu * mu, 0.f);
        const float r1 = __builtin_amdgcn_rsqf(var + EPS);
        const float rs = r1 * __builtin_amdgcn_rsqf(var * r1 * r1 + EPS), irs = __builtin_amdgcn_rcpf(rs);
        float t0, t1;
        const unsigned mh = bfsplit(-mu, t0), ml = bfsplit(t0, t1), ih = bfsplit(irs, t0), il = bfsplit(t0, t1);
        const u32x4 xe = {mh | (ml << 16), mh | (ih << 16), mh | (ml << 16), mh | (il << 16)};
        acc[0] = mfma(ext[0], xe, acc[0]); acc[1] = mfma(ext[1], xe, acc[1]);
        acc[2] = mfma(ext[2], xe, acc[2]); acc[3] = mfma(ext[3], xe, acc[3]);
        PIN(acc[0]); PIN(acc[1]); PIN(acc[2]); PIN(acc[3]); SBAR();
        constexpr float C0 = -2.0f * 0.7978845608028654f * 1.4426950408889634f, C1 = C0 * 0.044715f;
        const float k0 = C0 * rs, k1 = C1 * rs * rs * rs;
        f32x4 ga;
        auto gelu = [&](f32x16& v, int i, int stage) INL {
#pragma unroll
            for (int k = 0; k < 4; ++k) {
                const float a = v[4 * i + k];
                if (stage == 0) ga[k] = smul(a, sfma(smul(a, a), k1, k0));
                if (stage == 1) ga[k] = __builtin_amdgcn_exp2f(ga[k]);
                if (stage == 2) ga[k] = __builtin_amdgcn_rcpf(sfma(ga[k], irs, irs));
                if (stage == 3) v[4 * i + k] = smul(a, ga[k]);
            }
            if (stage < 3) PIN(ga); else PIN(v);
            SBAR();
        };
        auto put = [&](int t) INL {
#pragma unroll
            for (int s = 0; s < 2; ++s) st128<SC1>(pack8(acc[t], s), rws, lane * 16, WS_G + (rt1 * 112 + ct1 * 8 + t * 2 + s) * 1024);
        };
        auto flag = [&](int stage) INL { publish(L + L_SYNC4 + stage * 4, rws, WS_F4 + (stage * 256 + (xcd * 2 + j / 14) * 16 + ct1) * 128, tag, lane); };
        sfor<56>([&](auto ic) INL {
            constexpr int z = ic.value, t = 2 + z / 28;
            if constexpr (z + 3 < 56) RD2(z + 3);
            acc[t] = mfma(b2f[z & 3], af[z % 28], acc[t]); PIN(acc[t]); SBAR();
            constexpr int e = z - 4;
            if constexpr (e >= 0 && e < 48) { gelu(acc[e >> 4], (e >> 2) & 3, e & 3); if constexpr ((e & 15) == 15) { put(e >> 4); SBAR(); } }
            if constexpr (z == 47) { vm_wait<0>(); flag(0); SBAR(); }
        });
        ST(15);
        bar();
#pragma unroll
        for (int k = 0; k < 16; ++k) {
            const float a = acc[3][k];
            acc[3][k] = a * __builtin_amdgcn_rcpf(fmaf(__builtin_amdgcn_exp2f(a * fmaf(a * a, k1, k0)), irs, irs));
        }
        put(3);
        vm_wait<2>(); flag(1);
        ST(16);
        bar();
        ST(17);
        constexpr int WN2 = 10;
        u32x4 ag[WN2];
        const int rot2 = fc2_rot(j);
        const unsigned abase = WS_G + (rt * 112 + kh * 56) * 1024;
#pragma unroll
        for (int u = 0; u < WN2; ++u) ag[u] = ldh(rws, lane * 16, abase + fc2_kstep(u, rot2) * 1024);
        SBAR();
        vm_wait<WN2>(); flag(2);
        LAS char* w2b[3][2];
#pragma unroll
        for (int k = 0; k < 6; ++k) { w2b[k >> 1][k & 1] = L + sr[k & 1] + kh * 8192 + (k >> 1) * 48 * K1; PIN(w2b[k >> 1][k & 1]); }
        auto RD3 = [&](int u) INL {
#pragma unroll
            for (int t = 0; t < 2; ++t) bf[u & 3][t] = wfrag<1024>(w2b[w2slot(u >> 2) / (48 * K1)][t] + w2slot(u >> 2) % (48 * K1) + (u & 3) * 2048);
            SBAR();
        };
        RD3(0); RD3(1); RD3(2);
        ST(18);
        f32x16 a2[2] = {};
        u32x2 bb[4];
        sfor<56>([&](auto ic) INL {
            constexpr int u = ic.value;
            if constexpr (u == 16 || u == 32 || u == 44) bar();
            if constexpr (u == 16) ST(19);
            if constexpr (u + 3 < 56) RD3(u + 3);
            a2[0] = mfma(bf[u & 3][0], ag[u % WN2], a2[0]); a2[1] = mfma(bf[u & 3][1], ag[u % WN2], a2[1]);
            PIN(a2[0]); PIN(a2[1]); SBAR();
            if constexpr (u + WN2 < 56) ag[u % WN2] = ldh(rws, lane * 16, abase + fc2_kstep(u + WN2, rot2) * 1024);
            else if constexpr (u + WN2 < 60) bb[u + WN2 - 56] = __builtin_amdgcn_raw_buffer_load_b64(mkrs(p.b2, D * 2), hh * 8 + (u + WN2 - 56) * 16, ct * 128 + kh * 64, 0);
            SBAR();
        });
        ST(20);
        const f32x16 snd = kh ? a2[0] : a2[1];
        f32x16 v = kh ? a2[1] : a2[0];
#pragma unroll
        for (int i = 0; i < 4; ++i) *(LAS f32x4*)(L + L_RED2 + (w * 4 + i) * 1024 + lane * 16) = (f32x4){snd[4 * i], snd[4 * i + 1], snd[4 * i + 2], snd[4 * i + 3]};
        lds_bar();
        unsigned o[8];
#pragma unroll
        for (int i = 0; i < 4; ++i) {
            const f32x4 t = *(LAS f32x4*)(L + L_RED2 + ((w ^ 2) * 4 + i) * 1024 + lane * 16);
            const unsigned h0 = hpk[i >> 1][2 * (i & 1)], h1 = hpk[i >> 1][2 * (i & 1) + 1];
            o[2 * i] = cvtpk(v[4 * i] + t[0] + bflo(bb[i][0]) + bflo(h0), v[4 * i + 1] + t[1] + bfhi(bb[i][0]) + bfhi(h0));
            o[2 * i + 1] = cvtpk(v[4 * i + 2] + t[2] + bflo(bb[i][1]) + bflo(h1), v[4 * i + 3] + t[3] + bfhi(bb[i][1]) + bfhi(h1));
        }
        const rsrc_t ro = mkrs(p.out, S * D * 2);
#pragma unroll
        for (int k = 0; k < 2; ++k) {
            auto s0 = __builtin_amdgcn_permlane32_swap(o[4 * k], o[4 * k + 2], false, false);
            auto s1_ = __builtin_amdgcn_permlane32_swap(o[4 * k + 1], o[4 * k + 3], false, false);
            st128<0>((u32x4){s0[0], s1_[0], s0[1], s1_[1]}, ro, r * 896 + hh * 16 + k * 32, rt * 32 * 896 + ct * 128 + kh * 64);
        }
        ST(21);
    }
}

__global__ void __launch_bounds__(NT, 2) k_block(Params p) {
    extern __shared__ __attribute__((aligned(16))) char smem[];
    LAS char* L = (LAS char*)smem;
    ST_DECL
    ST(0);
    const int lane = threadIdx.x & 63, w = __builtin_amdgcn_readfirstlane(threadIdx.x >> 6);
    const int b = blockIdx.x, xcd = b & 7, j = b >> 3;
    const int rb = (xcd & 1) * 8 + (j & 7), sub = j >> 3, head = 4 * (xcd >> 1) + sub, pair = 2 * (xcd >> 1) + (sub >> 1), kind = sub & 1;
    const rsrc_t rws = mkrs(p.ws, WS_END);
    const int rot = ((j & 7) + 2 * sub) % 7;
    if (w == 7) {
        unsigned long long pc;
        asm volatile("s_getpc_b64 %0" : "=s"(pc));
        dma4(mkrs((const void*)pc, CODE_LEN), (lane * 8 + (j & 7)) * 128, 0, P1_EX + 1536);
    }
    unsigned tag;
    if (w < 4) {
        tag = load_tag(p);
        if (kind) p1_compute<true>(L, rws, w, lane, rb, pair, head, tag ST_PASS);
        else p1_compute<false>(L, rws, w, lane, rb, pair, head, tag ST_PASS);
        ST(4);
        attn_compute(L, rws, w, lane, head ST_PASS);
        attn_finish(L, rws, w, lane, rb, head, tag ST_PASS);
        if (j < 28) mlp_compute(p, L, rws, w, lane, xcd, j, tag ST_PASS);
        else wait_bar<0>();
    } else {
        if (w < 6) p1_loader(p, w - 4, lane, rb, pair, kind, head, rot, tag);
        else { tag = load_tag(p); p1_colsum(L, w - 6, lane); }
        ST(4);
        u32x4 sg[9][4];
        attn_helper(p, rws, w - 4, lane, head, j, tag, sg);
        attn_finish(L, rws, w, lane, rb, head, tag ST_PASS);
        wait_bar<0>();
        if (w == 7) set_flag(rws, WS_F2 + (rb * 16 + head) * 128, tag, lane == 0);
        ST(9);
        if (j < 28) mlp_helper(p, L, rws, w - 4, lane, xcd, j, tag, sg);
    }
    if (b == 0 && w == 0) set_flag(rws, WS_GEN, tag, lane == 0);
    ST_FLUSH();
}

extern "C" void kernel_launch(void* const* d_in, const int* in_sizes, int n_in, void* d_out, int out_size, void* d_ws, size_t ws_size, hipStream_t stream) {
    Params p;
    p.x = d_in[0]; p.wqkv = d_in[1]; p.wo = d_in[2]; p.w1 = d_in[4]; p.b1 = d_in[5]; p.w2 = d_in[6]; p.b2 = d_in[7];
    p.out = d_out; p.ws = d_ws;
    (void)hipFuncSetAttribute((const void*)k_block, hipFuncAttributeMaxDynamicSharedMemorySize, LDS_BYTES);
    k_block<<<NWG, NT, LDS_BYTES, stream>>>(p);
}
```

```cpp
#include <hip/hip_runtime.h>
#include <stdint.h>
#include <utility>

#define LAS __attribute__((address_space(3)))
#define GAS __attribute__((address_space(1)))
typedef __bf16 bf2 __attribute__((ext_vector_type(2)));
typedef __bf16 bf8 __attribute__((ext_vector_type(8)));
typedef __bf16 bf16v __attribute__((ext_vector_type(16)));
typedef float f32x4 __attribute__((ext_vector_type(4)));
typedef float f32x8 __attribute__((ext_vector_type(8)));
typedef float f32x16 __attribute__((ext_vector_type(16)));
typedef unsigned u32x2 __attribute__((ext_vector_type(2)));
typedef unsigned u32x4 __attribute__((ext_vector_type(4)));
typedef short s16x4 __attribute__((ext_vector_type(4)));
typedef __amdgpu_buffer_rsrc_t rsrc_t;

constexpr int S = 2048, D = 448, H = 16, F = 1792;
constexpr int NT = 512, NWG = 256;
constexpr float EPS = 1e-5f;
constexpr int LDS_BYTES = 160 * 1024;
constexpr int SC1 = 16;
constexpr unsigned CODE_LEN = 52 * 1024;
constexpr unsigned WS_GEN = 0;
constexpr unsigned WS_F1 = 4096;
constexpr unsigned WS_F2 = WS_F1 + 256 * 128;
constexpr unsigned WS_F3 = WS_F2 + 256 * 128;
constexpr unsigned WS_F4 = WS_F3 + 256 * 128;
constexpr unsigned WS_HS = WS_F4 + 768 * 128;
constexpr unsigned WS_KV = 512 * 1024;
constexpr unsigned WS_O = WS_KV + 16 * 64 * 4096;
constexpr unsigned WS_H = WS_O + 64 * 32 * 1024;
constexpr unsigned WS_G = WS_H + 64 * 28 * 1024;
constexpr unsigned WS_END = WS_G + 64 * 112 * 1024;

struct Params {
    const void *x, *wqkv, *wo, *w1, *b1, *w2, *b2;
    void *out, *ws;
};

template <class Fn, int... I> __device__ __forceinline__ void sfor_(Fn&& f, std::integer_sequence<int, I...>) { (f(std::integral_constant<int, I>{}), ...); }
template <int N, class Fn> __device__ __forceinline__ void sfor(Fn&& f) { sfor_(f, std::make_integer_sequence<int, N>{}); }
#define INL __attribute__((always_inline))
__device__ __forceinline__ rsrc_t mkrs(const void* p, unsigned bytes) { return __builtin_amdgcn_make_buffer_rsrc((void*)p, 0, (int)bytes, 0x00020000); }
__device__ __forceinline__ void dma16(rsrc_t rs, unsigned voff, unsigned soff, unsigned lds) {
    asm volatile("s_mov_b32 m0, %0\n\ts_nop 0\n\tbuffer_load_dwordx4 %1, %2, %3 offen lds" ::"s"(lds), "v"(voff), "s"(rs), "s"(soff) : "memory");
}
__device__ __forceinline__ void dma4(rsrc_t rs, unsigned voff, unsigned soff, unsigned lds) {
    asm volatile("s_mov_b32 m0, %0\n\ts_nop 0\n\tbuffer_load_dword %1, %2, %3 offen lds" ::"s"(lds), "v"(voff), "s"(rs), "s"(soff) : "memory");
}
__device__ __forceinline__ void dma4_if0(int skip, rsrc_t rs, unsigned voff, unsigned soff, unsigned lds) {
    asm volatile("s_cmp_lg_u32 %4, 0\n\ts_cbranch_scc1 1f\n\ts_mov_b32 m0, %0\n\ts_nop 0\n\tbuffer_load_dword %1, %2, %3 offen lds\n1:" ::"s"(lds), "v"(voff), "s"(rs), "s"(soff), "s"(skip) : "memory", "scc");
}
template <int N> __device__ __forceinline__ void wait_bar() { asm volatile("s_waitcnt vmcnt(%0) lgkmcnt(0)\n\ts_barrier" ::"i"(N) : "memory"); }
template <int N> __device__ __forceinline__ void vm_wait() { asm volatile("s_waitcnt vmcnt(%0)" ::"i"(N) : "memory"); }
__device__ __forceinline__ void lds_bar() { asm volatile("s_waitcnt lgkmcnt(0)\n\ts_barrier" ::: "memory"); }
__device__ __forceinline__ void bar() { asm volatile("s_barrier" ::: "memory"); }
__device__ __forceinline__ void lds_wait() { asm volatile("s_waitcnt lgkmcnt(0)" ::: "memory"); }
__device__ __forceinline__ f32x16 mfma(u32x4 a, u32x4 b, f32x16 c) {
    return __builtin_amdgcn_mfma_f32_32x32x16_bf16(__builtin_bit_cast(bf8, a), __builtin_bit_cast(bf8, b), c, 0, 0, 0);
}
__device__ __forceinline__ float dot2(unsigned a, unsigned b, float c) {
    return __builtin_amdgcn_fdot2_f32_bf16(__builtin_bit_cast(bf2, a), __builtin_bit_cast(bf2, b), c, false);
}
__device__ __forceinline__ unsigned cvtpk(float lo, float hi) {
    typedef float f2 __attribute__((ext_vector_type(2)));
    return __builtin_bit_cast(unsigned, __builtin_convertvector((f2){lo, hi}, bf2));
}
__device__ __forceinline__ float halfsum(float v) {
    auto r = __builtin_amdgcn_permlane32_swap(__float_as_uint(v), __float_as_uint(v), false, false);
    return __uint_as_float(r[0]) + __uint_as_float(r[1]);
}
template <int AUX> __device__ __forceinline__ void st128(u32x4 v, rsrc_t rs, unsigned voff, unsigned soff) { __builtin_amdgcn_raw_buffer_store_b128(v, rs, voff + soff, 0, AUX); }
__device__ __forceinline__ u32x4 ldh(rsrc_t rs, unsigned voff, unsigned soff) { return __builtin_amdgcn_raw_buffer_load_b128(rs, voff, soff, SC1); }
__device__ __forceinline__ u32x4 ldsr128(LAS char* p) { return *(LAS u32x4*)p; }
__device__ __forceinline__ u32x2 trread(LAS char* p) {
    return __builtin_bit_cast(u32x2, __builtin_amdgcn_ds_read_tr16_b64_v4i16((LAS s16x4*)p));
}
template <int ESTEP> __device__ __forceinline__ u32x4 wfrag(LAS char* p) {
    const u32x2 a = trread(p), b = trread(p + ESTEP);
    return (u32x4){a[0], a[1], b[0], b[1]};
}
__device__ __forceinline__ u32x4 pack8(const f32x16& v, int s) {
    return (u32x4){cvtpk(v[8 * s], v[8 * s + 1]), cvtpk(v[8 * s + 2], v[8 * s + 3]), cvtpk(v[8 * s + 4], v[8 * s + 5]), cvtpk(v[8 * s + 6], v[8 * s + 7])};
}
__device__ __forceinline__ float smul(float a, float b) { float r; asm("v_mul_f32 %0, %1, %2" : "=v"(r) : "v"(a), "v"(b)); return r; }
__device__ __forceinline__ float sadd(float a, float b) { float r; asm("v_add_f32 %0, %1, %2" : "=v"(r) : "v"(a), "v"(b)); return r; }
__device__ __forceinline__ float sfma(float a, float b, float c) { float r; asm("v_fma_f32 %0, %1, %2, %3" : "=v"(r) : "v"(a), "v"(b), "v"(c)); return r; }
__device__ __forceinline__ float bflo(unsigned u) { return __uint_as_float(u << 16); }
__device__ __forceinline__ float bfhi(unsigned u) { return __uint_as_float(u & 0xFFFF0000u); }
constexpr unsigned ONES = 0x3F803F80u;
#define PIN(x) asm volatile("" : "+v"(x))
#define SBAR() __builtin_amdgcn_sched_barrier(0)
#define PADWAIT() do { while (__builtin_amdgcn_s_memrealtime() - t_start < 5000ull) __builtin_amdgcn_s_sleep(1); } while (0)
#define ST(id) do { if ((id) == 99) PADWAIT(); } while (0)
#define ST_DECL const unsigned long long t_start = __builtin_amdgcn_s_memrealtime();
#define ST_ARG , unsigned long long t_start
#define ST_PASS , t_start
#define ST_FLUSH() do { const unsigned long long tl = *(volatile unsigned long long*)((char*)p.ws + 1024); while (__builtin_amdgcn_s_memrealtime() - tl < 5000ull) __builtin_amdgcn_s_sleep(1); } while (0)

constexpr int K1 = 1024;
constexpr int P1_XS = 0, P1_WS = 64 * K1, P1_WQ = 120 * K1, P1_EX = 148 * K1;
constexpr int L_SYNC1 = 150 * K1, L_SYNC4 = 144 * K1;
constexpr int L_P = 32 * K1, L_WO = 96 * K1, L_QF = 152 * K1;
constexpr int L_W1 = 0, SLAB = 56 * K1, L_RED1 = 80 * K1, L_EXT = 156 * K1, L_RED2 = 144 * K1;
__host__ __device__ constexpr int w2slot(int c) { return ((c % 9) < 2 ? 112 + 16 * (c % 9) : 16 * ((c % 9) - 2)) * K1; }

__device__ __forceinline__ void set_flag(rsrc_t rws, unsigned off, unsigned tag, bool me) {
    __builtin_amdgcn_raw_buffer_store_b32(tag, rws, me ? off : 0x7FFFFFF0u, 0, SC1);
}
__device__ __forceinline__ void publish(LAS char* cnt, rsrc_t rws, unsigned off, unsigned tag, int lane) {
    unsigned old = 0;
    if (lane == 0) old = __hip_atomic_fetch_add((LAS unsigned*)cnt, 1u, __ATOMIC_RELAXED, __HIP_MEMORY_SCOPE_WORKGROUP);
    set_flag(rws, off, tag, lane == 0 && old == 3u);
}
__device__ __forceinline__ void wait_word(rsrc_t rws, unsigned off, unsigned tag) {
#pragma unroll 1
    for (unsigned spins = 0; spins < (1u << 20); ++spins) {
        asm volatile("" ::: "memory");
        if (__all(__builtin_amdgcn_raw_buffer_load_b32(rws, off, 0, SC1) == tag)) break;
        __builtin_amdgcn_s_sleep(2);
    }
    asm volatile("" ::: "memory");
}
__device__ __forceinline__ unsigned load_tag(const Params& p) {
    unsigned g;
    asm volatile("s_load_dword %0, %1, 0x0\n\ts_waitcnt lgkmcnt(0)" : "=s"(g) : "s"(p.ws) : "memory");
    return g + 1;
}
__device__ __forceinline__ int mod7(int v) { return v >= 7 ? v - 7 : v; }

__device__ __forceinline__ int p1_wrd(int lane, int t) {
    const int q = (lane & 15) >> 2, pp = lane & 3, g1 = (lane >> 4) & 1, hh = lane >> 5;
    return P1_WS + (8 * hh + q) * 128 + (((7 * t + 4 * g1 + pp) ^ ((q >> 1) << 3)) << 3);
}
template <bool VT>
__device__ __forceinline__ void p1_compute(LAS char* L, rsrc_t rws, int w, int lane, int rb, int pair, int head, unsigned tag ST_ARG) {
    const int r = lane & 31, hh = lane >> 5;
    LAS char* xa[4];
#pragma unroll
    for (int uu = 0; uu < 4; ++uu) xa[uu] = L + P1_XS + w * 4096 + r * 128 + (((2 * uu + hh) ^ ((r >> 1) & 7)) << 4);
    LAS char* wb[2] = {L + p1_wrd(lane, 0), L + p1_wrd(lane, 1)};
    PIN(wb[0]); PIN(wb[1]);
    u32x4 ones = {ONES, ONES, ONES, ONES};
    PIN(ones);
    u32x4 xf[28];
    f32x16 acc[2] = {};
    float sx[2] = {}, sxx[2] = {};
    u32x4 bf[8][2];
    auto RD = [&](int u) INL {
        xf[u] = ldsr128(xa[u & 3] + ((u >> 2) & 3) * 16384);
        bf[u & 7][0] = wfrag<512>(wb[0] + u * 2048);
        bf[u & 7][1] = wfrag<512>(wb[1] + u * 2048);
        SBAR();
    };
    auto MM = [&](int u) INL {
        acc[0] = VT ? mfma(xf[u], bf[u & 7][0], acc[0]) : mfma(bf[u & 7][0], xf[u], acc[0]);
        acc[1] = VT ? mfma(xf[u], bf[u & 7][1], acc[1]) : mfma(bf[u & 7][1], xf[u], acc[1]);
        PIN(acc[0]); PIN(acc[1]); SBAR();
    };
    auto SQ = [&](int c) INL {
#pragma unroll
        for (int k = 0; k < 16; ++k) { const unsigned a = xf[4 * c + (k >> 2)][k & 3]; sx[k & 1] = dot2(a, ONES, sx[k & 1]); sxx[k & 1] = dot2(a, a, sxx[k & 1]); }
        PIN(sx[0]); PIN(sx[1]); PIN(sxx[0]); PIN(sxx[1]); SBAR();
    };
    sfor<7>([&](auto ic) INL {
        constexpr int c = ic.value;
        lds_bar();
        if (c == 0) ST(1);
#pragma unroll
        for (int i = 0; i < 4; ++i) { if (c > 0 && c < 6) MM(4 * c - 4 + i); RD(4 * c + i); }
        SQ(c);
        if constexpr (c >= 5) { MM(4 * c); MM(4 * c + 1); MM(4 * c + 2); MM(4 * c + 3); }
    });
    ST(2);
    const float mu = halfsum(sx[0] + sx[1]) * (1.0f / D);
    const float var = fmaxf(halfsum(sxx[0] + sxx[1]) * (1.0f / D) - mu * mu, 0.f);
    const float rstd = __builtin_amdgcn_rsqf(var + EPS);
    LAS float* ex = (LAS float*)(L + P1_EX + w * 256);
    LAS float* cs = (LAS float*)(L + P1_EX + 1024);
    const int tile = rb * 4 + w;
    if (VT) ex[lane] = hh ? rstd : -mu * rstd;
    lds_bar();
    if (VT) {
        f32x4 a4[4], b4[4];
#pragma unroll
        for (int i = 0; i < 4; ++i) { a4[i] = *(LAS f32x4*)(ex + 8 * i + 4 * hh); b4[i] = *(LAS f32x4*)(ex + 32 + 8 * i + 4 * hh); }
#pragma unroll
        for (int t = 0; t < 2; ++t) {
            const float c = cs[32 * t + r];
            f32x16 v;
#pragma unroll
            for (int k = 0; k < 16; ++k) v[k] = fmaf(acc[t][k], b4[k >> 2][k & 3], a4[k >> 2][k & 3] * c);
#pragma unroll
            for (int s = 0; s < 2; ++s) {
                u32x4 o = pack8(v, s);
                if (r >= 28) { const unsigned z = r == 28 ? ONES : 0u; o = (u32x4){z, z, z, z}; }
                st128<SC1>(o, rws, lane * 16, WS_KV + ((2 * pair + t) * 64 + tile) * 4096 + (2 + s) * 1024);
            }
        }
    } else {
        const int rp = (r & 19) | ((r & 4) << 1) | ((r & 8) >> 1);
        const float nm = -mu * rstd;
#pragma unroll
        for (int t = 0; t < 2; ++t) {
            f32x16 v;
#pragma unroll
            for (int i = 0; i < 4; ++i) {
                const f32x4 c4 = *(LAS f32x4*)(cs + 32 * t + 8 * i + 4 * hh);
#pragma unroll
                for (int k = 0; k < 4; ++k) v[4 * i + k] = fmaf(acc[t][4 * i + k], rstd, nm * c4[k]);
            }
#pragma unroll
            for (int s = 0; s < 2; ++s) {
                u32x4 o = pack8(v, s);
                if (s == 1 && hh) { o[2] = 0u; o[3] = 0u; }
                st128<SC1>(o, rws, (rp + 32 * hh) * 16, WS_KV + ((2 * pair + t) * 64 + tile) * 4096 + s * 1024);
            }
        }
    }
    ST(3);
    lds_bar();
    LAS char* wq = L + P1_WQ + (8 * hh + ((lane & 15) >> 2)) * 64 + ((head & 1) + 4 * ((lane >> 4) & 1) + (lane & 3)) * 8;
    PIN(wq);
    f32x16 aq = {}, cq = {};
    u32x4 bq[4];
    bq[0] = wfrag<256>(wq); bq[1] = wfrag<256>(wq + 1024); bq[2] = wfrag<256>(wq + 2048);
    sfor<28>([&](auto ic) INL {
        constexpr int u = ic.value;
        if constexpr (u + 3 < 28) bq[(u + 3) & 3] = wfrag<256>(wq + (u + 3) * 1024);
        SBAR();
        aq = mfma(bq[u & 3], xf[u], aq);
        cq = mfma(bq[u & 3], ones, cq);
        PIN(aq); PIN(cq); SBAR();
        if constexpr (u == 11) { vm_wait<0>(); publish(L + L_SYNC1, rws, WS_F1 + ((pair * 16 + rb) * 2 + (VT ? 1 : 0)) * 128, tag, lane); SBAR(); }
    });
    const float qs = rstd * (0.18898223650461362f * 1.4426950408889634f), nmq = -mu * qs;
    f32x16 v;
#pragma unroll
    for (int k = 0; k < 16; ++k) v[k] = fmaf(aq[k], qs, nmq * cq[k]);
#pragma unroll
    for (int s = 0; s < 2; ++s) {
        u32x4 o = pack8(v, s);
        if (s == 1 && hh) { o[2] = 0u; o[3] = 0u; }
        *(LAS u32x4*)(L + L_QF + (w * 2 + s) * 1024 + lane * 16) = o;
    }
}

__device__ __forceinline__ void p1_loader(const Params& p, int lw, int lane, int rb, int pair, int kind, int head, int rot, unsigned& tag) {
    const rsrc_t rx = mkrs(p.x, S * D * 2), rw = mkrs(p.wqkv, D * 3 * D * 2);
    unsigned vx[2];
#pragma unroll
    for (int i = 0; i < 2; ++i) vx[i] = (lane >> 3) * 896 + (((lane & 7) ^ ((4 * i + (lane >> 4)) & 7)) << 4);
    int piece = (lane & 7) ^ (((lane >> 4) & 1) << 2);
    if (piece == 7) piece = 6;
    const unsigned vw = (lane >> 3) * 2688 + piece * 16;
    const unsigned xb = (rb * 128 + lw * 64) * 896, wb = lw * 32 * 2688 + (448 * (1 + kind) + 56 * pair) * 2;
    auto chunk = [&](int c) INL {
        const int cc = mod7(c + rot);
#pragma unroll
        for (int i = 0; i < 4; ++i) dma16(rw, vw, wb + (cc * 64 + i * 8) * 2688, P1_WS + (c * 64 + lw * 32 + i * 8) * 128);
#pragma unroll
        for (int i = 0; i < 8; ++i) dma16(rx, vx[i & 1], xb + i * 8 * 896 + cc * 128, P1_XS + (c & 3) * 16384 + lw * 8192 + i * 1024);
    };
    const unsigned vq = (lane >> 2) * 2688 + (lane & 3) * 16;
    const unsigned qb = 56 * head - 8 * (head & 1) + lw * 32 * 2688;
    auto wq = [&](int c) INL {
#pragma unroll
        for (int i = 0; i < 2; ++i) dma16(rw, vq, (mod7(c + rot) * 64 + i * 16) * 2688 + qb, P1_WQ + (c * 64 + lw * 32 + i * 16) * 64);
    };
    chunk(0); chunk(1);
#pragma unroll
    for (int c = 0; c < 5; ++c) { chunk(c + 2); wait_bar<24>(); }
    wq(0); wq(1); wq(2);
    wait_bar<18>();
    wq(3); wq(4);
    wait_bar<10>();
    wq(5); wq(6);
    tag = load_tag(p);
    bar();
    wait_bar<0>();
}

__device__ __forceinline__ void p1_colsum(LAS char* L, int t, int lane) {
    LAS char* wb = L + p1_wrd(lane, t);
    PIN(wb);
    ((LAS unsigned*)(L + L_SYNC1))[t] = 0u;
    float c2[4] = {};
#pragma unroll
    for (int c = 0; c < 7; ++c) {
        lds_bar();
#pragma unroll
        for (int uu = 0; uu < 4; ++uu) {
            const u32x4 b = wfrag<512>(wb + (4 * c + uu) * 2048);
#pragma unroll
            for (int j = 0; j < 4; ++j) { c2[2 * (j & 1)] = sadd(c2[2 * (j & 1)], bflo(b[j])); c2[2 * (j & 1) + 1] = sadd(c2[2 * (j & 1) + 1], bfhi(b[j])); }
        }
        PIN(c2[0]); PIN(c2[1]); PIN(c2[2]); PIN(c2[3]);
    }
    ((LAS float*)(L + P1_EX + 1024))[32 * t + (lane & 31)] = halfsum((c2[0] + c2[1]) + (c2[2] + c2[3]));
    lds_bar();
    lds_bar();
}

__device__ __forceinline__ f32x16 pv(u32x4 v, f32x8 pr, f32x16 o) {
    return __builtin_amdgcn_smfmac_f32_32x32x32_bf16(__builtin_bit_cast(bf8, v), __builtin_bit_cast(bf16v, pr), o, 0xDDDD, 0, 0);
}
__device__ __forceinline__ void mfma0(f32x16& s, u32x4 a, u32x4 b) { asm volatile("v_mfma_f32_32x32x16_bf16 %0, %1, %2, 0" : "+v"(s) : "v"(a), "v"(b)); }
__device__ __forceinline__ f32x8 lo8(const f32x16& s) { return __builtin_shufflevector(s, s, 0, 1, 2, 3, 4, 5, 6, 7); }
__device__ __forceinline__ f32x8 hi8(const f32x16& s) { return __builtin_shufflevector(s, s, 8, 9, 10, 11, 12, 13, 14, 15); }
__device__ __forceinline__ unsigned slab_voff(int lane, int pitch) { return (lane >> 3) * pitch + (((lane & 7) ^ (((lane >> 4) & 1) << 2)) << 4); }
__device__ __forceinline__ int slab_rd(int lane, int t) {
    const int q = (lane & 15) >> 2, pp = lane & 3, g1 = (lane >> 4) & 1, hh = lane >> 5;
    return (4 * hh + q) * 128 + (((8 * t + 4 * g1 + pp) ^ ((q >> 1) << 3)) << 3);
}
__device__ __forceinline__ f32x16 redo_o(rsrc_t rws, int lane, int head, const u32x4 (&qf)[2]) {
    float m = -INFINITY;
#pragma unroll 1
    for (int t = 0; t < 64; ++t) {
        const unsigned b = WS_KV + (head * 64 + t) * 4096;
        f32x16 s = {};
        s = mfma(ldh(rws, lane * 16, b), qf[0], s);
        s = mfma(ldh(rws, lane * 16 + 1024, b), qf[1], s);
#pragma unroll
        for (int k = 0; k < 16; ++k) m = fmaxf(m, s[k]);
    }
    auto mm = __builtin_amdgcn_permlane32_swap(__float_as_uint(m), __float_as_uint(m), false, false);
    m = fmaxf(__uint_as_float(mm[0]), __uint_as_float(mm[1]));
    f32x16 o = {};
#pragma unroll 1
    for (int t = 0; t < 64; ++t) {
        const unsigned b = WS_KV + (head * 64 + t) * 4096;
        f32x16 s = {};
        s = mfma(ldh(rws, lane * 16, b), qf[0], s);
        s = mfma(ldh(rws, lane * 16 + 1024, b), qf[1], s);
#pragma unroll
        for (int k = 0; k < 16; ++k) s[k] = __builtin_amdgcn_exp2f(s[k] - m);
        o = pv(ldh(rws, lane * 16 + 2048, b), lo8(s), o);
        o = pv(ldh(rws, lane * 16 + 3072, b), hi8(s), o);
    }
    return o;
}

#define W1DMA(hf, u, e) do { int uu_ = (u) + 2 * ct1; if (uu_ >= 28) uu_ -= 28; dma16(r1, v1, (uu_ * 16 + (e) * 8) * 3584 + ct1 * 256 + (hf) * 128, L_W1 + (hf) * SLAB + (u) * 2048 + (e) * 1024); } while (0)

__host__ __device__ constexpr int step_q(int n) { return n < 52 ? (n & 3) : (n - 52) / 3; }
__host__ __device__ constexpr int step_k(int n) { return n < 52 ? (n >> 2) : 13 + (n - 52) % 3; }

__device__ __forceinline__ void attn_compute(LAS char* L, rsrc_t rws, int w, int lane, int head ST_ARG) {
    u32x4 kf[5][2], vf[5][2];
    const unsigned kvb = WS_KV + (head * 64 + w * 16) * 4096;
    auto ld = [&](int t, int s) INL { (s < 2 ? kf : vf)[t % 5][s & 1] = ldh(rws, lane * 16 + s * 1024, kvb + t * 4096); SBAR(); };
    f32x16 o[4], sc[3];
    { float z = 0.f; PIN(z);
#pragma unroll
      for (int k = 0; k < 64; ++k) o[k >> 4][k & 15] = z;
      sc[0] = o[0]; sc[1] = o[0]; sc[2] = o[0];
      PIN(o[0]); PIN(o[1]); PIN(o[2]); PIN(o[3]); PIN(sc[0]); PIN(sc[1]); PIN(sc[2]); }
    __builtin_amdgcn_s_setprio(1);
    lds_bar();
    ST(5);
#pragma unroll
    for (int k = 0; k < 10; ++k) ld(k >> 2, k & 3);
    u32x4 qf[4][2];
#pragma unroll
    for (int k = 0; k < 8; ++k) qf[k >> 1][k & 1] = ldsr128(L + L_QF + k * 1024 + lane * 16);
    mfma0(sc[0], kf[0][0], qf[0][0]);
    sc[0] = mfma(kf[0][1], qf[0][1], sc[0]);
    LAS char* pw = L + L_P + w * 4096 + lane * 16;
    ST(6);
#define EXP4(X, i) do { X[i] = __builtin_amdgcn_exp2f(X[i]); X[i + 1] = __builtin_amdgcn_exp2f(X[i + 1]); X[i + 2] = __builtin_amdgcn_exp2f(X[i + 2]); X[i + 3] = __builtin_amdgcn_exp2f(X[i + 3]); PIN(X); SBAR(); } while (0)
    auto step = [&](auto ic) INL {
        constexpr int n = ic.value, qn = step_q(n + 1), kn = step_k(n + 1) % 5, qp = step_q(n > 0 ? n - 1 : 0), kp = step_k(n > 0 ? n - 1 : 0) % 5;
        f32x16 &sn = sc[(n + 1) % 3], &s = sc[n % 3], &sp = sc[(n + 2) % 3];
        if constexpr (n + 1 < 64) { mfma0(sn, kf[kn][0], qf[qn][0]); SBAR(); }
        if constexpr (n < 64) EXP4(s, 0);
        if constexpr (n + 1 < 64) { sn = mfma(kf[kn][1], qf[qn][1], sn); PIN(sn); SBAR(); }
        if constexpr (n < 64) EXP4(s, 4);
        if constexpr (n > 0) { o[qp] = pv(vf[kp][0], lo8(sp), o[qp]); PIN(o[qp]); SBAR(); }
        if constexpr (n < 64) EXP4(s, 8);
        if constexpr (n > 0) { o[qp] = pv(vf[kp][1], hi8(sp), o[qp]); PIN(o[qp]); SBAR(); }
        constexpr int issued = 10 + n + (n < 8 ? n : 8) < 64 ? 10 + n + (n < 8 ? n : 8) : 64;
        constexpr int nk = 4 * step_k(n + 2 < 64 ? n + 2 : 63) + 1, nv = 4 * step_k(n < 64 ? n : 63) + 3, need = nk > nv ? nk : nv, left = issued - 1 - need;
        if constexpr (n < 55) { __builtin_amdgcn_s_waitcnt(0x0F70 | (left & 15) | ((left >> 4) << 14)); SBAR(); }
        if constexpr (n < 64) { s[12] = __builtin_amdgcn_exp2f(s[12]); PIN(s); SBAR(); }
        if constexpr (issued < 64) ld(issued >> 2, issued & 3);
        if constexpr (n < 8) ld((issued + 1) >> 2, (issued + 1) & 3);
        if constexpr (n < 64) { s[13] = __builtin_amdgcn_exp2f(s[13]); s[14] = __builtin_amdgcn_exp2f(s[14]); s[15] = __builtin_amdgcn_exp2f(s[15]); PIN(s); SBAR(); }
        if constexpr (n == 56 || n == 59 || n == 62 || n == 64) {
            constexpr int qt = n == 64 ? 3 : (n - 56) / 3;
#pragma unroll
            for (int i = 0; i < 4; ++i) *(LAS f32x4*)(pw + qt * 16384 + i * 1024) = (f32x4){o[qt][4 * i], o[qt][4 * i + 1], o[qt][4 * i + 2], o[qt][4 * i + 3]};
            SBAR();
        }
    };
    sfor<65>(step);
    __builtin_amdgcn_s_setprio(0);
}

__device__ __forceinline__ int fc2_kstep(int p, int rot) { return mod7((p % 14) / 2 + rot) * 8 + (p / 14) * 2 + (p & 1); }
__device__ __forceinline__ int fc2_rot(int j) { return (j % 7 + 2 * (j / 7)) % 7; }
__device__ __forceinline__ unsigned w2src(int c, int hw, int i, int j) {
    const int ks = (hw & 1) ? fc2_kstep(4 * c + 2 + (i >> 1), fc2_rot(j)) : fc2_kstep(4 * c + (i >> 1), fc2_rot(j));
    return (((hw >> 1) * 56 + ks) * 16 + (i & 1) * 8) * 896 + (j % 7) * 128;
}
__device__ __forceinline__ void attn_helper(const Params& p, rsrc_t rws, int hw, int lane, int head, int j, unsigned tag, u32x4 (&sg)[9][4]) {
    const rsrc_t rw = mkrs(p.wo, D * D * 2), r1 = mkrs(p.w1, D * F * 2), r2 = mkrs(p.w2, F * D * 2);
    const unsigned vt = lane < 49 ? lane * 128 : 0x7FFFFF00u, land = L_W1 + hw * 1024;
    dma4(r1, vt, (j * 4 + hw) * 49 * 128, land);
    if (hw == 3) { __builtin_amdgcn_s_sleep(22); wait_word(rws, WS_F1 + ((head >> 1) * 32 + (lane & 31)) * 128, tag); }
    bar();
    __builtin_amdgcn_s_sleep(32);
    dma4(r1, vt, (128 + j * 4 + hw) * 49 * 128, land);
    vm_wait<0>();
    const unsigned v2 = slab_voff(lane, 896), v1 = slab_voff(lane, 3584);
    const int ct = j % 7, ct1 = j % 14;
#pragma unroll
    for (int i = 0; i < 14; ++i) {
        int ii = i + 4 * (j / 7); if (ii >= 14) ii -= 14; if (ii >= 14) ii -= 14;
        const int pos = ii * 4 + hw, kh = pos >= 28;
        int pp = pos - 28 * kh + 7 * (ct & 3); if (pp >= 28) pp -= 28;
        dma16(rw, v2, (kh * 28 + pp) * 8 * 896 + ct * 128, L_WO + pos * 1024);
        vm_wait<4>();
    }
#pragma unroll
    for (int k = 0; k < 36; ++k) { sg[k >> 2][k & 3] = __builtin_amdgcn_raw_buffer_load_b128(r2, v2, w2src(5 + (k >> 2), hw, k & 3, j), 0); vm_wait<4>(); }
#pragma unroll
    for (int i = 0; i < 8; ++i) { W1DMA(0, i * 2 + (hw >> 1), hw & 1); vm_wait<4>(); }
}

__device__ __forceinline__ void attn_finish(LAS char* L, rsrc_t rws, int w, int lane, int rb, int head, unsigned tag ST_ARG) {
    const int qt = w >> 1, s = w & 1;
    LAS char* P = L + L_P + qt * 16384 + s * 2048 + lane * 16;
    LAS char* PL = L + L_P + qt * 16384 + 3 * 1024 + (32 + (lane & 31)) * 16;
    unsigned dst = WS_O + (((rb * 4 + qt) * 16 + head) * 2 + s) * 1024 + lane * 16;
    PIN(P); PIN(PL); PIN(dst);
    ST(7);
    wait_bar<0>();
    ST(8);
    f32x4 a[4][2];
    float lp[4];
#pragma unroll
    for (int ww = 0; ww < 4; ++ww) lp[ww] = *(LAS float*)(PL + ww * 4096);
#pragma unroll
    for (int ww = 0; ww < 4; ++ww) { a[ww][0] = *(LAS f32x4*)(P + ww * 4096); a[ww][1] = *(LAS f32x4*)(P + ww * 4096 + 1024); }
    SBAR();
    float l = (lp[0] + lp[1]) + (lp[2] + lp[3]);
    f32x4 x = (a[0][0] + a[1][0]) + (a[2][0] + a[3][0]), y = (a[0][1] + a[1][1]) + (a[2][1] + a[3][1]);
    const float asum = ((fabsf(x[0]) + fabsf(x[1])) + (fabsf(x[2]) + fabsf(x[3]))) + ((fabsf(y[0]) + fabsf(y[1])) + (fabsf(y[2]) + fabsf(y[3])));
    if (__builtin_expect(!__all(l >= 0x1p-40f && l <= 0x1p40f && asum <= 0x1p120f), 0)) {
        const u32x4 qf[2] = {ldsr128(L + L_QF + qt * 2048 + lane * 16), ldsr128(L + L_QF + qt * 2048 + 1024 + lane * 16)};
        const f32x16 o = redo_o(rws, lane, head, qf);
        l = __uint_as_float(__builtin_amdgcn_permlane32_swap(__float_as_uint(o[12]), __float_as_uint(o[12]), false, false)[1]);
#pragma unroll
        for (int k = 0; k < 4; ++k) { x[k] = s ? o[8 + k] : o[k]; y[k] = s ? o[12 + k] : o[4 + k]; }
    }
    const float il = __builtin_amdgcn_rcpf(l);
    u32x4 v = {cvtpk(x[0] * il, x[1] * il), cvtpk(x[2] * il, x[3] * il), cvtpk(y[0] * il, y[1] * il), cvtpk(y[2] * il, y[3] * il)};
    if (s == 1 && lane >= 32) { v[2] = 0u; v[3] = 0u; }
    st128<SC1>(v, rws, dst, 0);
    ST(24);
}

__device__ __forceinline__ unsigned bfsplit(float v, float& rest) { const unsigned h = cvtpk(v, v) & 0xFFFFu; rest = v - bflo(h); return h; }

__device__ __forceinline__ void mlp_helper(const Params& p, LAS char* L, rsrc_t rws, int hw, int lane, int xcd, int j, unsigned tag, const u32x4 (&sg)[9][4]) {
    const int r = lane & 31, hh = lane >> 5, ct1 = j % 14, ct = j % 7;
    const int b4 = xcd * 4 + j / 7, b2 = xcd * 2 + j / 14;
    const rsrc_t r1 = mkrs(p.w1, D * F * 2), r2 = mkrs(p.w2, F * D * 2);
    const unsigned v1 = slab_voff(lane, 3584), v2 = slab_voff(lane, 896);
    unsigned bb[2];
#pragma unroll
    for (int hf = 0; hf < 2; ++hf) bb[hf] = hh ? 0u : ((const unsigned short*)p.b1)[ct1 * 128 + hf * 64 + (hw & 1) * 32 + r];
    auto colsum = [&](int hf) INL {
        float c2[4] = {};
        LAS char* wb = L + L_W1 + hf * SLAB + slab_rd(lane, hw & 1) + (hw >> 1) * 14 * 2048;
        PIN(wb);
#pragma unroll
        for (int u = 0; u < 14; ++u) {
            const u32x4 f = wfrag<1024>(wb + u * 2048);
#pragma unroll
            for (int k = 0; k < 4; ++k) {
                if (hf == 0) c2[k] = dot2(f[k], ONES, c2[k]);
                else { c2[2 * (k & 1)] = sadd(c2[2 * (k & 1)], bflo(f[k])); c2[2 * (k & 1) + 1] = sadd(c2[2 * (k & 1) + 1], bfhi(f[k])); }
            }
        }
        float lo, lo2;
        const unsigned ch = bfsplit((c2[0] + c2[1]) + (c2[2] + c2[3]), lo), cl = bfsplit(lo, lo2);
        *(LAS u32x2*)(L + L_EXT + (2 * hf + (hw & 1)) * 1024 + lane * 16 + (hw >> 1) * 8) = (u32x2){ch | (ch << 16), cl | (bb[hf] << 16)};
    };
    auto w2dma = [&](int c) INL {
#pragma unroll
        for (int i = 0; i < 4; ++i) dma16(r2, v2, w2src(c, hw, i, j), w2slot(c) + hw * 4096 + i * 1024);
    };
    auto w2st = [&](int c) INL {
#pragma unroll
        for (int i = 0; i < 4; ++i) *(LAS u32x4*)(L + w2slot(c) + hw * 4096 + i * 1024 + lane * 16) = sg[c - 5][i];
        SBAR();
    };
    if (hw == 3) { __builtin_amdgcn_s_sleep(8); wait_word(rws, WS_F2 + ((b4 >> 1) * 16 + (lane & 15)) * 128, tag); }
    else if (hw < 2) {
#pragma unroll
        for (int u = 16; u < 28; ++u) W1DMA(0, u, hw);
#pragma unroll
        for (int u = 0; u < 12; ++u) W1DMA(1, u, hw);
    }
    bar();
    bar();
    wait_bar<0>();
    if (hw == 3) set_flag(rws, WS_F3 + (b4 * 8 + ct) * 128, tag, lane == 0);
    if (hw == 2) ((LAS unsigned*)(L + L_SYNC4))[lane] = 0u;
    if (hw < 2) {
#pragma unroll
        for (int u = 12; u < 28; ++u) W1DMA(1, u, hw);
    }
    colsum(0);
    if (hw == 3) { const int l = lane < 14 ? lane : 0; wait_word(rws, WS_F3 + ((b2 * 2 + (l >= 7)) * 8 + mod7(l)) * 128, tag); }
    wait_bar<0>();
    colsum(1);
    lds_bar();
    w2dma(0); w2dma(1); w2dma(2); w2dma(3); w2dma(4);
    bar();
    w2st(5); w2st(6); w2st(7); w2st(8);
    const unsigned f4 = WS_F4 + ((b4 >> 1) * 16 + (lane < 14 ? lane : 0)) * 128;
    if (hw == 3) wait_word(rws, f4, tag);
    wait_bar<0>();
    if (hw == 3) wait_word(rws, f4 + 256 * 128, tag);
    bar();
    w2st(9); w2st(10); w2st(11); w2st(12);
    if (hw == 3) wait_word(rws, f4 + 512 * 128, tag);
    lds_bar();
    w2st(13);
    lds_bar();
    bar();
}

__device__ __forceinline__ void mlp_compute(const Params& p, LAS char* L, rsrc_t rws, int w, int lane, int xcd, int j, unsigned tag ST_ARG) {
    const int r = lane & 31, hh = lane >> 5;
    const int r4 = j / 7, ct = j % 7, m = w & 1, kh = w >> 1;
    const int rt = xcd * 8 + r4 * 2 + m;
    const int sr[2] = {slab_rd(lane, 0), slab_rd(lane, 1)};
    u32x4 hpk[2];
    u32x4 bf[4][2];
    {
        u32x4 wf[16][2];
        LAS char* wo[2][2];
#pragma unroll
        for (int t = 0; t < 2; ++t) { wo[t][0] = L + L_WO + sr[t] + kh * 224 * 128; wo[t][1] = wo[t][0] - hh * 512; PIN(wo[t][0]); PIN(wo[t][1]); }
#pragma unroll
        for (int u = 0; u < 16; ++u)
#pragma unroll
            for (int t = 0; t < 2; ++t) {
                const int row = (u >> 1) * 28 + (u & 1) * 16;
                const u32x2 a = trread(wo[t][0] + row * 128), b = trread(wo[t][u & 1] + (row + 8) * 128);
                wf[u][t] = (u32x4){a[0], a[1], b[0], b[1]};
            }
        wait_bar<0>();
        ST(9);
        const rsrc_t rx = mkrs(p.x, S * D * 2);
        u32x2 xr[4];
#pragma unroll
        for (int i = 0; i < 4; ++i) xr[i] = __builtin_amdgcn_raw_buffer_load_b64(rx, r * 896 + hh * 8 + i * 16, rt * 32 * 896 + ct * 128 + kh * 64, 0);
        bar();
        ST(10);
        u32x4 af[16];
#pragma unroll
        for (int u = 0; u < 16; ++u) af[u] = ldh(rws, lane * 16, WS_O + (rt * 32 + kh * 16 + ((u + 4 * ct) & 15)) * 1024);
        SBAR();
        f32x16 acc[2] = {};
#pragma unroll
        for (int u = 0; u < 16; ++u) { acc[0] = mfma(wf[u][0], af[u], acc[0]); acc[1] = mfma(wf[u][1], af[u], acc[1]); }
        const f32x16 snd = kh ? acc[0] : acc[1];
        f32x16 hv = kh ? acc[1] : acc[0];
#pragma unroll
        for (int i = 0; i < 4; ++i) *(LAS f32x4*)(L + L_RED1 + (w * 4 + i) * 1024 + lane * 16) = (f32x4){snd[4 * i], snd[4 * i + 1], snd[4 * i + 2], snd[4 * i + 3]};
        ST(11);
        lds_bar();
#pragma unroll
        for (int i = 0; i < 4; ++i) {
            const f32x4 o = *(LAS f32x4*)(L + L_RED1 + ((w ^ 2) * 4 + i) * 1024 + lane * 16);
            hv[4 * i] += o[0] + bflo(xr[i][0]); hv[4 * i + 1] += o[1] + bfhi(xr[i][0]);
            hv[4 * i + 2] += o[2] + bflo(xr[i][1]); hv[4 * i + 3] += o[3] + bfhi(xr[i][1]);
        }
#pragma unroll
        for (int s = 0; s < 2; ++s) { hpk[s] = pack8(hv, s); st128<SC1>(hpk[s], rws, lane * 16, WS_H + (rt * 28 + ct * 4 + kh * 2 + s) * 1024); }
        float s1 = 0.f, s2 = 0.f;
#pragma unroll
        for (int k = 0; k < 8; ++k) { const unsigned u = hpk[k >> 2][k & 3]; s1 = dot2(u, ONES, s1); s2 = dot2(u, u, s2); }
        __builtin_amdgcn_raw_buffer_store_b64((u32x2){__float_as_uint(halfsum(s1)), __float_as_uint(halfsum(s2))}, rws,
                                              hh ? 0x7FFFFFF0u : WS_HS + ((rt * 14 + ct * 2 + kh) * 32 + r) * 8, 0, SC1);
        wait_bar<0>();
        ST(12);
    }
    {
        const int ct1 = j % 14, rt1 = xcd * 8 + (j / 14) * 4 + w;
        auto RD = [&](int u) INL {
#pragma unroll
            for (int t = 0; t < 2; ++t) bf[u & 3][t] = wfrag<1024>(L + L_W1 + sr[t] + u * 2048);
            SBAR();
        };
        RD(0); RD(1); RD(2);
        bar();
        ST(13);
        u32x4 af[28];
        auto aload = [&](int u) INL { int uu = u + 2 * ct1; if (uu >= 28) uu -= 28; af[u] = ldh(rws, lane * 16, WS_H + (rt1 * 28 + uu) * 1024); };
        constexpr int WIN = 10;
#pragma unroll
        for (int u = 0; u < WIN; ++u) aload(u);
        SBAR();
        f32x16 acc[4] = {};
        u32x2 st[7];
        u32x4 b2f[4];
        LAS char* w1b[2] = {L + L_W1 + SLAB + sr[0], L + L_W1 + SLAB + sr[1]};
        PIN(w1b[0]); PIN(w1b[1]);
        auto RD2 = [&](int z) INL { b2f[z & 3] = wfrag<1024>(w1b[z / 28] + (z % 28) * 2048); SBAR(); };
        sfor<28>([&](auto ic) INL {
            constexpr int u = ic.value;
            if constexpr (u + 3 < 28) RD(u + 3); else RD2(u + 3 - 28);
            acc[0] = mfma(bf[u & 3][0], af[u], acc[0]); acc[1] = mfma(bf[u & 3][1], af[u], acc[1]);
            PIN(acc[0]); PIN(acc[1]); SBAR();
            if constexpr (u + WIN < 28) aload(u + WIN);
            else if constexpr (u + WIN < 35) st[u + WIN - 28] = __builtin_amdgcn_raw_buffer_load_b64(rws, (hh * 7 * 32 + r) * 8, WS_HS + (rt1 * 14 + u + WIN - 28) * 256, SC1);
            SBAR();
        });
        ST(14);
        bar();
        u32x4 ext[4];
#pragma unroll
        for (int t = 0; t < 4; ++t) ext[t] = ldsr128(L + L_EXT + t * 1024 + lane * 16);
        float s1 = 0.f, s2 = 0.f;
#pragma unroll
        for (int k = 0; k < 7; ++k) { s1 += __uint_as_float(st[k][0]); s2 += __uint_as_float(st[k][1]); }
        const float mu = halfsum(s1) * (1.0f / D);
        const float var = fmaxf(halfsum(s2) * (1.0f / D) - mu * mu, 0.f);
        const float r1 = __builtin_amdgcn_rsqf(var + EPS);
        const float rs = r1 * __builtin_amdgcn_rsqf(var * r1 * r1 + EPS), irs = __builtin_amdgcn_rcpf(rs);
        float t0, t1;
        const unsigned mh = bfsplit(-mu, t0), ml = bfsplit(t0, t1), ih = bfsplit(irs, t0), il = bfsplit(t0, t1);
        const u32x4 xe = {mh | (ml << 16), mh | (ih << 16), mh | (ml << 16), mh | (il << 16)};
        acc[0] = mfma(ext[0], xe, acc[0]); acc[1] = mfma(ext[1], xe, acc[1]);
        acc[2] = mfma(ext[2], xe, acc[2]); acc[3] = mfma(ext[3], xe, acc[3]);
        PIN(acc[0]); PIN(acc[1]); PIN(acc[2]); PIN(acc[3]); SBAR();
        constexpr float C0 = -2.0f * 0.7978845608028654f * 1.4426950408889634f, C1 = C0 * 0.044715f;
        const float k0 = C0 * rs, k1 = C1 * rs * rs * rs;
        f32x4 ga;
        auto gelu = [&](f32x16& v, int i, int stage) INL {
#pragma unroll
            for (int k = 0; k < 4; ++k) {
                const float a = v[4 * i + k];
                if (stage == 0) ga[k] = smul(a, sfma(smul(a, a), k1, k0));
                if (stage == 1) ga[k] = __builtin_amdgcn_exp2f(ga[k]);
                if (stage == 2) ga[k] = __builtin_amdgcn_rcpf(sfma(ga[k], irs, irs));
                if (stage == 3) v[4 * i + k] = smul(a, ga[k]);
            }
            if (stage < 3) PIN(ga); else PIN(v);
            SBAR();
        };
        auto put = [&](int t) INL {
#pragma unroll
            for (int s = 0; s < 2; ++s) st128<SC1>(pack8(acc[t], s), rws, lane * 16, WS_G + (rt1 * 112 + ct1 * 8 + t * 2 + s) * 1024);
        };
        auto flag = [&](int stage) INL { publish(L + L_SYNC4 + stage * 4, rws, WS_F4 + (stage * 256 + (xcd * 2 + j / 14) * 16 + ct1) * 128, tag, lane); };
        sfor<56>([&](auto ic) INL {
            constexpr int z = ic.value, t = 2 + z / 28;
            if constexpr (z + 3 < 56) RD2(z + 3);
            acc[t] = mfma(b2f[z & 3], af[z % 28], acc[t]); PIN(acc[t]); SBAR();
            constexpr int e = z - 4;
            if constexpr (e >= 0 && e < 48) { gelu(acc[e >> 4], (e >> 2) & 3, e & 3); if constexpr ((e & 15) == 15) { put(e >> 4); SBAR(); } }
            if constexpr (z == 47) { vm_wait<0>(); flag(0); SBAR(); }
        });
        ST(15);
        bar();
#pragma unroll
        for (int k = 0; k < 16; ++k) {
            const float a = acc[3][k];
            acc[3][k] = a * __builtin_amdgcn_rcpf(fmaf(__builtin_amdgcn_exp2f(a * fmaf(a * a, k1, k0)), irs, irs));
        }
        put(3);
        vm_wait<2>(); flag(1);
        ST(16);
        bar();
        ST(17);
        constexpr int WN2 = 10;
        u32x4 ag[WN2];
        const int rot2 = fc2_rot(j);
        const unsigned abase = WS_G + (rt * 112 + kh * 56) * 1024;
#pragma unroll
        for (int u = 0; u < WN2; ++u) ag[u] = ldh(rws, lane * 16, abase + fc2_kstep(u, rot2) * 1024);
        SBAR();
        vm_wait<WN2>(); flag(2);
        LAS char* w2b[3][2];
#pragma unroll
        for (int k = 0; k < 6; ++k) { w2b[k >> 1][k & 1] = L + sr[k & 1] + kh * 8192 + (k >> 1) * 48 * K1; PIN(w2b[k >> 1][k & 1]); }
        auto RD3 = [&](int u) INL {
#pragma unroll
            for (int t = 0; t < 2; ++t) bf[u & 3][t] = wfrag<1024>(w2b[w2slot(u >> 2) / (48 * K1)][t] + w2slot(u >> 2) % (48 * K1) + (u & 3) * 2048);
            SBAR();
        };
        RD3(0); RD3(1); RD3(2);
        ST(18);
        f32x16 a2[2] = {};
        u32x2 bb[4];
        sfor<56>([&](auto ic) INL {
            constexpr int u = ic.value;
            if constexpr (u == 16 || u == 32 || u == 44) bar();
            if constexpr (u == 16) ST(19);
            if constexpr (u + 3 < 56) RD3(u + 3);
            a2[0] = mfma(bf[u & 3][0], ag[u % WN2], a2[0]); a2[1] = mfma(bf[u & 3][1], ag[u % WN2], a2[1]);
            PIN(a2[0]); PIN(a2[1]); SBAR();
            if constexpr (u + WN2 < 56) ag[u % WN2] = ldh(rws, lane * 16, abase + fc2_kstep(u + WN2, rot2) * 1024);
            else if constexpr (u + WN2 < 60) bb[u + WN2 - 56] = __builtin_amdgcn_raw_buffer_load_b64(mkrs(p.b2, D * 2), hh * 8 + (u + WN2 - 56) * 16, ct * 128 + kh * 64, 0);
            SBAR();
        });
        ST(20);
        const f32x16 snd = kh ? a2[0] : a2[1];
        f32x16 v = kh ? a2[1] : a2[0];
#pragma unroll
        for (int i = 0; i < 4; ++i) *(LAS f32x4*)(L + L_RED2 + (w * 4 + i) * 1024 + lane * 16) = (f32x4){snd[4 * i], snd[4 * i + 1], snd[4 * i + 2], snd[4 * i + 3]};
        lds_bar();
        unsigned o[8];
#pragma unroll
        for (int i = 0; i < 4; ++i) {
            const f32x4 t = *(LAS f32x4*)(L + L_RED2 + ((w ^ 2) * 4 + i) * 1024 + lane * 16);
            const unsigned h0 = hpk[i >> 1][2 * (i & 1)], h1 = hpk[i >> 1][2 * (i & 1) + 1];
            o[2 * i] = cvtpk(v[4 * i] + t[0] + bflo(bb[i][0]) + bflo(h0), v[4 * i + 1] + t[1] + bfhi(bb[i][0]) + bfhi(h0));
            o[2 * i + 1] = cvtpk(v[4 * i + 2] + t[2] + bflo(bb[i][1]) + bflo(h1), v[4 * i + 3] + t[3] + bfhi(bb[i][1]) + bfhi(h1));
        }
        const rsrc_t ro = mkrs(p.out, S * D * 2);
#pragma unroll
        for (int k = 0; k < 2; ++k) {
            auto s0 = __builtin_amdgcn_permlane32_swap(o[4 * k], o[4 * k + 2], false, false);
            auto s1_ = __builtin_amdgcn_permlane32_swap(o[4 * k + 1], o[4 * k + 3], false, false);
            st128<SC1>((u32x4){s0[0], s1_[0], s0[1], s1_[1]}, ro, r * 896 + hh * 16 + k * 32, rt * 32 * 896 + ct * 128 + kh * 64);
        }
        ST(21);
    }
}

__global__ void __launch_bounds__(NT, 2) k_block(Params p) {
    extern __shared__ __attribute__((aligned(16))) char smem[];
    LAS char* L = (LAS char*)smem;
    ST_DECL
    ST(0);
    const int lane = threadIdx.x & 63, w = __builtin_amdgcn_readfirstlane(threadIdx.x >> 6);
    const int b = blockIdx.x, xcd = b & 7, j = b >> 3;
    const int rb = (xcd & 1) * 8 + (j & 7), sub = j >> 3, head = 4 * (xcd >> 1) + sub, pair = 2 * (xcd >> 1) + (sub >> 1), kind = sub & 1;
    const rsrc_t rws = mkrs(p.ws, WS_END);
    const int rot = ((j & 7) + 2 * sub) % 7;
    if (w == 7) {
        unsigned long long pc;
        asm volatile("s_getpc_b64 %0" : "=s"(pc));
        dma4(mkrs((const void*)pc, CODE_LEN), (lane * 8 + (j & 7)) * 128, 0, P1_EX + 1536);
    }
    unsigned tag;
    if (w < 4) {
        tag = load_tag(p);
        if (kind) p1_compute<true>(L, rws, w, lane, rb, pair, head, tag ST_PASS);
        else p1_compute<false>(L, rws, w, lane, rb, pair, head, tag ST_PASS);
        ST(4);
        attn_compute(L, rws, w, lane, head ST_PASS);
        attn_finish(L, rws, w, lane, rb, head, tag ST_PASS);
        if (j < 28) mlp_compute(p, L, rws, w, lane, xcd, j, tag ST_PASS);
        else wait_bar<0>();
    } else {
        if (w < 6) p1_loader(p, w - 4, lane, rb, pair, kind, head, rot, tag);
        else { tag = load_tag(p); p1_colsum(L, w - 6, lane); }
        ST(4);
        u32x4 sg[9][4];
        attn_helper(p, rws, w - 4, lane, head, j, tag, sg);
        attn_finish(L, rws, w, lane, rb, head, tag ST_PASS);
        wait_bar<0>();
        if (w == 7) set_flag(rws, WS_F2 + (rb * 16 + head) * 128, tag, lane == 0);
        ST(9);
        if (j < 28) mlp_helper(p, L, rws, w - 4, lane, xcd, j, tag, sg);
    }
    if (b == 0 && w == 0) set_flag(rws, WS_GEN, tag, lane == 0);
    ST_FLUSH();
}

__global__ void k_lead(unsigned long long* q) { if (threadIdx.x == 0) q[0] = __builtin_amdgcn_s_memrealtime(); }
extern "C" void kernel_launch(void* const* d_in, const int* in_sizes, int n_in, void* d_out, int out_size, void* d_ws, size_t ws_size, hipStream_t stream) {
    Params p;
    p.x = d_in[0]; p.wqkv = d_in[1]; p.wo = d_in[2]; p.w1 = d_in[4]; p.b1 = d_in[5]; p.w2 = d_in[6]; p.b2 = d_in[7];
    p.out = d_out; p.ws = d_ws;
    (void)hipFuncSetAttribute((const void*)k_block, hipFuncAttributeMaxDynamicSharedMemorySize, LDS_BYTES);
    k_lead<<<1, 64, 0, stream>>>((unsigned long long*)((char*)d_ws + 1024));
    k_block<<<NWG, NT, LDS_BYTES, stream>>>(p);
}
```

```cpp
#include <hip/hip_runtime.h>
#include <stdint.h>
#include <utility>

#define LAS __attribute__((address_space(3)))
#define GAS __attribute__((address_space(1)))
typedef __bf16 bf2 __attribute__((ext_vector_type(2)));
typedef __bf16 bf8 __attribute__((ext_vector_type(8)));
typedef __bf16 bf16v __attribute__((ext_vector_type(16)));
typedef float f32x4 __attribute__((ext_vector_type(4)));
typedef float f32x8 __attribute__((ext_vector_type(8)));
typedef float f32x16 __attribute__((ext_vector_type(16)));
typedef unsigned u32x2 __attribute__((ext_vector_type(2)));
typedef unsigned u32x4 __attribute__((ext_vector_type(4)));
typedef short s16x4 __attribute__((ext_vector_type(4)));
typedef __amdgpu_buffer_rsrc_t rsrc_t;

constexpr int S = 2048, D = 448, H = 16, F = 1792;
constexpr int NT = 512, NWG = 256;
constexpr float EPS = 1e-5f;
constexpr int LDS_BYTES = 160 * 1024;
constexpr int SC1 = 16;
constexpr int OAUX = 2, NE = 8, TD0 = 8, TD = 3, T1 = 20, T2 = 28, NFILL = 13, QFILL = 0, HSL = 8, HD = 3, FILL_AT = 0;
constexpr unsigned CODE_LEN = 52356;
constexpr unsigned WS_GEN = 0;
constexpr unsigned WS_F1 = 4096;
constexpr unsigned WS_F2 = WS_F1 + 256 * 128;
constexpr unsigned WS_F3 = WS_F2 + 256 * 128;
constexpr unsigned WS_F4 = WS_F3 + 256 * 128;
constexpr unsigned WS_HS = WS_F4 + 768 * 128;
constexpr unsigned WS_XS = WS_HS + 64 * 14 * 256;
constexpr unsigned WS_CS = 512 * 1024;
constexpr unsigned WS_KV = 640 * 1024;
constexpr unsigned WS_O = WS_KV + 16 * 64 * 4096;
constexpr unsigned WS_H = WS_O + 64 * 32 * 1024;
constexpr unsigned WS_G = WS_H + 64 * 28 * 1024;
constexpr unsigned WS_END = WS_G + 64 * 112 * 1024;

struct Params {
    const void *x, *wqkv, *wo, *w1, *b1, *w2, *b2;
    void *out, *ws;
};

template <class Fn, int... I> __device__ __forceinline__ void sfor_(Fn&& f, std::integer_sequence<int, I...>) { (f(std::integral_constant<int, I>{}), ...); }
template <int N, class Fn> __device__ __forceinline__ void sfor(Fn&& f) { sfor_(f, std::make_integer_sequence<int, N>{}); }
#define INL __attribute__((always_inline))
__device__ __forceinline__ rsrc_t mkrs(const void* p, unsigned bytes) { return __builtin_amdgcn_make_buffer_rsrc((void*)p, 0, (int)bytes, 0x00020000); }
__device__ __forceinline__ void dma16(rsrc_t rs, unsigned voff, unsigned soff, unsigned lds) {
    asm volatile("s_mov_b32 m0, %0\n\ts_nop 0\n\tbuffer_load_dwordx4 %1, %2, %3 offen lds" ::"s"(lds), "v"(voff), "s"(rs), "s"(soff) : "memory");
}
__device__ __forceinline__ void dma4(rsrc_t rs, unsigned voff, unsigned soff, unsigned lds) {
    asm volatile("s_mov_b32 m0, %0\n\ts_nop 0\n\tbuffer_load_dword %1, %2, %3 offen lds" ::"s"(lds), "v"(voff), "s"(rs), "s"(soff) : "memory");
}
__device__ __forceinline__ void dma4_if0(int skip, rsrc_t rs, unsigned voff, unsigned soff, unsigned lds) {
    asm volatile("s_cmp_lg_u32 %4, 0\n\ts_cbranch_scc1 1f\n\ts_mov_b32 m0, %0\n\ts_nop 0\n\tbuffer_load_dword %1, %2, %3 offen lds\n1:" ::"s"(lds), "v"(voff), "s"(rs), "s"(soff), "s"(skip) : "memory", "scc");
}
template <int N> __device__ __forceinline__ void wait_bar() { asm volatile("s_waitcnt vmcnt(%0) lgkmcnt(0)\n\ts_barrier" ::"i"(N) : "memory"); }
template <int N> __device__ __forceinline__ void vm_wait() { asm volatile("s_waitcnt vmcnt(%0)" ::"i"(N) : "memory"); }
__device__ __forceinline__ void lds_bar() { asm volatile("s_waitcnt lgkmcnt(0)\n\ts_barrier" ::: "memory"); }
template <int VM, int LG> __device__ __forceinline__ void cwait() { __builtin_amdgcn_s_waitcnt((VM & 15) | 0x70 | (LG << 8) | ((VM >> 4) << 14)); __builtin_amdgcn_sched_barrier(0); }
__device__ __forceinline__ void bar() { asm volatile("s_barrier" ::: "memory"); }
__device__ __forceinline__ void lds_wait() { asm volatile("s_waitcnt lgkmcnt(0)" ::: "memory"); }
__device__ __forceinline__ f32x16 mfma(u32x4 a, u32x4 b, f32x16 c) {
    return __builtin_amdgcn_mfma_f32_32x32x16_bf16(__builtin_bit_cast(bf8, a), __builtin_bit_cast(bf8, b), c, 0, 0, 0);
}
__device__ __forceinline__ float dot2(unsigned a, unsigned b, float c) {
    return __builtin_amdgcn_fdot2_f32_bf16(__builtin_bit_cast(bf2, a), __builtin_bit_cast(bf2, b), c, false);
}
__device__ __forceinline__ unsigned cvtpk(float lo, float hi) {
    typedef float f2 __attribute__((ext_vector_type(2)));
    return __builtin_bit_cast(unsigned, __builtin_convertvector((f2){lo, hi}, bf2));
}
__device__ __forceinline__ float halfsum(float v) {
    auto r = __builtin_amdgcn_permlane32_swap(__float_as_uint(v), __float_as_uint(v), false, false);
    return __uint_as_float(r[0]) + __uint_as_float(r[1]);
}
template <int AUX> __device__ __forceinline__ void st128(u32x4 v, rsrc_t rs, unsigned voff, unsigned soff) { __builtin_amdgcn_raw_buffer_store_b128(v, rs, voff + soff, 0, AUX); }
__device__ __forceinline__ u32x4 ldh(rsrc_t rs, unsigned voff, unsigned soff) { return __builtin_amdgcn_raw_buffer_load_b128(rs, voff, soff, SC1); }
__device__ __forceinline__ u32x4 ldsr128(LAS char* p) { return *(LAS u32x4*)p; }
__device__ __forceinline__ u32x2 trread(LAS char* p) {
    return __builtin_bit_cast(u32x2, __builtin_amdgcn_ds_read_tr16_b64_v4i16((LAS s16x4*)p));
}
template <int ESTEP> __device__ __forceinline__ u32x4 wfrag(LAS char* p) {
    const u32x2 a = trread(p), b = trread(p + ESTEP);
    return (u32x4){a[0], a[1], b[0], b[1]};
}
__device__ __forceinline__ u32x4 pack8(const f32x16& v, int s) {
    return (u32x4){cvtpk(v[8 * s], v[8 * s + 1]), cvtpk(v[8 * s + 2], v[8 * s + 3]), cvtpk(v[8 * s + 4], v[8 * s + 5]), cvtpk(v[8 * s + 6], v[8 * s + 7])};
}
__device__ __forceinline__ float smul(float a, float b) { float r; asm("v_mul_f32 %0, %1, %2" : "=v"(r) : "v"(a), "v"(b)); return r; }
__device__ __forceinline__ float sadd(float a, float b) { float r; asm("v_add_f32 %0, %1, %2" : "=v"(r) : "v"(a), "v"(b)); return r; }
__device__ __forceinline__ float sfma(float a, float b, float c) { float r; asm("v_fma_f32 %0, %1, %2, %3" : "=v"(r) : "v"(a), "v"(b), "v"(c)); return r; }
__device__ __forceinline__ float bflo(unsigned u) { return __uint_as_float(u << 16); }
__device__ __forceinline__ float bfhi(unsigned u) { return __uint_as_float(u & 0xFFFF0000u); }
constexpr unsigned ONES = 0x3F803F80u;
#define PIN(x) asm volatile("" : "+v"(x))
#define SBAR() __builtin_amdgcn_sched_barrier(0)
#define ST(id)
#define CNT(id)
#define ST_DECL
#define ST_ARG
#define ST_PASS
#define ST_FLUSH()

constexpr int K1 = 1024;
constexpr int L_GK = 146 * K1;
constexpr int L_P = 48 * K1, L_WO = 96 * K1, L_QF = 152 * K1;
constexpr int P1_XS = 0, P1_WS = 64 * K1, P1_WQ = 120 * K1, P1_EXT = 148 * K1, P1_EX = L_QF + K1, P1_SY = 151 * K1 + 512, P1_TMP = 48 * K1;
constexpr int L_W1 = 0, SLAB = 56 * K1, L_RED1 = 112 * K1, L_XR = 128 * K1, L_EXT = 156 * K1, L_RED2 = 144 * K1;
__host__ __device__ constexpr int w2slot(int c) { return ((c % 9) < 2 ? 112 + 16 * (c % 9) : 16 * ((c % 9) - 2)) * K1; }

constexpr unsigned FILL = 0xFFFFFFFFu, SPIN_CAP = 1u << 16;
__device__ __forceinline__ void set_flag(rsrc_t rws, unsigned off, unsigned tag, int lane) {
    __builtin_amdgcn_raw_buffer_store_b32(tag, rws, lane < 32 ? off + lane * 4 : 0x7FFFFFF0u, 0, SC1);
}
__device__ __forceinline__ void wait_flags(rsrc_t rws, unsigned off, unsigned tag) {
#pragma unroll 1
    for (unsigned spins = 0; spins < (1u << 20); ++spins) {
        asm volatile("" ::: "memory");
        const u32x4 f = ldh(rws, off, 0);
        if (__all(f[0] == tag && f[1] == tag && f[2] == tag && f[3] == tag)) break;
        __builtin_amdgcn_s_sleep(2);
    }
    asm volatile("" ::: "memory");
}
__device__ __forceinline__ void wait_word(rsrc_t rws, unsigned off, unsigned tag) {
#pragma unroll 1
    for (unsigned spins = 0; spins < (1u << 20); ++spins) {
        asm volatile("" ::: "memory");
        if (__all(__builtin_amdgcn_raw_buffer_load_b32(rws, off, 0, SC1) == tag)) break;
        __builtin_amdgcn_s_sleep(2);
    }
    asm volatile("" ::: "memory");
}
__device__ __forceinline__ void fill16(rsrc_t rws, unsigned voff, unsigned soff) { st128<SC1>((u32x4){FILL, FILL, FILL, FILL}, rws, voff, soff); }
__device__ __forceinline__ unsigned umax(unsigned a, unsigned b) { return a > b ? a : b; }
__device__ __forceinline__ unsigned umax4(u32x4 a) { return umax(umax(a[0], a[1]), umax(a[2], a[3])); }
__device__ __forceinline__ void scan(unsigned& mx, u32x4 f, bool first) {
    if (first) asm volatile("v_max3_u32 %0, %1, %2, %3\n\tv_max_u32 %0, %0, %4" : "=&v"(mx) : "v"(f[0]), "v"(f[1]), "v"(f[2]), "v"(f[3]));
    else asm volatile("v_max3_u32 %0, %0, %1, %2\n\tv_max3_u32 %0, %0, %3, %4" : "+v"(mx) : "v"(f[0]), "v"(f[1]), "v"(f[2]), "v"(f[3]));
}
__device__ __forceinline__ void settle4(bool late, u32x4& a, u32x4& b, u32x4& c, u32x4& d, rsrc_t rs, unsigned voff, unsigned sa, unsigned sc) {
    if (__builtin_expect(late, 0)) {
#pragma unroll 1
        for (unsigned spins = 0; spins < SPIN_CAP; ++spins) {
            asm volatile("s_sleep 2\n\ts_nop 4\n\tbuffer_load_dwordx4 %0, %4, %5, %6 offen sc1\n\tbuffer_load_dwordx4 %1, %4, %5, %6 offen offset:1024 sc1\n\t"
                         "buffer_load_dwordx4 %2, %4, %5, %7 offen sc1\n\tbuffer_load_dwordx4 %3, %4, %5, %7 offen offset:1024 sc1\n\ts_waitcnt vmcnt(0)"
                         : "+v"(a), "+v"(b), "+v"(c), "+v"(d) : "v"(voff), "s"(rs), "s"(sa), "s"(sc) : "memory");
            if (!__any(umax(umax(umax4(a), umax4(b)), umax(umax4(c), umax4(d))) == FILL)) break;
        }
    }
}
__device__ __forceinline__ int mod7(int v) { return v >= 7 ? v - 7 : v; }

__device__ __forceinline__ int p1_wrd(int lane, int t) {
    const int q = (lane & 15) >> 2, pp = lane & 3, g1 = (lane >> 4) & 1, hh = lane >> 5;
    return P1_WS + (8 * hh + q) * 128 + (((7 * t + 4 * g1 + pp) ^ ((q >> 1) << 3)) << 3);
}
__device__ __forceinline__ unsigned bfsplit(float v, float& rest) { const unsigned h = cvtpk(v, v) & 0xFFFFu; rest = v - bflo(h); return h; }
__device__ __forceinline__ u32x4 sy_ld(LAS char* L, int q) { asm volatile("" ::: "memory"); const u32x4 v = *(volatile LAS u32x4*)(L + P1_SY + q * 16); asm volatile("" ::: "memory"); return v; }
__device__ __forceinline__ void sy_st(LAS char* L, int word, unsigned v) { asm volatile("" ::: "memory"); *(volatile LAS unsigned*)(L + P1_SY + word * 4) = v; asm volatile("" ::: "memory"); }
template <class Fn> __device__ __forceinline__ void sy_wait(LAS char* L, int q, u32x4& v, Fn&& ok) {
#pragma unroll 1
    while (!__all(ok(v))) { __builtin_amdgcn_s_sleep(0); v = sy_ld(L, q); }
}
__device__ __forceinline__ unsigned get_tag(LAS char* L) {
    u32x4 sy = sy_ld(L, 0);
    sy_wait(L, 0, sy, [&](u32x4 v) INL { return v[2] >= 1; });
    return __builtin_amdgcn_readfirstlane(sy_ld(L, 16)[0]) + 1;
}
__device__ __forceinline__ unsigned umin(unsigned a, unsigned b) { return a < b ? a : b; }
__device__ __forceinline__ u32x2 ext_half(const f32x16& cs, unsigned b, int lane) {
    float lo, lo2;
    const unsigned ch = bfsplit(cs[0], lo), cl = bfsplit(lo, lo2);
    return lane < 32 ? (u32x2){ch | (ch << 16), cl | (b << 16)} : (u32x2){0u, 0u};
}
__device__ __forceinline__ unsigned umin4(u32x4 v) { return umin(umin(v[0], v[1]), umin(v[2], v[3])); }
__device__ __forceinline__ int p1_xrd(int lane, int m, int uu) { const int r = lane & 31; return P1_XS + m * 4096 + r * 128 + (((2 * uu + (lane >> 5)) ^ ((r >> 1) & 7)) << 4); }

template <bool VT>
__device__ __forceinline__ void p1_compute(LAS char* L, rsrc_t rws, int w, int lane, int rb, int pair, int head, unsigned& tag ST_ARG) {
    const int r = lane & 31, hh = lane >> 5;
    LAS char* xa[4];
#pragma unroll
    for (int uu = 0; uu < 4; ++uu) { xa[uu] = L + p1_xrd(lane, w, uu); PIN(xa[uu]); }
    LAS char* wb[2] = {L + p1_wrd(lane, 0), L + p1_wrd(lane, 1)};
    PIN(wb[0]); PIN(wb[1]);
    u32x4 xf[28];
    f32x16 acc[2] = {};
    u32x4 bf[8][2];
    auto RD = [&](int u) INL {
        xf[u] = ldsr128(xa[u & 3] + ((u >> 2) & 3) * 16384);
        bf[u & 7][0] = wfrag<512>(wb[0] + u * 2048);
        bf[u & 7][1] = wfrag<512>(wb[1] + u * 2048);
        SBAR();
    };
    auto MM = [&](int u) INL {
        acc[0] = VT ? mfma(xf[u], bf[u & 7][0], acc[0]) : mfma(bf[u & 7][0], xf[u], acc[0]);
        acc[1] = VT ? mfma(xf[u], bf[u & 7][1], acc[1]) : mfma(bf[u & 7][1], xf[u], acc[1]);
        PIN(acc[0]); PIN(acc[1]); SBAR();
    };
    u32x4 sy = sy_ld(L, 0), st;
    sfor<7>([&](auto ic) INL {
        constexpr int c = ic.value;
        sy_wait(L, 0, sy, [&](u32x4 v) INL { return umin(v[0], v[1]) > c; });
        if (c == 0) ST(1);
        RD(4 * c);
        if constexpr (c > 0) MM(4 * c - 2);
        RD(4 * c + 1);
        if constexpr (c > 0) MM(4 * c - 1);
        RD(4 * c + 2); RD(4 * c + 3);
        if constexpr (c <= 3) sy_st(L, 4 + w, c + 1);
        sy = sy_ld(L, 0);
        MM(4 * c); MM(4 * c + 1);
        if constexpr (c == 4) { st = ldh(rws, r * 16, WS_XS + (rb * 128 + w * 32) * 16); tag = get_tag(L); }
    });
    MM(26); MM(27);
    ST(2);
#pragma unroll 1
    for (unsigned spins = 0; spins < (1u << 20) && !__all(st[1] == tag && st[3] == tag); ++spins) {
        asm volatile("" ::: "memory");
        st = ldh(rws, r * 16, WS_XS + (rb * 128 + w * 32) * 16);
    }
    const float rstd = __uint_as_float(st[2]);
    LAS float* ex = (LAS float*)(L + P1_EX + w * 2048);
    if (VT) ex[r] = rstd;
    float t0, t1;
    const unsigned mh = bfsplit(__uint_as_float(st[0]), t0), ml = bfsplit(t0, t1);
    sy_wait(L, 0, sy, [&](u32x4 v) INL { return umin(v[2], v[3]) >= 8; });
    const u32x4 xe = {mh | (ml << 16), mh, mh | (ml << 16), mh};
    const int tile = rb * 4 + w;
#pragma unroll
    for (int t = 0; t < 2; ++t) { const u32x4 e = ldsr128(L + P1_EXT + t * 1024 + lane * 16); acc[t] = VT ? mfma(xe, e, acc[t]) : mfma(e, xe, acc[t]); }
    u32x4 kvo[4];
    if (VT) {
        f32x4 b4[4];
#pragma unroll
        for (int i = 0; i < 4; ++i) b4[i] = *(LAS f32x4*)(ex + 8 * i + 4 * hh);
#pragma unroll
        for (int k = 0; k < 32; ++k) acc[k >> 4][k & 15] *= b4[(k >> 2) & 3][k & 3];
    } else { acc[0] *= rstd; acc[1] *= rstd; }
#pragma unroll
    for (int k = 0; k < 4; ++k) {
        kvo[k] = pack8(acc[k >> 1], k & 1);
        if (VT && r >= 28) { const unsigned z = r == 28 ? ONES : 0u; kvo[k] = (u32x4){z, z, z, z}; }
        if (!VT && (k & 1) && hh) { kvo[k][2] = 0u; kvo[k][3] = 0u; }
        PIN(kvo[k]);
    }
    unsigned kvd = WS_KV + (2 * pair * 64 + tile) * 4096 + (VT ? 2048 + lane * 16 : (((r & 19) | ((r & 4) << 1) | ((r & 8) >> 1)) + 32 * hh) * 16);
    PIN(kvd);
    auto put = [&](int k) INL { st128<SC1>(kvo[k], rws, kvd, (k >> 1) * 64 * 4096 + (k & 1) * 1024); SBAR(); };
    put(0);
    ST(3);
    LAS char* wq = L + P1_WQ + (8 * hh + ((lane & 15) >> 2)) * 64 + ((head & 1) + 4 * ((lane >> 4) & 1) + (lane & 3)) * 8;
    PIN(wq);
    f32x16 aq = {};
    u32x4 bq[4];
    sy_wait(L, 0, sy, [&](u32x4 v) INL { return umin(v[0], v[1]) >= 8; });
    bq[0] = wfrag<256>(wq); bq[1] = wfrag<256>(wq + 1024); bq[2] = wfrag<256>(wq + 2048);
    sfor<28>([&](auto ic) INL {
        constexpr int u = ic.value;
        if constexpr (u == 13) sy_wait(L, 0, sy, [&](u32x4 v) INL { return umin(v[0], v[1]) >= 9; });
        aq = mfma(bq[u & 3], xf[u], aq);
        PIN(aq); SBAR();
        if constexpr (u + 3 < 28) bq[(u + 3) & 3] = wfrag<256>(wq + (u + 3) * 1024);
        if constexpr (u == 9 || u == 24) sy = sy_ld(L, 0);
        SBAR();
        if constexpr (u == 0 || u == 6 || u == 12) put(1 + u / 6);
    });
    sy_wait(L, 0, sy, [&](u32x4 v) INL { return umin(v[2], v[3]) >= 9; });
    aq = mfma(ldsr128(L + P1_EXT + 2048 + lane * 16), xe, aq);
    aq *= rstd * (0.18898223650461362f * 1.4426950408889634f);
#pragma unroll
    for (int s = 0; s < 2; ++s) {
        u32x4 o = pack8(aq, s);
        if (s == 1 && hh) { o[2] = 0u; o[3] = 0u; }
        *(LAS u32x4*)(L + L_QF + (w * 2 + s) * 1024 + lane * 16) = o;
    }
}

__device__ __forceinline__ unsigned slab_voff(int lane, int pitch) { return (lane >> 3) * pitch + (((lane & 7) ^ (((lane >> 4) & 1) << 2)) << 4); }
__device__ __forceinline__ int slab_rd(int lane, int t) {
    const int q = (lane & 15) >> 2, pp = lane & 3, g1 = (lane >> 4) & 1, hh = lane >> 5;
    return (4 * hh + q) * 128 + (((8 * t + 4 * g1 + pp) ^ ((q >> 1) << 3)) << 3);
}
__device__ __forceinline__ void p1_chunk(const Params& p, int c, int lw, int lane, int rb, int pair, int kind, int rot) {
    int piece = (lane & 7) ^ (((lane >> 4) & 1) << 2);
    if (piece == 7) piece = 6;
    const int cc = mod7(c + rot);
    const unsigned wb = (cc * 64 + lw * 32) * 2688 + (448 * (1 + kind) + 56 * pair) * 2, xb = (rb * 128 + lw * 64) * 896 + cc * 128;
#pragma unroll
    for (int i = 0; i < 4; ++i) dma16(mkrs(p.wqkv, D * 3 * D * 2), (lane >> 3) * 2688 + piece * 16, wb + i * 8 * 2688, P1_WS + (c * 64 + lw * 32 + i * 8) * 128);
#pragma unroll
    for (int i = 0; i < 8; ++i)
        dma16(mkrs(p.x, S * D * 2), (lane >> 3) * 896 + (((lane & 7) ^ ((4 * (i & 1) + (lane >> 4)) & 7)) << 4), xb + i * 8 * 896, P1_XS + (c & 3) * 16384 + lw * 8192 + i * 1024);
}
__device__ __forceinline__ void p1_loader(const Params& p, LAS char* L, int lw, int lane, int rb, int pair, int kind, int head, int rot ST_ARG) {
    u32x4 sy = {0u, 0u, 0u, 0u};
#pragma unroll 1
    for (int c = 1; c < 7; ++c) {
        sy_wait(L, 1, sy, [&](u32x4 v) INL { return (int)(lw ? umin(v[2], v[3]) : umin(v[0], v[1])) > c - 4; });
        p1_chunk(p, c, lw, lane, rb, pair, kind, rot);
        vm_wait<12>(); sy_st(L, lw, c);
    }
    const unsigned vq = (lane >> 2) * 2688 + (lane & 3) * 16, qb = 56 * head - 8 * (head & 1) + lw * 32 * 2688;
#pragma unroll
    for (int k = 0; k < 14; ++k) {
        dma16(mkrs(p.wqkv, D * 3 * D * 2), vq, (mod7((k >> 1) + rot) * 64 + (k & 1) * 16) * 2688 + qb, P1_WQ + ((k >> 1) * 64 + lw * 32 + (k & 1) * 16) * 64);
        if (k == 5) { vm_wait<6>(); sy_st(L, lw, 7); ST(2); }
    }
    sy_wait(L, 1, sy, [&](u32x4 v) INL { return umin4(v) >= 4; });
    if (lw == 0) {
#pragma unroll
        for (int i = 0; i < 8; ++i) dma16(mkrs(p.w1, D * F * 2), slab_voff(lane, 3584), blockIdx.x < 196 ? ((blockIdx.x % 7) * 64 + i * 8) * 3584 + (blockIdx.x / 7) * 128 : 0x7F000000u, P1_TMP + i * 1024);
        vm_wait<14>(); sy_st(L, lw, 8);
        vm_wait<8>(); sy_st(L, lw, 9);
    } else {
        dma4(mkrs(p.w2, D * F * 2), lane < 49 ? lane * 128 : 0x7FFFFF00u, blockIdx.x * 49 * 128, P1_TMP + 8192);
        dma4(mkrs(p.wo, D * D * 2), lane < 13 ? lane * 128 : 0x7FFFFF00u, blockIdx.x * 13 * 128, P1_TMP + 8192);
        vm_wait<8>(); sy_st(L, lw, 8);
        vm_wait<2>(); sy_st(L, lw, 9);
    }
    ST(6);
}
__device__ __forceinline__ void p1_aux(const Params& p, LAS char* L, rsrc_t rws, int t, int lane, int rb, int pair, int kind, int head ST_ARG) {
    if (t == 0) {
        const int row = rb * 128 + head * 8 + (lane >> 3);
        u32x4 xv[7];
#pragma unroll
        for (int k = 0; k < 7; ++k) xv[k] = __builtin_amdgcn_raw_buffer_load_b128(mkrs(p.x, S * D * 2), row * 896 + (lane & 7) * 16 + k * 128, 0, 0);
        float sx[2] = {}, sxx[2] = {};
#pragma unroll
        for (int k = 0; k < 28; ++k) { const unsigned a = xv[k >> 2][k & 3]; sx[k & 1] = dot2(a, ONES, sx[k & 1]); sxx[k & 1] = dot2(a, a, sxx[k & 1]); }
        float s1 = sx[0] + sx[1], s2 = sxx[0] + sxx[1];
#pragma unroll
        for (int m = 1; m < 8; m <<= 1) { s1 += __shfl_xor(s1, m); s2 += __shfl_xor(s2, m); }
        const float mu = s1 * (1.0f / D), var = fmaxf(s2 * (1.0f / D) - mu * mu, 0.f);
        vm_wait<0>(); sy_st(L, 2, 1);
        const unsigned tag = get_tag(L);
        st128<SC1>((u32x4){__float_as_uint(-mu), tag, __float_as_uint(__builtin_amdgcn_rsqf(var + EPS)), tag}, rws, (lane & 7) ? 0x7F000000u : row * 16, WS_XS);
    }
    __builtin_amdgcn_s_setprio(3);
    LAS char* rd = L + p1_wrd(lane, t);
    u32x4 ones = {ONES, ONES, ONES, ONES}, sy = {0u, 0u, 0u, 0u};
    PIN(ones);
    f32x16 cs = {};
#pragma unroll 1
    for (int c = 0; c < 7; ++c) {
        sy_wait(L, 0, sy, [&](u32x4 v) INL { return (int)umin(v[0], v[1]) > c; });
        if (t == 1 && c >= FILL_AT && c < FILL_AT + 4) {
#pragma unroll
            for (int k = 0; k < 4; ++k) fill16(rws, lane * 16, WS_KV + ((2 * pair + (k >> 1)) * 64 + rb * 4 + c - FILL_AT) * 4096 + (2 * kind + (k & 1)) * 1024);
        }
#pragma unroll
        for (int k = 0; k < 4; ++k) cs = mfma(ones, wfrag<512>(rd + c * 8192 + k * 2048), cs);
    }
    { const u32x2 e = ext_half(cs, 0u, lane); *(LAS u32x4*)(L + P1_EXT + t * 1024 + lane * 16) = (u32x4){e[0], e[1], 0u, 0u}; }
    vm_wait<0>();
    sy_st(L, 2 + t, 8);
    ST(3);
    if (t == 1) set_flag(rws, WS_F1 + ((pair * 16 + rb) * 2 + kind) * 128, get_tag(L), lane);
    LAS char* rq = L + P1_WQ + t * 16384 + (8 * (lane >> 5) + ((lane & 15) >> 2)) * 64 + ((head & 1) + 4 * ((lane >> 4) & 1) + (lane & 3)) * 8;
    PIN(rq);
    f32x16 cq = {};
    sy_wait(L, 0, sy, [&](u32x4 v) INL { return umin(v[0], v[1]) >= 8u + t; });
#pragma unroll
    for (int k = 0; k < 16; ++k) if (k < 12 || t == 0) cq = mfma(ones, wfrag<256>(rq + k * 1024), cq);
    *(LAS u32x2*)(L + P1_EXT + 2048 + lane * 16 + t * 8) = ext_half(cq, 0u, lane);
    sy_st(L, 2 + t, 9);
}

__device__ __forceinline__ f32x16 pv(u32x4 v, f32x8 pr, f32x16 o) {
    return __builtin_amdgcn_smfmac_f32_32x32x32_bf16(__builtin_bit_cast(bf8, v), __builtin_bit_cast(bf16v, pr), o, 0xDDDD, 0, 0);
}
__device__ __forceinline__ f32x8 lo8(const f32x16& s) { return __builtin_shufflevector(s, s, 0, 1, 2, 3, 4, 5, 6, 7); }
__device__ __forceinline__ f32x8 hi8(const f32x16& s) { return __builtin_shufflevector(s, s, 8, 9, 10, 11, 12, 13, 14, 15); }
__device__ __forceinline__ f32x16 redo_o(rsrc_t rws, int lane, int head, const u32x4 (&qf)[2]) {
#pragma unroll 1
    for (unsigned f = 0, spins = 0; f < 256 && spins < SPIN_CAP;) {
        asm volatile("" ::: "memory");
        if (__any(umax4(ldh(rws, lane * 16, WS_KV + (head * 256 + f) * 1024)) == FILL)) ++spins; else ++f;
    }
    asm volatile("" ::: "memory");
    float m = -INFINITY;
#pragma unroll 1
    for (int t = 0; t < 64; ++t) {
        const unsigned b = WS_KV + (head * 64 + t) * 4096;
        f32x16 s = {};
        s = mfma(ldh(rws, lane * 16, b), qf[0], s);
        s = mfma(ldh(rws, lane * 16 + 1024, b), qf[1], s);
#pragma unroll
        for (int k = 0; k < 16; ++k) m = fmaxf(m, s[k]);
    }
    auto mm = __builtin_amdgcn_permlane32_swap(__float_as_uint(m), __float_as_uint(m), false, false);
    m = fmaxf(__uint_as_float(mm[0]), __uint_as_float(mm[1]));
    f32x16 o = {};
#pragma unroll 1
    for (int t = 0; t < 64; ++t) {
        const unsigned b = WS_KV + (head * 64 + t) * 4096;
        f32x16 s = {};
        s = mfma(ldh(rws, lane * 16, b), qf[0], s);
        s = mfma(ldh(rws, lane * 16 + 1024, b), qf[1], s);
#pragma unroll
        for (int k = 0; k < 16; ++k) s[k] = __builtin_amdgcn_exp2f(s[k] - m);
        o = pv(ldh(rws, lane * 16 + 2048, b), lo8(s), o);
        o = pv(ldh(rws, lane * 16 + 3072, b), hi8(s), o);
    }
    return o;
}

#define W1DMA(hf, u, e) do { int uu_ = (u) + 2 * ct1; if (uu_ >= 28) uu_ -= 28; dma16(r1, v1, (uu_ * 16 + (e) * 8) * 3584 + ct1 * 256 + (hf) * 128, L_W1 + (hf) * SLAB + (u) * 2048 + (e) * 1024); } while (0)

__device__ __forceinline__ void attn_compute(LAS char* L, rsrc_t rws, int w, int lane, int rb, int head ST_ARG) {
    unsigned so, cap;
    f32x16 o;
    LAS char *q = L + L_QF + lane * 16, *pp = L + L_P + lane * 16;
    asm volatile(
        "s_mov_b32 %[cap], 0x10000\n\ts_setprio 1\n\ts_waitcnt lgkmcnt(0)\n\ts_barrier\n\t1:\n\ts_add_u32 %[so], %[kvb], 0\n\t"
        "buffer_load_dwordx4 v[144:147], %[vo], %[rs], %[so] offen offset:0 sc1\n\t"
        "buffer_load_dwordx4 v[148:151], %[vo], %[rs], %[so] offen offset:1024 sc1\n\t"
        "buffer_load_dwordx4 v[152:155], %[vo], %[rs], %[so] offen offset:2048 sc1\n\t"
        "buffer_load_dwordx4 v[156:159], %[vo], %[rs], %[so] offen offset:3072 sc1\n\ts_add_u32 %[so], %[kvb], 4096\n\t"
        "buffer_load_dwordx4 v[160:163], %[vo], %[rs], %[so] offen offset:0 sc1\n\t"
        "buffer_load_dwordx4 v[164:167], %[vo], %[rs], %[so] offen offset:1024 sc1\n\t"
        "buffer_load_dwordx4 v[168:171], %[vo], %[rs], %[so] offen offset:2048 sc1\n\t"
        "buffer_load_dwordx4 v[172:175], %[vo], %[rs], %[so] offen offset:3072 sc1\n\ts_add_u32 %[so], %[kvb], 8192\n\t"
        "buffer_load_dwordx4 v[176:179], %[vo], %[rs], %[so] offen offset:0 sc1\n\t"
        "buffer_load_dwordx4 v[180:183], %[vo], %[rs], %[so] offen offset:1024 sc1\n\t"
        "buffer_load_dwordx4 v[184:187], %[vo], %[rs], %[so] offen offset:2048 sc1\n\t"
        "buffer_load_dwordx4 v[188:191], %[vo], %[rs], %[so] offen offset:3072 sc1\n\tds_read_b128 v[112:115], %[q0] offset:0\n\t"
        "ds_read_b128 v[116:119], %[q0] offset:1024\n\tds_read_b128 v[120:123], %[q1] offset:0\n\tds_read_b128 v[124:127], %[q1] offset:1024\n\t"
        "ds_read_b128 v[128:131], %[q2] offset:0\n\tds_read_b128 v[132:135], %[q2] offset:1024\n\tds_read_b128 v[136:139], %[q3] offset:0\n\t"
        "ds_read_b128 v[140:143], %[q3] offset:1024\n\tv_mov_b32 v0, 0\n\tv_mov_b32 v1, 0\n\tv_mov_b32 v2, 0\n\tv_mov_b32 v3, 0\n\tv_mov_b32 v4, 0\n\t"
        "v_mov_b32 v5, 0\n\tv_mov_b32 v6, 0\n\tv_mov_b32 v7, 0\n\tv_mov_b32 v8, 0\n\tv_mov_b32 v9, 0\n\tv_mov_b32 v10, 0\n\tv_mov_b32 v11, 0\n\t"
        "v_mov_b32 v12, 0\n\tv_mov_b32 v13, 0\n\tv_mov_b32 v14, 0\n\tv_mov_b32 v15, 0\n\tv_mov_b32 v16, 0\n\tv_mov_b32 v17, 0\n\tv_mov_b32 v18, 0\n\t"
        "v_mov_b32 v19, 0\n\tv_mov_b32 v20, 0\n\tv_mov_b32 v21, 0\n\tv_mov_b32 v22, 0\n\tv_mov_b32 v23, 0\n\tv_mov_b32 v24, 0\n\tv_mov_b32 v25, 0\n\t"
        "v_mov_b32 v26, 0\n\tv_mov_b32 v27, 0\n\tv_mov_b32 v28, 0\n\tv_mov_b32 v29, 0\n\tv_mov_b32 v30, 0\n\tv_mov_b32 v31, 0\n\tv_mov_b32 v32, 0\n\t"
        "v_mov_b32 v33, 0\n\tv_mov_b32 v34, 0\n\tv_mov_b32 v35, 0\n\tv_mov_b32 v36, 0\n\tv_mov_b32 v37, 0\n\tv_mov_b32 v38, 0\n\tv_mov_b32 v39, 0\n\t"
        "v_mov_b32 v40, 0\n\tv_mov_b32 v41, 0\n\tv_mov_b32 v42, 0\n\tv_mov_b32 v43, 0\n\tv_mov_b32 v44, 0\n\tv_mov_b32 v45, 0\n\tv_mov_b32 v46, 0\n\t"
        "v_mov_b32 v47, 0\n\tv_mov_b32 v48, 0\n\tv_mov_b32 v49, 0\n\tv_mov_b32 v50, 0\n\tv_mov_b32 v51, 0\n\tv_mov_b32 v52, 0\n\tv_mov_b32 v53, 0\n\t"
        "v_mov_b32 v54, 0\n\tv_mov_b32 v55, 0\n\tv_mov_b32 v56, 0\n\tv_mov_b32 v57, 0\n\tv_mov_b32 v58, 0\n\tv_mov_b32 v59, 0\n\tv_mov_b32 v60, 0\n\t"
        "v_mov_b32 v61, 0\n\tv_mov_b32 v62, 0\n\tv_mov_b32 v63, 0\n\tv_mov_b32 v224, 0xdddd\n\ts_waitcnt vmcnt(8)\n\tv_max3_u32 v225, v144, v148, v152\n\t"
        "v_max_u32 v225, v225, v156\n\tv_cmp_eq_u32 vcc, -1, v225\n\ts_cbranch_vccz 2f\n\ts_sub_u32 %[cap], %[cap], 1\n\ts_cbranch_scc1 2f\n\t"
        "s_waitcnt vmcnt(0) lgkmcnt(0)\n\ts_sleep 1\n\ts_branch 1b\n\t2:\n\ts_waitcnt lgkmcnt(0)\n\t"
        "v_mfma_f32_32x32x16_bf16 v[64:79], v[144:147], v[112:115], 0\n\tv_mfma_f32_32x32x16_bf16 v[64:79], v[148:151], v[116:119], v[64:79]\n\t"
        "v_mfma_f32_32x32x16_bf16 v[80:95], v[144:147], v[120:123], 0\n\ts_nop 10\n\tv_exp_f32 v64, v64\n\tv_exp_f32 v65, v65\n\tv_exp_f32 v66, v66\n\t"
        "v_exp_f32 v67, v67\n\tv_mfma_f32_32x32x16_bf16 v[80:95], v[148:151], v[124:127], v[80:95]\n\tv_exp_f32 v68, v68\n\tv_exp_f32 v69, v69\n\t"
        "v_exp_f32 v70, v70\n\tv_exp_f32 v71, v71\n\tv_exp_f32 v72, v72\n\tv_exp_f32 v73, v73\n\tv_exp_f32 v74, v74\n\tv_exp_f32 v75, v75\n\t"
        "v_exp_f32 v76, v76\n\tv_exp_f32 v77, v77\n\tv_exp_f32 v78, v78\n\tv_exp_f32 v79, v79\n\t"
        "v_mfma_f32_32x32x16_bf16 v[96:111], v[144:147], v[128:131], 0\n\tv_exp_f32 v80, v80\n\tv_exp_f32 v81, v81\n\tv_exp_f32 v82, v82\n\t"
        "v_exp_f32 v83, v83\n\tv_mfma_f32_32x32x16_bf16 v[96:111], v[148:151], v[132:135], v[96:111]\n\tv_exp_f32 v84, v84\n\tv_exp_f32 v85, v85\n\t"
        "v_exp_f32 v86, v86\n\tv_exp_f32 v87, v87\n\tv_smfmac_f32_32x32x32_bf16 v[0:15], v[152:155], v[64:71], v224\n\tv_exp_f32 v88, v88\n\t"
        "v_exp_f32 v89, v89\n\tv_exp_f32 v90, v90\n\tv_exp_f32 v91, v91\n\tv_smfmac_f32_32x32x32_bf16 v[0:15], v[156:159], v[72:79], v224\n\t"
        "s_mov_b32 %[cap], 0x10000\n\t3:\n\ts_waitcnt vmcnt(0)\n\tv_max3_u32 v225, v160, v164, v168\n\tv_max3_u32 v225, v225, v172, v176\n\t"
        "v_max3_u32 v225, v225, v180, v184\n\tv_max_u32 v225, v225, v188\n\tv_cmp_eq_u32 vcc, -1, v225\n\ts_cbranch_vccz 4f\n\ts_sub_u32 %[cap], %[cap], 1\n\t"
        "s_cbranch_scc1 4f\n\ts_sleep 1\n\ts_add_u32 %[so], %[kvb], 4096\n\tbuffer_load_dwordx4 v[160:163], %[vo], %[rs], %[so] offen offset:0 sc1\n\t"
        "buffer_load_dwordx4 v[164:167], %[vo], %[rs], %[so] offen offset:1024 sc1\n\t"
        "buffer_load_dwordx4 v[168:171], %[vo], %[rs], %[so] offen offset:2048 sc1\n\t"
        "buffer_load_dwordx4 v[172:175], %[vo], %[rs], %[so] offen offset:3072 sc1\n\ts_add_u32 %[so], %[kvb], 8192\n\t"
        "buffer_load_dwordx4 v[176:179], %[vo], %[rs], %[so] offen offset:0 sc1\n\t"
        "buffer_load_dwordx4 v[180:183], %[vo], %[rs], %[so] offen offset:1024 sc1\n\t"
        "buffer_load_dwordx4 v[184:187], %[vo], %[rs], %[so] offen offset:2048 sc1\n\t"
        "buffer_load_dwordx4 v[188:191], %[vo], %[rs], %[so] offen offset:3072 sc1\n\ts_branch 3b\n\t4:\n\ts_mov_b32 %[cap], 0x10000\n\t"
        "s_add_u32 %[so], %[kvb], 12288\n\tv_exp_f32 v92, v92\n\tbuffer_load_dwordx4 v[192:195], %[vo], %[rs], %[so] offen offset:0 sc1\n\t"
        "buffer_load_dwordx4 v[196:199], %[vo], %[rs], %[so] offen offset:1024 sc1\n\tv_exp_f32 v93, v93\n\tv_exp_f32 v94, v94\n\tv_exp_f32 v95, v95\n\t"
        "v_mfma_f32_32x32x16_bf16 v[64:79], v[144:147], v[136:139], 0\n\tv_exp_f32 v96, v96\n\tv_exp_f32 v97, v97\n\tv_exp_f32 v98, v98\n\t"
        "v_exp_f32 v99, v99\n\tv_mfma_f32_32x32x16_bf16 v[64:79], v[148:151], v[140:143], v[64:79]\n\tv_exp_f32 v100, v100\n\tv_exp_f32 v101, v101\n\t"
        "v_exp_f32 v102, v102\n\tv_exp_f32 v103, v103\n\tv_smfmac_f32_32x32x32_bf16 v[16:31], v[152:155], v[80:87], v224\n\tv_exp_f32 v104, v104\n\t"
        "v_exp_f32 v105, v105\n\tv_exp_f32 v106, v106\n\tv_exp_f32 v107, v107\n\tv_smfmac_f32_32x32x32_bf16 v[16:31], v[156:159], v[88:95], v224\n\t"
        "v_exp_f32 v108, v108\n\tbuffer_load_dwordx4 v[200:203], %[vo], %[rs], %[so] offen offset:2048 sc1\n\t"
        "buffer_load_dwordx4 v[204:207], %[vo], %[rs], %[so] offen offset:3072 sc1\n\tv_exp_f32 v109, v109\n\tv_exp_f32 v110, v110\n\tv_exp_f32 v111, v111\n\t"
        "v_mfma_f32_32x32x16_bf16 v[80:95], v[160:163], v[112:115], 0\n\tv_exp_f32 v64, v64\n\tv_exp_f32 v65, v65\n\tv_exp_f32 v66, v66\n\t"
        "v_exp_f32 v67, v67\n\tv_mfma_f32_32x32x16_bf16 v[80:95], v[164:167], v[116:119], v[80:95]\n\tv_exp_f32 v68, v68\n\tv_exp_f32 v69, v69\n\t"
        "v_exp_f32 v70, v70\n\tv_exp_f32 v71, v71\n\tv_smfmac_f32_32x32x32_bf16 v[32:47], v[152:155], v[96:103], v224\n\tv_exp_f32 v72, v72\n\t"
        "v_exp_f32 v73, v73\n\tv_exp_f32 v74, v74\n\tv_exp_f32 v75, v75\n\tv_smfmac_f32_32x32x32_bf16 v[32:47], v[156:159], v[104:111], v224\n\t"
        "s_add_u32 %[so], %[kvb], 16384\n\tv_exp_f32 v76, v76\n\tbuffer_load_dwordx4 v[208:211], %[vo], %[rs], %[so] offen offset:0 sc1\n\t"
        "buffer_load_dwordx4 v[212:215], %[vo], %[rs], %[so] offen offset:1024 sc1\n\tv_exp_f32 v77, v77\n\tv_exp_f32 v78, v78\n\tv_exp_f32 v79, v79\n\t"
        "v_mfma_f32_32x32x16_bf16 v[96:111], v[160:163], v[120:123], 0\n\tv_exp_f32 v80, v80\n\tv_exp_f32 v81, v81\n\tv_exp_f32 v82, v82\n\t"
        "v_exp_f32 v83, v83\n\tv_mfma_f32_32x32x16_bf16 v[96:111], v[164:167], v[124:127], v[96:111]\n\tv_exp_f32 v84, v84\n\tv_exp_f32 v85, v85\n\t"
        "v_exp_f32 v86, v86\n\tv_exp_f32 v87, v87\n\tv_smfmac_f32_32x32x32_bf16 v[48:63], v[152:155], v[64:71], v224\n\tv_exp_f32 v88, v88\n\t"
        "v_exp_f32 v89, v89\n\tv_exp_f32 v90, v90\n\tv_exp_f32 v91, v91\n\tv_smfmac_f32_32x32x32_bf16 v[48:63], v[156:159], v[72:79], v224\n\t"
        "v_exp_f32 v92, v92\n\tbuffer_load_dwordx4 v[216:219], %[vo], %[rs], %[so] offen offset:2048 sc1\n\tv_exp_f32 v93, v93\n\tv_exp_f32 v94, v94\n\t"
        "v_exp_f32 v95, v95\n\tv_mfma_f32_32x32x16_bf16 v[64:79], v[160:163], v[128:131], 0\n\tv_exp_f32 v96, v96\n\tv_exp_f32 v97, v97\n\t"
        "v_exp_f32 v98, v98\n\tv_exp_f32 v99, v99\n\tv_mfma_f32_32x32x16_bf16 v[64:79], v[164:167], v[132:135], v[64:79]\n\tv_exp_f32 v100, v100\n\t"
        "v_exp_f32 v101, v101\n\tv_exp_f32 v102, v102\n\tv_exp_f32 v103, v103\n\tv_smfmac_f32_32x32x32_bf16 v[0:15], v[168:171], v[80:87], v224\n\t"
        "v_exp_f32 v104, v104\n\tv_exp_f32 v105, v105\n\tv_exp_f32 v106, v106\n\tv_exp_f32 v107, v107\n\t"
        "v_smfmac_f32_32x32x32_bf16 v[0:15], v[172:175], v[88:95], v224\n\tv_exp_f32 v108, v108\n\t"
        "buffer_load_dwordx4 v[220:223], %[vo], %[rs], %[so] offen offset:3072 sc1\n\tv_exp_f32 v109, v109\n\tv_exp_f32 v110, v110\n\tv_exp_f32 v111, v111\n\t"
        "v_mfma_f32_32x32x16_bf16 v[80:95], v[160:163], v[136:139], 0\n\tv_exp_f32 v64, v64\n\tv_exp_f32 v65, v65\n\tv_exp_f32 v66, v66\n\t"
        "v_exp_f32 v67, v67\n\tv_mfma_f32_32x32x16_bf16 v[80:95], v[164:167], v[140:143], v[80:95]\n\tv_exp_f32 v68, v68\n\tv_exp_f32 v69, v69\n\t"
        "v_exp_f32 v70, v70\n\tv_exp_f32 v71, v71\n\tv_smfmac_f32_32x32x32_bf16 v[16:31], v[168:171], v[96:103], v224\n\tv_exp_f32 v72, v72\n\t"
        "v_exp_f32 v73, v73\n\tv_exp_f32 v74, v74\n\tv_exp_f32 v75, v75\n\tv_smfmac_f32_32x32x32_bf16 v[16:31], v[172:175], v[104:111], v224\n\t"
        "s_add_u32 %[so], %[kvb], 20480\n\tv_exp_f32 v76, v76\n\tbuffer_load_dwordx4 v[144:147], %[vo], %[rs], %[so] offen offset:0 sc1\n\t"
        "v_exp_f32 v77, v77\n\tv_exp_f32 v78, v78\n\tv_exp_f32 v79, v79\n\tv_mfma_f32_32x32x16_bf16 v[96:111], v[176:179], v[112:115], 0\n\t"
        "v_exp_f32 v80, v80\n\tv_exp_f32 v81, v81\n\tv_exp_f32 v82, v82\n\tv_exp_f32 v83, v83\n\t"
        "v_mfma_f32_32x32x16_bf16 v[96:111], v[180:183], v[116:119], v[96:111]\n\tv_exp_f32 v84, v84\n\tv_exp_f32 v85, v85\n\tv_exp_f32 v86, v86\n\t"
        "v_exp_f32 v87, v87\n\tv_smfmac_f32_32x32x32_bf16 v[32:47], v[168:171], v[64:71], v224\n\tv_exp_f32 v88, v88\n\tv_exp_f32 v89, v89\n\t"
        "v_exp_f32 v90, v90\n\tv_exp_f32 v91, v91\n\tv_smfmac_f32_32x32x32_bf16 v[32:47], v[172:175], v[72:79], v224\n\tv_exp_f32 v92, v92\n\t"
        "buffer_load_dwordx4 v[148:151], %[vo], %[rs], %[so] offen offset:1024 sc1\n\tv_exp_f32 v93, v93\n\tv_exp_f32 v94, v94\n\tv_exp_f32 v95, v95\n\t"
        "v_mfma_f32_32x32x16_bf16 v[64:79], v[176:179], v[120:123], 0\n\tv_exp_f32 v96, v96\n\tv_exp_f32 v97, v97\n\tv_exp_f32 v98, v98\n\t"
        "v_exp_f32 v99, v99\n\tv_mfma_f32_32x32x16_bf16 v[64:79], v[180:183], v[124:127], v[64:79]\n\tv_exp_f32 v100, v100\n\tv_exp_f32 v101, v101\n\t"
        "v_exp_f32 v102, v102\n\tv_exp_f32 v103, v103\n\tv_smfmac_f32_32x32x32_bf16 v[48:63], v[168:171], v[80:87], v224\n\tv_exp_f32 v104, v104\n\t"
        "v_exp_f32 v105, v105\n\tv_exp_f32 v106, v106\n\tv_exp_f32 v107, v107\n\tv_smfmac_f32_32x32x32_bf16 v[48:63], v[172:175], v[88:95], v224\n\t"
        "v_exp_f32 v108, v108\n\tbuffer_load_dwordx4 v[152:155], %[vo], %[rs], %[so] offen offset:2048 sc1\n\tv_exp_f32 v109, v109\n\tv_exp_f32 v110, v110\n\t"
        "v_exp_f32 v111, v111\n\tv_mfma_f32_32x32x16_bf16 v[80:95], v[176:179], v[128:131], 0\n\tv_exp_f32 v64, v64\n\tv_exp_f32 v65, v65\n\t"
        "v_exp_f32 v66, v66\n\tv_exp_f32 v67, v67\n\tv_mfma_f32_32x32x16_bf16 v[80:95], v[180:183], v[132:135], v[80:95]\n\tv_exp_f32 v68, v68\n\t"
        "v_exp_f32 v69, v69\n\tv_exp_f32 v70, v70\n\tv_exp_f32 v71, v71\n\tv_smfmac_f32_32x32x32_bf16 v[0:15], v[184:187], v[96:103], v224\n\t"
        "v_exp_f32 v72, v72\n\tv_exp_f32 v73, v73\n\tv_exp_f32 v74, v74\n\tv_exp_f32 v75, v75\n\t"
        "v_smfmac_f32_32x32x32_bf16 v[0:15], v[188:191], v[104:111], v224\n\tv_exp_f32 v76, v76\n\t"
        "buffer_load_dwordx4 v[156:159], %[vo], %[rs], %[so] offen offset:3072 sc1\n\tv_exp_f32 v77, v77\n\tv_exp_f32 v78, v78\n\tv_exp_f32 v79, v79\n\t"
        "v_mfma_f32_32x32x16_bf16 v[96:111], v[176:179], v[136:139], 0\n\tv_exp_f32 v80, v80\n\tv_exp_f32 v81, v81\n\tv_exp_f32 v82, v82\n\t"
        "v_exp_f32 v83, v83\n\tv_mfma_f32_32x32x16_bf16 v[96:111], v[180:183], v[140:143], v[96:111]\n\tv_exp_f32 v84, v84\n\tv_exp_f32 v85, v85\n\t"
        "v_exp_f32 v86, v86\n\tv_exp_f32 v87, v87\n\tv_smfmac_f32_32x32x32_bf16 v[16:31], v[184:187], v[64:71], v224\n\tv_exp_f32 v88, v88\n\t"
        "v_exp_f32 v89, v89\n\tv_exp_f32 v90, v90\n\tv_exp_f32 v91, v91\n\tv_smfmac_f32_32x32x32_bf16 v[16:31], v[188:191], v[72:79], v224\n\t"
        "s_waitcnt vmcnt(8)\n\t13:\n\tv_max3_u32 v225, v192, v196, v200\n\tv_max_u32 v225, v225, v204\n\tv_cmp_eq_u32 vcc, -1, v225\n\ts_cbranch_vccnz 33f\n\t"
        "53:\n\tv_exp_f32 v92, v92\n\ts_add_u32 %[so], %[kvb], 24576\n\tbuffer_load_dwordx4 v[160:163], %[vo], %[rs], %[so] offen offset:0 sc1\n\t"
        "v_exp_f32 v93, v93\n\tv_exp_f32 v94, v94\n\tv_exp_f32 v95, v95\n\tv_mfma_f32_32x32x16_bf16 v[64:79], v[192:195], v[112:115], 0\n\t"
        "v_exp_f32 v96, v96\n\tv_exp_f32 v97, v97\n\tv_exp_f32 v98, v98\n\tv_exp_f32 v99, v99\n\t"
        "v_mfma_f32_32x32x16_bf16 v[64:79], v[196:199], v[116:119], v[64:79]\n\tv_exp_f32 v100, v100\n\tv_exp_f32 v101, v101\n\tv_exp_f32 v102, v102\n\t"
        "v_exp_f32 v103, v103\n\tv_smfmac_f32_32x32x32_bf16 v[32:47], v[184:187], v[80:87], v224\n\tv_exp_f32 v104, v104\n\tv_exp_f32 v105, v105\n\t"
        "v_exp_f32 v106, v106\n\tv_exp_f32 v107, v107\n\tv_smfmac_f32_32x32x32_bf16 v[32:47], v[188:191], v[88:95], v224\n\tv_exp_f32 v108, v108\n\t"
        "buffer_load_dwordx4 v[164:167], %[vo], %[rs], %[so] offen offset:1024 sc1\n\tv_exp_f32 v109, v109\n\tv_exp_f32 v110, v110\n\tv_exp_f32 v111, v111\n\t"
        "v_mfma_f32_32x32x16_bf16 v[80:95], v[192:195], v[120:123], 0\n\tv_exp_f32 v64, v64\n\tv_exp_f32 v65, v65\n\tv_exp_f32 v66, v66\n\t"
        "v_exp_f32 v67, v67\n\tv_mfma_f32_32x32x16_bf16 v[80:95], v[196:199], v[124:127], v[80:95]\n\tv_exp_f32 v68, v68\n\tv_exp_f32 v69, v69\n\t"
        "v_exp_f32 v70, v70\n\tv_exp_f32 v71, v71\n\tv_smfmac_f32_32x32x32_bf16 v[48:63], v[184:187], v[96:103], v224\n\tv_exp_f32 v72, v72\n\t"
        "v_exp_f32 v73, v73\n\tv_exp_f32 v74, v74\n\tv_exp_f32 v75, v75\n\tv_smfmac_f32_32x32x32_bf16 v[48:63], v[188:191], v[104:111], v224\n\t"
        "v_exp_f32 v76, v76\n\tbuffer_load_dwordx4 v[168:171], %[vo], %[rs], %[so] offen offset:2048 sc1\n\tv_exp_f32 v77, v77\n\tv_exp_f32 v78, v78\n\t"
        "v_exp_f32 v79, v79\n\tv_mfma_f32_32x32x16_bf16 v[96:111], v[192:195], v[128:131], 0\n\tv_exp_f32 v80, v80\n\tv_exp_f32 v81, v81\n\t"
        "v_exp_f32 v82, v82\n\tv_exp_f32 v83, v83\n\tv_mfma_f32_32x32x16_bf16 v[96:111], v[196:199], v[132:135], v[96:111]\n\tv_exp_f32 v84, v84\n\t"
        "v_exp_f32 v85, v85\n\tv_exp_f32 v86, v86\n\tv_exp_f32 v87, v87\n\tv_smfmac_f32_32x32x32_bf16 v[0:15], v[200:203], v[64:71], v224\n\t"
        "v_exp_f32 v88, v88\n\tv_exp_f32 v89, v89\n\tv_exp_f32 v90, v90\n\tv_exp_f32 v91, v91\n\t"
        "v_smfmac_f32_32x32x32_bf16 v[0:15], v[204:207], v[72:79], v224\n\tv_exp_f32 v92, v92\n\t"
        "buffer_load_dwordx4 v[172:175], %[vo], %[rs], %[so] offen offset:3072 sc1\n\tv_exp_f32 v93, v93\n\tv_exp_f32 v94, v94\n\tv_exp_f32 v95, v95\n\t"
        "v_mfma_f32_32x32x16_bf16 v[64:79], v[192:195], v[136:139], 0\n\tv_exp_f32 v96, v96\n\tv_exp_f32 v97, v97\n\tv_exp_f32 v98, v98\n\t"
        "v_exp_f32 v99, v99\n\tv_mfma_f32_32x32x16_bf16 v[64:79], v[196:199], v[140:143], v[64:79]\n\tv_exp_f32 v100, v100\n\tv_exp_f32 v101, v101\n\t"
        "v_exp_f32 v102, v102\n\tv_exp_f32 v103, v103\n\tv_smfmac_f32_32x32x32_bf16 v[16:31], v[200:203], v[80:87], v224\n\tv_exp_f32 v104, v104\n\t"
        "v_exp_f32 v105, v105\n\tv_exp_f32 v106, v106\n\tv_exp_f32 v107, v107\n\tv_smfmac_f32_32x32x32_bf16 v[16:31], v[204:207], v[88:95], v224\n\t"
        "s_waitcnt vmcnt(8)\n\t14:\n\tv_max3_u32 v225, v208, v212, v216\n\tv_max_u32 v225, v225, v220\n\tv_cmp_eq_u32 vcc, -1, v225\n\ts_cbranch_vccnz 34f\n\t"
        "54:\n\tv_exp_f32 v108, v108\n\ts_add_u32 %[so], %[kvb], 28672\n\tbuffer_load_dwordx4 v[176:179], %[vo], %[rs], %[so] offen offset:0 sc1\n\t"
        "v_exp_f32 v109, v109\n\tv_exp_f32 v110, v110\n\tv_exp_f32 v111, v111\n\tv_mfma_f32_32x32x16_bf16 v[80:95], v[208:211], v[112:115], 0\n\t"
        "v_exp_f32 v64, v64\n\tv_exp_f32 v65, v65\n\tv_exp_f32 v66, v66\n\tv_exp_f32 v67, v67\n\t"
        "v_mfma_f32_32x32x16_bf16 v[80:95], v[212:215], v[116:119], v[80:95]\n\tv_exp_f32 v68, v68\n\tv_exp_f32 v69, v69\n\tv_exp_f32 v70, v70\n\t"
        "v_exp_f32 v71, v71\n\tv_smfmac_f32_32x32x32_bf16 v[32:47], v[200:203], v[96:103], v224\n\tv_exp_f32 v72, v72\n\tv_exp_f32 v73, v73\n\t"
        "v_exp_f32 v74, v74\n\tv_exp_f32 v75, v75\n\tv_smfmac_f32_32x32x32_bf16 v[32:47], v[204:207], v[104:111], v224\n\tv_exp_f32 v76, v76\n\t"
        "buffer_load_dwordx4 v[180:183], %[vo], %[rs], %[so] offen offset:1024 sc1\n\tv_exp_f32 v77, v77\n\tv_exp_f32 v78, v78\n\tv_exp_f32 v79, v79\n\t"
        "v_mfma_f32_32x32x16_bf16 v[96:111], v[208:211], v[120:123], 0\n\tv_exp_f32 v80, v80\n\tv_exp_f32 v81, v81\n\tv_exp_f32 v82, v82\n\t"
        "v_exp_f32 v83, v83\n\tv_mfma_f32_32x32x16_bf16 v[96:111], v[212:215], v[124:127], v[96:111]\n\tv_exp_f32 v84, v84\n\tv_exp_f32 v85, v85\n\t"
        "v_exp_f32 v86, v86\n\tv_exp_f32 v87, v87\n\tv_smfmac_f32_32x32x32_bf16 v[48:63], v[200:203], v[64:71], v224\n\tv_exp_f32 v88, v88\n\t"
        "v_exp_f32 v89, v89\n\tv_exp_f32 v90, v90\n\tv_exp_f32 v91, v91\n\tv_smfmac_f32_32x32x32_bf16 v[48:63], v[204:207], v[72:79], v224\n\t"
        "v_exp_f32 v92, v92\n\tbuffer_load_dwordx4 v[184:187], %[vo], %[rs], %[so] offen offset:2048 sc1\n\tv_exp_f32 v93, v93\n\tv_exp_f32 v94, v94\n\t"
        "v_exp_f32 v95, v95\n\tv_mfma_f32_32x32x16_bf16 v[64:79], v[208:211], v[128:131], 0\n\tv_exp_f32 v96, v96\n\tv_exp_f32 v97, v97\n\t"
        "v_exp_f32 v98, v98\n\tv_exp_f32 v99, v99\n\tv_mfma_f32_32x32x16_bf16 v[64:79], v[212:215], v[132:135], v[64:79]\n\tv_exp_f32 v100, v100\n\t"
        "v_exp_f32 v101, v101\n\tv_exp_f32 v102, v102\n\tv_exp_f32 v103, v103\n\tv_smfmac_f32_32x32x32_bf16 v[0:15], v[216:219], v[80:87], v224\n\t"
        "v_exp_f32 v104, v104\n\tv_exp_f32 v105, v105\n\tv_exp_f32 v106, v106\n\tv_exp_f32 v107, v107\n\t"
        "v_smfmac_f32_32x32x32_bf16 v[0:15], v[220:223], v[88:95], v224\n\tv_exp_f32 v108, v108\n\t"
        "buffer_load_dwordx4 v[188:191], %[vo], %[rs], %[so] offen offset:3072 sc1\n\tv_exp_f32 v109, v109\n\tv_exp_f32 v110, v110\n\tv_exp_f32 v111, v111\n\t"
        "v_mfma_f32_32x32x16_bf16 v[80:95], v[208:211], v[136:139], 0\n\tv_exp_f32 v64, v64\n\tv_exp_f32 v65, v65\n\tv_exp_f32 v66, v66\n\t"
        "v_exp_f32 v67, v67\n\tv_mfma_f32_32x32x16_bf16 v[80:95], v[212:215], v[140:143], v[80:95]\n\tv_exp_f32 v68, v68\n\tv_exp_f32 v69, v69\n\t"
        "v_exp_f32 v70, v70\n\tv_exp_f32 v71, v71\n\tv_smfmac_f32_32x32x32_bf16 v[16:31], v[216:219], v[96:103], v224\n\tv_exp_f32 v72, v72\n\t"
        "v_exp_f32 v73, v73\n\tv_exp_f32 v74, v74\n\tv_exp_f32 v75, v75\n\tv_smfmac_f32_32x32x32_bf16 v[16:31], v[220:223], v[104:111], v224\n\t"
        "s_waitcnt vmcnt(8)\n\t15:\n\tv_max3_u32 v225, v144, v148, v152\n\tv_max_u32 v225, v225, v156\n\tv_cmp_eq_u32 vcc, -1, v225\n\ts_cbranch_vccnz 35f\n\t"
        "55:\n\tv_exp_f32 v76, v76\n\ts_add_u32 %[so], %[kvb], 32768\n\tbuffer_load_dwordx4 v[192:195], %[vo], %[rs], %[so] offen offset:0 sc1\n\t"
        "v_exp_f32 v77, v77\n\tv_exp_f32 v78, v78\n\tv_exp_f32 v79, v79\n\tv_mfma_f32_32x32x16_bf16 v[96:111], v[144:147], v[112:115], 0\n\t"
        "v_exp_f32 v80, v80\n\tv_exp_f32 v81, v81\n\tv_exp_f32 v82, v82\n\tv_exp_f32 v83, v83\n\t"
        "v_mfma_f32_32x32x16_bf16 v[96:111], v[148:151], v[116:119], v[96:111]\n\tv_exp_f32 v84, v84\n\tv_exp_f32 v85, v85\n\tv_exp_f32 v86, v86\n\t"
        "v_exp_f32 v87, v87\n\tv_smfmac_f32_32x32x32_bf16 v[32:47], v[216:219], v[64:71], v224\n\tv_exp_f32 v88, v88\n\tv_exp_f32 v89, v89\n\t"
        "v_exp_f32 v90, v90\n\tv_exp_f32 v91, v91\n\tv_smfmac_f32_32x32x32_bf16 v[32:47], v[220:223], v[72:79], v224\n\tv_exp_f32 v92, v92\n\t"
        "buffer_load_dwordx4 v[196:199], %[vo], %[rs], %[so] offen offset:1024 sc1\n\tv_exp_f32 v93, v93\n\tv_exp_f32 v94, v94\n\tv_exp_f32 v95, v95\n\t"
        "v_mfma_f32_32x32x16_bf16 v[64:79], v[144:147], v[120:123], 0\n\tv_exp_f32 v96, v96\n\tv_exp_f32 v97, v97\n\tv_exp_f32 v98, v98\n\t"
        "v_exp_f32 v99, v99\n\tv_mfma_f32_32x32x16_bf16 v[64:79], v[148:151], v[124:127], v[64:79]\n\tv_exp_f32 v100, v100\n\tv_exp_f32 v101, v101\n\t"
        "v_exp_f32 v102, v102\n\tv_exp_f32 v103, v103\n\tv_smfmac_f32_32x32x32_bf16 v[48:63], v[216:219], v[80:87], v224\n\tv_exp_f32 v104, v104\n\t"
        "v_exp_f32 v105, v105\n\tv_exp_f32 v106, v106\n\tv_exp_f32 v107, v107\n\tv_smfmac_f32_32x32x32_bf16 v[48:63], v[220:223], v[88:95], v224\n\t"
        "v_exp_f32 v108, v108\n\tbuffer_load_dwordx4 v[200:203], %[vo], %[rs], %[so] offen offset:2048 sc1\n\tv_exp_f32 v109, v109\n\tv_exp_f32 v110, v110\n\t"
        "v_exp_f32 v111, v111\n\tv_mfma_f32_32x32x16_bf16 v[80:95], v[144:147], v[128:131], 0\n\tv_exp_f32 v64, v64\n\tv_exp_f32 v65, v65\n\t"
        "v_exp_f32 v66, v66\n\tv_exp_f32 v67, v67\n\tv_mfma_f32_32x32x16_bf16 v[80:95], v[148:151], v[132:135], v[80:95]\n\tv_exp_f32 v68, v68\n\t"
        "v_exp_f32 v69, v69\n\tv_exp_f32 v70, v70\n\tv_exp_f32 v71, v71\n\tv_smfmac_f32_32x32x32_bf16 v[0:15], v[152:155], v[96:103], v224\n\t"
        "v_exp_f32 v72, v72\n\tv_exp_f32 v73, v73\n\tv_exp_f32 v74, v74\n\tv_exp_f32 v75, v75\n\t"
        "v_smfmac_f32_32x32x32_bf16 v[0:15], v[156:159], v[104:111], v224\n\tv_exp_f32 v76, v76\n\t"
        "buffer_load_dwordx4 v[204:207], %[vo], %[rs], %[so] offen offset:3072 sc1\n\tv_exp_f32 v77, v77\n\tv_exp_f32 v78, v78\n\tv_exp_f32 v79, v79\n\t"
        "v_mfma_f32_32x32x16_bf16 v[96:111], v[144:147], v[136:139], 0\n\tv_exp_f32 v80, v80\n\tv_exp_f32 v81, v81\n\tv_exp_f32 v82, v82\n\t"
        "v_exp_f32 v83, v83\n\tv_mfma_f32_32x32x16_bf16 v[96:111], v[148:151], v[140:143], v[96:111]\n\tv_exp_f32 v84, v84\n\tv_exp_f32 v85, v85\n\t"
        "v_exp_f32 v86, v86\n\tv_exp_f32 v87, v87\n\tv_smfmac_f32_32x32x32_bf16 v[16:31], v[152:155], v[64:71], v224\n\tv_exp_f32 v88, v88\n\t"
        "v_exp_f32 v89, v89\n\tv_exp_f32 v90, v90\n\tv_exp_f32 v91, v91\n\tv_smfmac_f32_32x32x32_bf16 v[16:31], v[156:159], v[72:79], v224\n\t"
        "s_waitcnt vmcnt(8)\n\t16:\n\tv_max3_u32 v225, v160, v164, v168\n\tv_max_u32 v225, v225, v172\n\tv_cmp_eq_u32 vcc, -1, v225\n\ts_cbranch_vccnz 36f\n\t"
        "56:\n\tv_exp_f32 v92, v92\n\ts_add_u32 %[so], %[kvb], 36864\n\tbuffer_load_dwordx4 v[208:211], %[vo], %[rs], %[so] offen offset:0 sc1\n\t"
        "v_exp_f32 v93, v93\n\tv_exp_f32 v94, v94\n\tv_exp_f32 v95, v95\n\tv_mfma_f32_32x32x16_bf16 v[64:79], v[160:163], v[112:115], 0\n\t"
        "v_exp_f32 v96, v96\n\tv_exp_f32 v97, v97\n\tv_exp_f32 v98, v98\n\tv_exp_f32 v99, v99\n\t"
        "v_mfma_f32_32x32x16_bf16 v[64:79], v[164:167], v[116:119], v[64:79]\n\tv_exp_f32 v100, v100\n\tv_exp_f32 v101, v101\n\tv_exp_f32 v102, v102\n\t"
        "v_exp_f32 v103, v103\n\tv_smfmac_f32_32x32x32_bf16 v[32:47], v[152:155], v[80:87], v224\n\tv_exp_f32 v104, v104\n\tv_exp_f32 v105, v105\n\t"
        "v_exp_f32 v106, v106\n\tv_exp_f32 v107, v107\n\tv_smfmac_f32_32x32x32_bf16 v[32:47], v[156:159], v[88:95], v224\n\tv_exp_f32 v108, v108\n\t"
        "buffer_load_dwordx4 v[212:215], %[vo], %[rs], %[so] offen offset:1024 sc1\n\tv_exp_f32 v109, v109\n\tv_exp_f32 v110, v110\n\tv_exp_f32 v111, v111\n\t"
        "v_mfma_f32_32x32x16_bf16 v[80:95], v[160:163], v[120:123], 0\n\tv_exp_f32 v64, v64\n\tv_exp_f32 v65, v65\n\tv_exp_f32 v66, v66\n\t"
        "v_exp_f32 v67, v67\n\tv_mfma_f32_32x32x16_bf16 v[80:95], v[164:167], v[124:127], v[80:95]\n\tv_exp_f32 v68, v68\n\tv_exp_f32 v69, v69\n\t"
        "v_exp_f32 v70, v70\n\tv_exp_f32 v71, v71\n\tv_smfmac_f32_32x32x32_bf16 v[48:63], v[152:155], v[96:103], v224\n\tv_exp_f32 v72, v72\n\t"
        "v_exp_f32 v73, v73\n\tv_exp_f32 v74, v74\n\tv_exp_f32 v75, v75\n\tv_smfmac_f32_32x32x32_bf16 v[48:63], v[156:159], v[104:111], v224\n\t"
        "v_exp_f32 v76, v76\n\tbuffer_load_dwordx4 v[216:219], %[vo], %[rs], %[so] offen offset:2048 sc1\n\tv_exp_f32 v77, v77\n\tv_exp_f32 v78, v78\n\t"
        "v_exp_f32 v79, v79\n\tv_mfma_f32_32x32x16_bf16 v[96:111], v[160:163], v[128:131], 0\n\tv_exp_f32 v80, v80\n\tv_exp_f32 v81, v81\n\t"
        "v_exp_f32 v82, v82\n\tv_exp_f32 v83, v83\n\tv_mfma_f32_32x32x16_bf16 v[96:111], v[164:167], v[132:135], v[96:111]\n\tv_exp_f32 v84, v84\n\t"
        "v_exp_f32 v85, v85\n\tv_exp_f32 v86, v86\n\tv_exp_f32 v87, v87\n\tv_smfmac_f32_32x32x32_bf16 v[0:15], v[168:171], v[64:71], v224\n\t"
        "v_exp_f32 v88, v88\n\tv_exp_f32 v89, v89\n\tv_exp_f32 v90, v90\n\tv_exp_f32 v91, v91\n\t"
        "v_smfmac_f32_32x32x32_bf16 v[0:15], v[172:175], v[72:79], v224\n\tv_exp_f32 v92, v92\n\t"
        "buffer_load_dwordx4 v[220:223], %[vo], %[rs], %[so] offen offset:3072 sc1\n\tv_exp_f32 v93, v93\n\tv_exp_f32 v94, v94\n\tv_exp_f32 v95, v95\n\t"
        "v_mfma_f32_32x32x16_bf16 v[64:79], v[160:163], v[136:139], 0\n\tv_exp_f32 v96, v96\n\tv_exp_f32 v97, v97\n\tv_exp_f32 v98, v98\n\t"
        "v_exp_f32 v99, v99\n\tv_mfma_f32_32x32x16_bf16 v[64:79], v[164:167], v[140:143], v[64:79]\n\tv_exp_f32 v100, v100\n\tv_exp_f32 v101, v101\n\t"
        "v_exp_f32 v102, v102\n\tv_exp_f32 v103, v103\n\tv_smfmac_f32_32x32x32_bf16 v[16:31], v[168:171], v[80:87], v224\n\tv_exp_f32 v104, v104\n\t"
        "v_exp_f32 v105, v105\n\tv_exp_f32 v106, v106\n\tv_exp_f32 v107, v107\n\tv_smfmac_f32_32x32x32_bf16 v[16:31], v[172:175], v[88:95], v224\n\t"
        "s_waitcnt vmcnt(8)\n\t17:\n\tv_max3_u32 v225, v176, v180, v184\n\tv_max_u32 v225, v225, v188\n\tv_cmp_eq_u32 vcc, -1, v225\n\ts_cbranch_vccnz 37f\n\t"
        "57:\n\tv_exp_f32 v108, v108\n\ts_add_u32 %[so], %[kvb], 40960\n\tbuffer_load_dwordx4 v[144:147], %[vo], %[rs], %[so] offen offset:0 sc1\n\t"
        "v_exp_f32 v109, v109\n\tv_exp_f32 v110, v110\n\tv_exp_f32 v111, v111\n\tv_mfma_f32_32x32x16_bf16 v[80:95], v[176:179], v[112:115], 0\n\t"
        "v_exp_f32 v64, v64\n\tv_exp_f32 v65, v65\n\tv_exp_f32 v66, v66\n\tv_exp_f32 v67, v67\n\t"
        "v_mfma_f32_32x32x16_bf16 v[80:95], v[180:183], v[116:119], v[80:95]\n\tv_exp_f32 v68, v68\n\tv_exp_f32 v69, v69\n\tv_exp_f32 v70, v70\n\t"
        "v_exp_f32 v71, v71\n\tv_smfmac_f32_32x32x32_bf16 v[32:47], v[168:171], v[96:103], v224\n\tv_exp_f32 v72, v72\n\tv_exp_f32 v73, v73\n\t"
        "v_exp_f32 v74, v74\n\tv_exp_f32 v75, v75\n\tv_smfmac_f32_32x32x32_bf16 v[32:47], v[172:175], v[104:111], v224\n\tv_exp_f32 v76, v76\n\t"
        "buffer_load_dwordx4 v[148:151], %[vo], %[rs], %[so] offen offset:1024 sc1\n\tv_exp_f32 v77, v77\n\tv_exp_f32 v78, v78\n\tv_exp_f32 v79, v79\n\t"
        "v_mfma_f32_32x32x16_bf16 v[96:111], v[176:179], v[120:123], 0\n\tv_exp_f32 v80, v80\n\tv_exp_f32 v81, v81\n\tv_exp_f32 v82, v82\n\t"
        "v_exp_f32 v83, v83\n\tv_mfma_f32_32x32x16_bf16 v[96:111], v[180:183], v[124:127], v[96:111]\n\tv_exp_f32 v84, v84\n\tv_exp_f32 v85, v85\n\t"
        "v_exp_f32 v86, v86\n\tv_exp_f32 v87, v87\n\tv_smfmac_f32_32x32x32_bf16 v[48:63], v[168:171], v[64:71], v224\n\tv_exp_f32 v88, v88\n\t"
        "v_exp_f32 v89, v89\n\tv_exp_f32 v90, v90\n\tv_exp_f32 v91, v91\n\tv_smfmac_f32_32x32x32_bf16 v[48:63], v[172:175], v[72:79], v224\n\t"
        "v_exp_f32 v92, v92\n\tbuffer_load_dwordx4 v[152:155], %[vo], %[rs], %[so] offen offset:2048 sc1\n\tv_exp_f32 v93, v93\n\tv_exp_f32 v94, v94\n\t"
        "v_exp_f32 v95, v95\n\tv_mfma_f32_32x32x16_bf16 v[64:79], v[176:179], v[128:131], 0\n\tv_exp_f32 v96, v96\n\tv_exp_f32 v97, v97\n\t"
        "v_exp_f32 v98, v98\n\tv_exp_f32 v99, v99\n\tv_mfma_f32_32x32x16_bf16 v[64:79], v[180:183], v[132:135], v[64:79]\n\tv_exp_f32 v100, v100\n\t"
        "v_exp_f32 v101, v101\n\tv_exp_f32 v102, v102\n\tv_exp_f32 v103, v103\n\tv_smfmac_f32_32x32x32_bf16 v[0:15], v[184:187], v[80:87], v224\n\t"
        "v_exp_f32 v104, v104\n\tv_exp_f32 v105, v105\n\tv_exp_f32 v106, v106\n\tv_exp_f32 v107, v107\n\t"
        "v_smfmac_f32_32x32x32_bf16 v[0:15], v[188:191], v[88:95], v224\n\tv_exp_f32 v108, v108\n\t"
        "buffer_load_dwordx4 v[156:159], %[vo], %[rs], %[so] offen offset:3072 sc1\n\tv_exp_f32 v109, v109\n\tv_exp_f32 v110, v110\n\tv_exp_f32 v111, v111\n\t"
        "v_mfma_f32_32x32x16_bf16 v[80:95], v[176:179], v[136:139], 0\n\tv_exp_f32 v64, v64\n\tv_exp_f32 v65, v65\n\tv_exp_f32 v66, v66\n\t"
        "v_exp_f32 v67, v67\n\tv_mfma_f32_32x32x16_bf16 v[80:95], v[180:183], v[140:143], v[80:95]\n\tv_exp_f32 v68, v68\n\tv_exp_f32 v69, v69\n\t"
        "v_exp_f32 v70, v70\n\tv_exp_f32 v71, v71\n\tv_smfmac_f32_32x32x32_bf16 v[16:31], v[184:187], v[96:103], v224\n\tv_exp_f32 v72, v72\n\t"
        "v_exp_f32 v73, v73\n\tv_exp_f32 v74, v74\n\tv_exp_f32 v75, v75\n\tv_smfmac_f32_32x32x32_bf16 v[16:31], v[188:191], v[104:111], v224\n\t"
        "s_waitcnt vmcnt(8)\n\t18:\n\tv_max3_u32 v225, v192, v196, v200\n\tv_max_u32 v225, v225, v204\n\tv_cmp_eq_u32 vcc, -1, v225\n\ts_cbranch_vccnz 38f\n\t"
        "58:\n\tv_exp_f32 v76, v76\n\ts_add_u32 %[so], %[kvb], 45056\n\tbuffer_load_dwordx4 v[160:163], %[vo], %[rs], %[so] offen offset:0 sc1\n\t"
        "v_exp_f32 v77, v77\n\tv_exp_f32 v78, v78\n\tv_exp_f32 v79, v79\n\tv_mfma_f32_32x32x16_bf16 v[96:111], v[192:195], v[112:115], 0\n\t"
        "v_exp_f32 v80, v80\n\tv_exp_f32 v81, v81\n\tv_exp_f32 v82, v82\n\tv_exp_f32 v83, v83\n\t"
        "v_mfma_f32_32x32x16_bf16 v[96:111], v[196:199], v[116:119], v[96:111]\n\tv_exp_f32 v84, v84\n\tv_exp_f32 v85, v85\n\tv_exp_f32 v86, v86\n\t"
        "v_exp_f32 v87, v87\n\tv_smfmac_f32_32x32x32_bf16 v[32:47], v[184:187], v[64:71], v224\n\tv_exp_f32 v88, v88\n\tv_exp_f32 v89, v89\n\t"
        "v_exp_f32 v90, v90\n\tv_exp_f32 v91, v91\n\tv_smfmac_f32_32x32x32_bf16 v[32:47], v[188:191], v[72:79], v224\n\tv_exp_f32 v92, v92\n\t"
        "buffer_load_dwordx4 v[164:167], %[vo], %[rs], %[so] offen offset:1024 sc1\n\tv_exp_f32 v93, v93\n\tv_exp_f32 v94, v94\n\tv_exp_f32 v95, v95\n\t"
        "v_mfma_f32_32x32x16_bf16 v[64:79], v[192:195], v[120:123], 0\n\tv_exp_f32 v96, v96\n\tv_exp_f32 v97, v97\n\tv_exp_f32 v98, v98\n\t"
        "v_exp_f32 v99, v99\n\tv_mfma_f32_32x32x16_bf16 v[64:79], v[196:199], v[124:127], v[64:79]\n\tv_exp_f32 v100, v100\n\tv_exp_f32 v101, v101\n\t"
        "v_exp_f32 v102, v102\n\tv_exp_f32 v103, v103\n\tv_smfmac_f32_32x32x32_bf16 v[48:63], v[184:187], v[80:87], v224\n\tv_exp_f32 v104, v104\n\t"
        "v_exp_f32 v105, v105\n\tv_exp_f32 v106, v106\n\tv_exp_f32 v107, v107\n\tv_smfmac_f32_32x32x32_bf16 v[48:63], v[188:191], v[88:95], v224\n\t"
        "v_exp_f32 v108, v108\n\tbuffer_load_dwordx4 v[168:171], %[vo], %[rs], %[so] offen offset:2048 sc1\n\tv_exp_f32 v109, v109\n\tv_exp_f32 v110, v110\n\t"
        "v_exp_f32 v111, v111\n\tv_mfma_f32_32x32x16_bf16 v[80:95], v[192:195], v[128:131], 0\n\tv_exp_f32 v64, v64\n\tv_exp_f32 v65, v65\n\t"
        "v_exp_f32 v66, v66\n\tv_exp_f32 v67, v67\n\tv_mfma_f32_32x32x16_bf16 v[80:95], v[196:199], v[132:135], v[80:95]\n\tv_exp_f32 v68, v68\n\t"
        "v_exp_f32 v69, v69\n\tv_exp_f32 v70, v70\n\tv_exp_f32 v71, v71\n\tv_smfmac_f32_32x32x32_bf16 v[0:15], v[200:203], v[96:103], v224\n\t"
        "v_exp_f32 v72, v72\n\tv_exp_f32 v73, v73\n\tv_exp_f32 v74, v74\n\tv_exp_f32 v75, v75\n\t"
        "v_smfmac_f32_32x32x32_bf16 v[0:15], v[204:207], v[104:111], v224\n\tv_exp_f32 v76, v76\n\t"
        "buffer_load_dwordx4 v[172:175], %[vo], %[rs], %[so] offen offset:3072 sc1\n\tv_exp_f32 v77, v77\n\tv_exp_f32 v78, v78\n\tv_exp_f32 v79, v79\n\t"
        "v_mfma_f32_32x32x16_bf16 v[96:111], v[192:195], v[136:139], 0\n\tv_exp_f32 v80, v80\n\tv_exp_f32 v81, v81\n\tv_exp_f32 v82, v82\n\t"
        "v_exp_f32 v83, v83\n\tv_mfma_f32_32x32x16_bf16 v[96:111], v[196:199], v[140:143], v[96:111]\n\tv_exp_f32 v84, v84\n\tv_exp_f32 v85, v85\n\t"
        "v_exp_f32 v86, v86\n\tv_exp_f32 v87, v87\n\tv_smfmac_f32_32x32x32_bf16 v[16:31], v[200:203], v[64:71], v224\n\tv_exp_f32 v88, v88\n\t"
        "v_exp_f32 v89, v89\n\tv_exp_f32 v90, v90\n\tv_exp_f32 v91, v91\n\tv_smfmac_f32_32x32x32_bf16 v[16:31], v[204:207], v[72:79], v224\n\t"
        "s_waitcnt vmcnt(8)\n\t19:\n\tv_max3_u32 v225, v208, v212, v216\n\tv_max_u32 v225, v225, v220\n\tv_cmp_eq_u32 vcc, -1, v225\n\ts_cbranch_vccnz 39f\n\t"
        "59:\n\tv_exp_f32 v92, v92\n\ts_add_u32 %[so], %[kvb], 49152\n\tbuffer_load_dwordx4 v[176:179], %[vo], %[rs], %[so] offen offset:0 sc1\n\t"
        "v_exp_f32 v93, v93\n\tv_exp_f32 v94, v94\n\tv_exp_f32 v95, v95\n\tv_mfma_f32_32x32x16_bf16 v[64:79], v[208:211], v[112:115], 0\n\t"
        "v_exp_f32 v96, v96\n\tv_exp_f32 v97, v97\n\tv_exp_f32 v98, v98\n\tv_exp_f32 v99, v99\n\t"
        "v_mfma_f32_32x32x16_bf16 v[64:79], v[212:215], v[116:119], v[64:79]\n\tv_exp_f32 v100, v100\n\tv_exp_f32 v101, v101\n\tv_exp_f32 v102, v102\n\t"
        "v_exp_f32 v103, v103\n\tv_smfmac_f32_32x32x32_bf16 v[32:47], v[200:203], v[80:87], v224\n\tv_exp_f32 v104, v104\n\tv_exp_f32 v105, v105\n\t"
        "v_exp_f32 v106, v106\n\tv_exp_f32 v107, v107\n\tv_smfmac_f32_32x32x32_bf16 v[32:47], v[204:207], v[88:95], v224\n\tv_exp_f32 v108, v108\n\t"
        "buffer_load_dwordx4 v[180:183], %[vo], %[rs], %[so] offen offset:1024 sc1\n\tv_exp_f32 v109, v109\n\tv_exp_f32 v110, v110\n\tv_exp_f32 v111, v111\n\t"
        "v_mfma_f32_32x32x16_bf16 v[80:95], v[208:211], v[120:123], 0\n\tv_exp_f32 v64, v64\n\tv_exp_f32 v65, v65\n\tv_exp_f32 v66, v66\n\t"
        "v_exp_f32 v67, v67\n\tv_mfma_f32_32x32x16_bf16 v[80:95], v[212:215], v[124:127], v[80:95]\n\tv_exp_f32 v68, v68\n\tv_exp_f32 v69, v69\n\t"
        "v_exp_f32 v70, v70\n\tv_exp_f32 v71, v71\n\tv_smfmac_f32_32x32x32_bf16 v[48:63], v[200:203], v[96:103], v224\n\tv_exp_f32 v72, v72\n\t"
        "v_exp_f32 v73, v73\n\tv_exp_f32 v74, v74\n\tv_exp_f32 v75, v75\n\tv_smfmac_f32_32x32x32_bf16 v[48:63], v[204:207], v[104:111], v224\n\t"
        "v_exp_f32 v76, v76\n\tbuffer_load_dwordx4 v[184:187], %[vo], %[rs], %[so] offen offset:2048 sc1\n\tv_exp_f32 v77, v77\n\tv_exp_f32 v78, v78\n\t"
        "v_exp_f32 v79, v79\n\tv_mfma_f32_32x32x16_bf16 v[96:111], v[208:211], v[128:131], 0\n\tv_exp_f32 v80, v80\n\tv_exp_f32 v81, v81\n\t"
        "v_exp_f32 v82, v82\n\tv_exp_f32 v83, v83\n\tv_mfma_f32_32x32x16_bf16 v[96:111], v[212:215], v[132:135], v[96:111]\n\tv_exp_f32 v84, v84\n\t"
        "v_exp_f32 v85, v85\n\tv_exp_f32 v86, v86\n\tv_exp_f32 v87, v87\n\tv_smfmac_f32_32x32x32_bf16 v[0:15], v[216:219], v[64:71], v224\n\t"
        "v_exp_f32 v88, v88\n\tv_exp_f32 v89, v89\n\tv_exp_f32 v90, v90\n\tv_exp_f32 v91, v91\n\t"
        "v_smfmac_f32_32x32x32_bf16 v[0:15], v[220:223], v[72:79], v224\n\tv_exp_f32 v92, v92\n\t"
        "buffer_load_dwordx4 v[188:191], %[vo], %[rs], %[so] offen offset:3072 sc1\n\tv_exp_f32 v93, v93\n\tv_exp_f32 v94, v94\n\tv_exp_f32 v95, v95\n\t"
        "v_mfma_f32_32x32x16_bf16 v[64:79], v[208:211], v[136:139], 0\n\tv_exp_f32 v96, v96\n\tv_exp_f32 v97, v97\n\tv_exp_f32 v98, v98\n\t"
        "v_exp_f32 v99, v99\n\tv_mfma_f32_32x32x16_bf16 v[64:79], v[212:215], v[140:143], v[64:79]\n\tv_exp_f32 v100, v100\n\tv_exp_f32 v101, v101\n\t"
        "v_exp_f32 v102, v102\n\tv_exp_f32 v103, v103\n\tv_smfmac_f32_32x32x32_bf16 v[16:31], v[216:219], v[80:87], v224\n\tv_exp_f32 v104, v104\n\t"
        "v_exp_f32 v105, v105\n\tv_exp_f32 v106, v106\n\tv_exp_f32 v107, v107\n\tv_smfmac_f32_32x32x32_bf16 v[16:31], v[220:223], v[88:95], v224\n\t"
        "s_waitcnt vmcnt(10)\n\tv_exp_f32 v108, v108\n\ts_add_u32 %[so], %[kvb], 53248\n\t"
        "buffer_load_dwordx4 v[192:195], %[vo], %[rs], %[so] offen offset:0 sc1\n\tv_exp_f32 v109, v109\n\tv_exp_f32 v110, v110\n\tv_exp_f32 v111, v111\n\t"
        "v_mfma_f32_32x32x16_bf16 v[80:95], v[144:147], v[112:115], 0\n\tv_exp_f32 v64, v64\n\tv_exp_f32 v65, v65\n\tv_exp_f32 v66, v66\n\t"
        "v_exp_f32 v67, v67\n\tv_mfma_f32_32x32x16_bf16 v[80:95], v[148:151], v[116:119], v[80:95]\n\tv_exp_f32 v68, v68\n\tv_exp_f32 v69, v69\n\t"
        "v_exp_f32 v70, v70\n\tv_exp_f32 v71, v71\n\tv_smfmac_f32_32x32x32_bf16 v[32:47], v[216:219], v[96:103], v224\n\tv_exp_f32 v72, v72\n\t"
        "v_exp_f32 v73, v73\n\tv_exp_f32 v74, v74\n\tv_exp_f32 v75, v75\n\tv_smfmac_f32_32x32x32_bf16 v[32:47], v[220:223], v[104:111], v224\n\t"
        "v_exp_f32 v76, v76\n\tbuffer_load_dwordx4 v[196:199], %[vo], %[rs], %[so] offen offset:1024 sc1\n\tv_exp_f32 v77, v77\n\tv_exp_f32 v78, v78\n\t"
        "v_exp_f32 v79, v79\n\tv_mfma_f32_32x32x16_bf16 v[96:111], v[144:147], v[120:123], 0\n\tv_exp_f32 v80, v80\n\tv_exp_f32 v81, v81\n\t"
        "v_exp_f32 v82, v82\n\tv_exp_f32 v83, v83\n\tv_mfma_f32_32x32x16_bf16 v[96:111], v[148:151], v[124:127], v[96:111]\n\tv_exp_f32 v84, v84\n\t"
        "v_exp_f32 v85, v85\n\tv_exp_f32 v86, v86\n\tv_exp_f32 v87, v87\n\tv_smfmac_f32_32x32x32_bf16 v[48:63], v[216:219], v[64:71], v224\n\t"
        "v_exp_f32 v88, v88\n\tv_exp_f32 v89, v89\n\tv_exp_f32 v90, v90\n\tv_exp_f32 v91, v91\n\t"
        "v_smfmac_f32_32x32x32_bf16 v[48:63], v[220:223], v[72:79], v224\n\ts_waitcnt vmcnt(10)\n\tv_exp_f32 v92, v92\n\t"
        "buffer_load_dwordx4 v[200:203], %[vo], %[rs], %[so] offen offset:2048 sc1\n\tv_exp_f32 v93, v93\n\tv_exp_f32 v94, v94\n\tv_exp_f32 v95, v95\n\t"
        "v_mfma_f32_32x32x16_bf16 v[64:79], v[144:147], v[128:131], 0\n\tv_exp_f32 v96, v96\n\tv_exp_f32 v97, v97\n\tv_exp_f32 v98, v98\n\t"
        "v_exp_f32 v99, v99\n\tv_mfma_f32_32x32x16_bf16 v[64:79], v[148:151], v[132:135], v[64:79]\n\tv_exp_f32 v100, v100\n\tv_exp_f32 v101, v101\n\t"
        "v_exp_f32 v102, v102\n\tv_exp_f32 v103, v103\n\tv_smfmac_f32_32x32x32_bf16 v[0:15], v[152:155], v[80:87], v224\n\tv_exp_f32 v104, v104\n\t"
        "v_exp_f32 v105, v105\n\tv_exp_f32 v106, v106\n\tv_exp_f32 v107, v107\n\tv_smfmac_f32_32x32x32_bf16 v[0:15], v[156:159], v[88:95], v224\n\t"
        "v_exp_f32 v108, v108\n\tbuffer_load_dwordx4 v[204:207], %[vo], %[rs], %[so] offen offset:3072 sc1\n\ts_add_u32 %[so], %[kvb], 57344\n\t"
        "buffer_load_dwordx4 v[208:211], %[vo], %[rs], %[so] offen offset:0 sc1\n\tv_exp_f32 v109, v109\n\tv_exp_f32 v110, v110\n\tv_exp_f32 v111, v111\n\t"
        "v_mfma_f32_32x32x16_bf16 v[80:95], v[144:147], v[136:139], 0\n\tv_exp_f32 v64, v64\n\tv_exp_f32 v65, v65\n\tv_exp_f32 v66, v66\n\t"
        "v_exp_f32 v67, v67\n\tv_mfma_f32_32x32x16_bf16 v[80:95], v[148:151], v[140:143], v[80:95]\n\tv_exp_f32 v68, v68\n\tv_exp_f32 v69, v69\n\t"
        "v_exp_f32 v70, v70\n\tv_exp_f32 v71, v71\n\tv_smfmac_f32_32x32x32_bf16 v[16:31], v[152:155], v[96:103], v224\n\tv_exp_f32 v72, v72\n\t"
        "v_exp_f32 v73, v73\n\tv_exp_f32 v74, v74\n\tv_exp_f32 v75, v75\n\tv_smfmac_f32_32x32x32_bf16 v[16:31], v[156:159], v[104:111], v224\n\t"
        "s_waitcnt vmcnt(11)\n\tv_exp_f32 v76, v76\n\tbuffer_load_dwordx4 v[212:215], %[vo], %[rs], %[so] offen offset:1024 sc1\n\t"
        "buffer_load_dwordx4 v[216:219], %[vo], %[rs], %[so] offen offset:2048 sc1\n\tv_exp_f32 v77, v77\n\tv_exp_f32 v78, v78\n\tv_exp_f32 v79, v79\n\t"
        "v_mfma_f32_32x32x16_bf16 v[96:111], v[160:163], v[112:115], 0\n\tv_exp_f32 v80, v80\n\tv_exp_f32 v81, v81\n\tv_exp_f32 v82, v82\n\t"
        "v_exp_f32 v83, v83\n\tv_mfma_f32_32x32x16_bf16 v[96:111], v[164:167], v[116:119], v[96:111]\n\tv_exp_f32 v84, v84\n\tv_exp_f32 v85, v85\n\t"
        "v_exp_f32 v86, v86\n\tv_exp_f32 v87, v87\n\tv_smfmac_f32_32x32x32_bf16 v[32:47], v[152:155], v[64:71], v224\n\tv_exp_f32 v88, v88\n\t"
        "v_exp_f32 v89, v89\n\tv_exp_f32 v90, v90\n\tv_exp_f32 v91, v91\n\tv_smfmac_f32_32x32x32_bf16 v[32:47], v[156:159], v[72:79], v224\n\t"
        "v_exp_f32 v92, v92\n\tbuffer_load_dwordx4 v[220:223], %[vo], %[rs], %[so] offen offset:3072 sc1\n\ts_add_u32 %[so], %[kvb], 61440\n\t"
        "buffer_load_dwordx4 v[144:147], %[vo], %[rs], %[so] offen offset:0 sc1\n\tv_exp_f32 v93, v93\n\tv_exp_f32 v94, v94\n\tv_exp_f32 v95, v95\n\t"
        "v_mfma_f32_32x32x16_bf16 v[64:79], v[160:163], v[120:123], 0\n\tv_exp_f32 v96, v96\n\tv_exp_f32 v97, v97\n\tv_exp_f32 v98, v98\n\t"
        "v_exp_f32 v99, v99\n\tv_mfma_f32_32x32x16_bf16 v[64:79], v[164:167], v[124:127], v[64:79]\n\tv_exp_f32 v100, v100\n\tv_exp_f32 v101, v101\n\t"
        "v_exp_f32 v102, v102\n\tv_exp_f32 v103, v103\n\tv_smfmac_f32_32x32x32_bf16 v[48:63], v[152:155], v[80:87], v224\n\tv_exp_f32 v104, v104\n\t"
        "v_exp_f32 v105, v105\n\tv_exp_f32 v106, v106\n\tv_exp_f32 v107, v107\n\tv_smfmac_f32_32x32x32_bf16 v[48:63], v[156:159], v[88:95], v224\n\t"
        "s_waitcnt vmcnt(13)\n\tv_exp_f32 v108, v108\n\tbuffer_load_dwordx4 v[148:151], %[vo], %[rs], %[so] offen offset:1024 sc1\n\t"
        "buffer_load_dwordx4 v[152:155], %[vo], %[rs], %[so] offen offset:2048 sc1\n\tv_exp_f32 v109, v109\n\tv_exp_f32 v110, v110\n\tv_exp_f32 v111, v111\n\t"
        "v_mfma_f32_32x32x16_bf16 v[80:95], v[160:163], v[128:131], 0\n\tv_exp_f32 v64, v64\n\tv_exp_f32 v65, v65\n\tv_exp_f32 v66, v66\n\t"
        "v_exp_f32 v67, v67\n\tv_mfma_f32_32x32x16_bf16 v[80:95], v[164:167], v[132:135], v[80:95]\n\tv_exp_f32 v68, v68\n\tv_exp_f32 v69, v69\n\t"
        "v_exp_f32 v70, v70\n\tv_exp_f32 v71, v71\n\tv_smfmac_f32_32x32x32_bf16 v[0:15], v[168:171], v[96:103], v224\n\tv_exp_f32 v72, v72\n\t"
        "v_exp_f32 v73, v73\n\tv_exp_f32 v74, v74\n\tv_exp_f32 v75, v75\n\tv_smfmac_f32_32x32x32_bf16 v[0:15], v[172:175], v[104:111], v224\n\t"
        "v_exp_f32 v76, v76\n\tbuffer_load_dwordx4 v[156:159], %[vo], %[rs], %[so] offen offset:3072 sc1\n\tv_exp_f32 v77, v77\n\tv_exp_f32 v78, v78\n\t"
        "v_exp_f32 v79, v79\n\tv_mfma_f32_32x32x16_bf16 v[96:111], v[160:163], v[136:139], 0\n\tv_exp_f32 v80, v80\n\tv_exp_f32 v81, v81\n\t"
        "v_exp_f32 v82, v82\n\tv_exp_f32 v83, v83\n\tv_mfma_f32_32x32x16_bf16 v[96:111], v[164:167], v[140:143], v[96:111]\n\tv_exp_f32 v84, v84\n\t"
        "v_exp_f32 v85, v85\n\tv_exp_f32 v86, v86\n\tv_exp_f32 v87, v87\n\tv_smfmac_f32_32x32x32_bf16 v[16:31], v[168:171], v[64:71], v224\n\t"
        "v_exp_f32 v88, v88\n\tv_exp_f32 v89, v89\n\tv_exp_f32 v90, v90\n\tv_exp_f32 v91, v91\n\t"
        "v_smfmac_f32_32x32x32_bf16 v[16:31], v[172:175], v[72:79], v224\n\ts_waitcnt vmcnt(14)\n\tv_exp_f32 v92, v92\n\tv_exp_f32 v93, v93\n\t"
        "v_exp_f32 v94, v94\n\tv_exp_f32 v95, v95\n\tv_mfma_f32_32x32x16_bf16 v[64:79], v[176:179], v[112:115], 0\n\tv_exp_f32 v96, v96\n\t"
        "v_exp_f32 v97, v97\n\tv_exp_f32 v98, v98\n\tv_exp_f32 v99, v99\n\tv_mfma_f32_32x32x16_bf16 v[64:79], v[180:183], v[116:119], v[64:79]\n\t"
        "v_exp_f32 v100, v100\n\tv_exp_f32 v101, v101\n\tv_exp_f32 v102, v102\n\tv_exp_f32 v103, v103\n\t"
        "v_smfmac_f32_32x32x32_bf16 v[32:47], v[168:171], v[80:87], v224\n\tv_exp_f32 v104, v104\n\tv_exp_f32 v105, v105\n\tv_exp_f32 v106, v106\n\t"
        "v_exp_f32 v107, v107\n\tv_smfmac_f32_32x32x32_bf16 v[32:47], v[172:175], v[88:95], v224\n\tv_exp_f32 v108, v108\n\tv_exp_f32 v109, v109\n\t"
        "v_exp_f32 v110, v110\n\tv_exp_f32 v111, v111\n\tv_mfma_f32_32x32x16_bf16 v[80:95], v[176:179], v[120:123], 0\n\tv_exp_f32 v64, v64\n\t"
        "v_exp_f32 v65, v65\n\tv_exp_f32 v66, v66\n\tv_exp_f32 v67, v67\n\tv_mfma_f32_32x32x16_bf16 v[80:95], v[180:183], v[124:127], v[80:95]\n\t"
        "v_exp_f32 v68, v68\n\tv_exp_f32 v69, v69\n\tv_exp_f32 v70, v70\n\tv_exp_f32 v71, v71\n\t"
        "v_smfmac_f32_32x32x32_bf16 v[48:63], v[168:171], v[96:103], v224\n\tv_exp_f32 v72, v72\n\tv_exp_f32 v73, v73\n\tv_exp_f32 v74, v74\n\t"
        "v_exp_f32 v75, v75\n\tv_smfmac_f32_32x32x32_bf16 v[48:63], v[172:175], v[104:111], v224\n\ts_waitcnt vmcnt(12)\n\tv_exp_f32 v76, v76\n\t"
        "v_exp_f32 v77, v77\n\tv_exp_f32 v78, v78\n\tv_exp_f32 v79, v79\n\tv_mfma_f32_32x32x16_bf16 v[96:111], v[176:179], v[128:131], 0\n\t"
        "v_exp_f32 v80, v80\n\tv_exp_f32 v81, v81\n\tv_exp_f32 v82, v82\n\tv_exp_f32 v83, v83\n\t"
        "v_mfma_f32_32x32x16_bf16 v[96:111], v[180:183], v[132:135], v[96:111]\n\tv_exp_f32 v84, v84\n\tv_exp_f32 v85, v85\n\tv_exp_f32 v86, v86\n\t"
        "v_exp_f32 v87, v87\n\tv_smfmac_f32_32x32x32_bf16 v[0:15], v[184:187], v[64:71], v224\n\tv_exp_f32 v88, v88\n\tv_exp_f32 v89, v89\n\t"
        "v_exp_f32 v90, v90\n\tv_exp_f32 v91, v91\n\tv_smfmac_f32_32x32x32_bf16 v[0:15], v[188:191], v[72:79], v224\n\tv_exp_f32 v92, v92\n\t"
        "v_exp_f32 v93, v93\n\tv_exp_f32 v94, v94\n\tv_exp_f32 v95, v95\n\tv_mfma_f32_32x32x16_bf16 v[64:79], v[176:179], v[136:139], 0\n\t"
        "v_exp_f32 v96, v96\n\tv_exp_f32 v97, v97\n\tv_exp_f32 v98, v98\n\tv_exp_f32 v99, v99\n\t"
        "v_mfma_f32_32x32x16_bf16 v[64:79], v[180:183], v[140:143], v[64:79]\n\tv_exp_f32 v100, v100\n\tv_exp_f32 v101, v101\n\tv_exp_f32 v102, v102\n\t"
        "v_exp_f32 v103, v103\n\tv_smfmac_f32_32x32x32_bf16 v[16:31], v[184:187], v[80:87], v224\n\tv_exp_f32 v104, v104\n\tv_exp_f32 v105, v105\n\t"
        "v_exp_f32 v106, v106\n\tv_exp_f32 v107, v107\n\tv_smfmac_f32_32x32x32_bf16 v[16:31], v[188:191], v[88:95], v224\n\ts_waitcnt vmcnt(10)\n\t"
        "v_exp_f32 v108, v108\n\tv_exp_f32 v109, v109\n\tv_exp_f32 v110, v110\n\tv_exp_f32 v111, v111\n\t"
        "v_mfma_f32_32x32x16_bf16 v[80:95], v[192:195], v[112:115], 0\n\tv_exp_f32 v64, v64\n\tv_exp_f32 v65, v65\n\tv_exp_f32 v66, v66\n\t"
        "v_exp_f32 v67, v67\n\tv_mfma_f32_32x32x16_bf16 v[80:95], v[196:199], v[116:119], v[80:95]\n\tv_exp_f32 v68, v68\n\tv_exp_f32 v69, v69\n\t"
        "v_exp_f32 v70, v70\n\tv_exp_f32 v71, v71\n\tv_smfmac_f32_32x32x32_bf16 v[32:47], v[184:187], v[96:103], v224\n\tv_exp_f32 v72, v72\n\t"
        "v_exp_f32 v73, v73\n\tv_exp_f32 v74, v74\n\tv_exp_f32 v75, v75\n\tv_smfmac_f32_32x32x32_bf16 v[32:47], v[188:191], v[104:111], v224\n\t"
        "s_waitcnt vmcnt(6)\n\tv_exp_f32 v76, v76\n\tv_exp_f32 v77, v77\n\tv_exp_f32 v78, v78\n\tv_exp_f32 v79, v79\n\t"
        "v_mfma_f32_32x32x16_bf16 v[96:111], v[208:211], v[112:115], 0\n\tv_exp_f32 v80, v80\n\tv_exp_f32 v81, v81\n\tv_exp_f32 v82, v82\n\t"
        "v_exp_f32 v83, v83\n\tv_mfma_f32_32x32x16_bf16 v[96:111], v[212:215], v[116:119], v[96:111]\n\tv_exp_f32 v84, v84\n\tv_exp_f32 v85, v85\n\t"
        "v_exp_f32 v86, v86\n\tv_exp_f32 v87, v87\n\tv_smfmac_f32_32x32x32_bf16 v[48:63], v[184:187], v[64:71], v224\n\tv_exp_f32 v88, v88\n\t"
        "v_exp_f32 v89, v89\n\tv_exp_f32 v90, v90\n\tv_exp_f32 v91, v91\n\tv_smfmac_f32_32x32x32_bf16 v[48:63], v[188:191], v[72:79], v224\n\t"
        "s_waitcnt vmcnt(2)\n\tv_exp_f32 v92, v92\n\tv_exp_f32 v93, v93\n\tv_exp_f32 v94, v94\n\tv_exp_f32 v95, v95\n\t"
        "v_mfma_f32_32x32x16_bf16 v[64:79], v[144:147], v[112:115], 0\n\tv_exp_f32 v96, v96\n\tv_exp_f32 v97, v97\n\tv_exp_f32 v98, v98\n\t"
        "v_exp_f32 v99, v99\n\tv_mfma_f32_32x32x16_bf16 v[64:79], v[148:151], v[116:119], v[64:79]\n\tv_exp_f32 v100, v100\n\tv_exp_f32 v101, v101\n\t"
        "v_exp_f32 v102, v102\n\tv_exp_f32 v103, v103\n\tv_smfmac_f32_32x32x32_bf16 v[0:15], v[200:203], v[80:87], v224\n\tv_exp_f32 v104, v104\n\t"
        "v_exp_f32 v105, v105\n\tv_exp_f32 v106, v106\n\tv_exp_f32 v107, v107\n\tv_smfmac_f32_32x32x32_bf16 v[0:15], v[204:207], v[88:95], v224\n\t"
        "v_exp_f32 v108, v108\n\tv_exp_f32 v109, v109\n\tv_exp_f32 v110, v110\n\tv_exp_f32 v111, v111\n\t"
        "v_mfma_f32_32x32x16_bf16 v[80:95], v[192:195], v[120:123], 0\n\tv_exp_f32 v64, v64\n\tv_exp_f32 v65, v65\n\tv_exp_f32 v66, v66\n\t"
        "v_exp_f32 v67, v67\n\tv_mfma_f32_32x32x16_bf16 v[80:95], v[196:199], v[124:127], v[80:95]\n\tv_exp_f32 v68, v68\n\tv_exp_f32 v69, v69\n\t"
        "v_exp_f32 v70, v70\n\tv_exp_f32 v71, v71\n\tv_smfmac_f32_32x32x32_bf16 v[0:15], v[216:219], v[96:103], v224\n\tv_exp_f32 v72, v72\n\t"
        "v_exp_f32 v73, v73\n\tv_exp_f32 v74, v74\n\tv_exp_f32 v75, v75\n\tv_smfmac_f32_32x32x32_bf16 v[0:15], v[220:223], v[104:111], v224\n\t"
        "s_waitcnt vmcnt(0)\n\tv_exp_f32 v76, v76\n\tv_exp_f32 v77, v77\n\tv_exp_f32 v78, v78\n\tv_exp_f32 v79, v79\n\t"
        "v_mfma_f32_32x32x16_bf16 v[96:111], v[208:211], v[120:123], 0\n\tv_exp_f32 v80, v80\n\tv_exp_f32 v81, v81\n\tv_exp_f32 v82, v82\n\t"
        "v_exp_f32 v83, v83\n\tv_mfma_f32_32x32x16_bf16 v[96:111], v[212:215], v[124:127], v[96:111]\n\tv_exp_f32 v84, v84\n\tv_exp_f32 v85, v85\n\t"
        "v_exp_f32 v86, v86\n\tv_exp_f32 v87, v87\n\tv_smfmac_f32_32x32x32_bf16 v[0:15], v[152:155], v[64:71], v224\n\tv_exp_f32 v88, v88\n\t"
        "v_exp_f32 v89, v89\n\tv_exp_f32 v90, v90\n\tv_exp_f32 v91, v91\n\tv_smfmac_f32_32x32x32_bf16 v[0:15], v[156:159], v[72:79], v224\n\t"
        "v_exp_f32 v92, v92\n\tv_exp_f32 v93, v93\n\tv_exp_f32 v94, v94\n\tv_exp_f32 v95, v95\n\t"
        "v_mfma_f32_32x32x16_bf16 v[64:79], v[144:147], v[120:123], 0\n\tv_exp_f32 v96, v96\n\tv_exp_f32 v97, v97\n\tv_exp_f32 v98, v98\n\t"
        "v_exp_f32 v99, v99\n\tv_mfma_f32_32x32x16_bf16 v[64:79], v[148:151], v[124:127], v[64:79]\n\tv_exp_f32 v100, v100\n\tv_exp_f32 v101, v101\n\t"
        "v_exp_f32 v102, v102\n\tv_exp_f32 v103, v103\n\tv_smfmac_f32_32x32x32_bf16 v[16:31], v[200:203], v[80:87], v224\n\tv_exp_f32 v104, v104\n\t"
        "v_exp_f32 v105, v105\n\tv_exp_f32 v106, v106\n\tv_exp_f32 v107, v107\n\tv_smfmac_f32_32x32x32_bf16 v[16:31], v[204:207], v[88:95], v224\n\t"
        "v_exp_f32 v108, v108\n\tv_exp_f32 v109, v109\n\tv_exp_f32 v110, v110\n\tv_exp_f32 v111, v111\n\tds_write_b128 %[p0], v[0:3] offset:0\n\t"
        "ds_write_b128 %[p0], v[4:7] offset:1024\n\tds_write_b128 %[p0], v[8:11] offset:2048\n\tds_write_b128 %[p0], v[12:15] offset:3072\n\t"
        "v_mfma_f32_32x32x16_bf16 v[80:95], v[192:195], v[128:131], 0\n\tv_exp_f32 v64, v64\n\tv_exp_f32 v65, v65\n\tv_exp_f32 v66, v66\n\t"
        "v_exp_f32 v67, v67\n\tv_mfma_f32_32x32x16_bf16 v[80:95], v[196:199], v[132:135], v[80:95]\n\tv_exp_f32 v68, v68\n\tv_exp_f32 v69, v69\n\t"
        "v_exp_f32 v70, v70\n\tv_exp_f32 v71, v71\n\tv_smfmac_f32_32x32x32_bf16 v[16:31], v[216:219], v[96:103], v224\n\tv_exp_f32 v72, v72\n\t"
        "v_exp_f32 v73, v73\n\tv_exp_f32 v74, v74\n\tv_exp_f32 v75, v75\n\tv_smfmac_f32_32x32x32_bf16 v[16:31], v[220:223], v[104:111], v224\n\t"
        "v_exp_f32 v76, v76\n\tv_exp_f32 v77, v77\n\tv_exp_f32 v78, v78\n\tv_exp_f32 v79, v79\n\t"
        "v_mfma_f32_32x32x16_bf16 v[96:111], v[208:211], v[128:131], 0\n\tv_exp_f32 v80, v80\n\tv_exp_f32 v81, v81\n\tv_exp_f32 v82, v82\n\t"
        "v_exp_f32 v83, v83\n\tv_mfma_f32_32x32x16_bf16 v[96:111], v[212:215], v[132:135], v[96:111]\n\tv_exp_f32 v84, v84\n\tv_exp_f32 v85, v85\n\t"
        "v_exp_f32 v86, v86\n\tv_exp_f32 v87, v87\n\tv_smfmac_f32_32x32x32_bf16 v[16:31], v[152:155], v[64:71], v224\n\tv_exp_f32 v88, v88\n\t"
        "v_exp_f32 v89, v89\n\tv_exp_f32 v90, v90\n\tv_exp_f32 v91, v91\n\tv_smfmac_f32_32x32x32_bf16 v[16:31], v[156:159], v[72:79], v224\n\t"
        "v_exp_f32 v92, v92\n\tv_exp_f32 v93, v93\n\tv_exp_f32 v94, v94\n\tv_exp_f32 v95, v95\n\t"
        "v_mfma_f32_32x32x16_bf16 v[64:79], v[144:147], v[128:131], 0\n\tv_exp_f32 v96, v96\n\tv_exp_f32 v97, v97\n\tv_exp_f32 v98, v98\n\t"
        "v_exp_f32 v99, v99\n\tv_mfma_f32_32x32x16_bf16 v[64:79], v[148:151], v[132:135], v[64:79]\n\tv_exp_f32 v100, v100\n\tv_exp_f32 v101, v101\n\t"
        "v_exp_f32 v102, v102\n\tv_exp_f32 v103, v103\n\tv_smfmac_f32_32x32x32_bf16 v[32:47], v[200:203], v[80:87], v224\n\tv_exp_f32 v104, v104\n\t"
        "v_exp_f32 v105, v105\n\tv_exp_f32 v106, v106\n\tv_exp_f32 v107, v107\n\tv_smfmac_f32_32x32x32_bf16 v[32:47], v[204:207], v[88:95], v224\n\t"
        "v_exp_f32 v108, v108\n\tv_exp_f32 v109, v109\n\tv_exp_f32 v110, v110\n\tv_exp_f32 v111, v111\n\tds_write_b128 %[p1], v[16:19] offset:0\n\t"
        "ds_write_b128 %[p1], v[20:23] offset:1024\n\tds_write_b128 %[p1], v[24:27] offset:2048\n\tds_write_b128 %[p1], v[28:31] offset:3072\n\t"
        "v_mfma_f32_32x32x16_bf16 v[80:95], v[192:195], v[136:139], 0\n\tv_exp_f32 v64, v64\n\tv_exp_f32 v65, v65\n\tv_exp_f32 v66, v66\n\t"
        "v_exp_f32 v67, v67\n\tv_mfma_f32_32x32x16_bf16 v[80:95], v[196:199], v[140:143], v[80:95]\n\tv_exp_f32 v68, v68\n\tv_exp_f32 v69, v69\n\t"
        "v_exp_f32 v70, v70\n\tv_exp_f32 v71, v71\n\tv_smfmac_f32_32x32x32_bf16 v[32:47], v[216:219], v[96:103], v224\n\tv_exp_f32 v72, v72\n\t"
        "v_exp_f32 v73, v73\n\tv_exp_f32 v74, v74\n\tv_exp_f32 v75, v75\n\tv_smfmac_f32_32x32x32_bf16 v[32:47], v[220:223], v[104:111], v224\n\t"
        "v_exp_f32 v76, v76\n\tv_exp_f32 v77, v77\n\tv_exp_f32 v78, v78\n\tv_exp_f32 v79, v79\n\t"
        "v_mfma_f32_32x32x16_bf16 v[96:111], v[208:211], v[136:139], 0\n\tv_exp_f32 v80, v80\n\tv_exp_f32 v81, v81\n\tv_exp_f32 v82, v82\n\t"
        "v_exp_f32 v83, v83\n\tv_mfma_f32_32x32x16_bf16 v[96:111], v[212:215], v[140:143], v[96:111]\n\tv_exp_f32 v84, v84\n\tv_exp_f32 v85, v85\n\t"
        "v_exp_f32 v86, v86\n\tv_exp_f32 v87, v87\n\tv_smfmac_f32_32x32x32_bf16 v[32:47], v[152:155], v[64:71], v224\n\tv_exp_f32 v88, v88\n\t"
        "v_exp_f32 v89, v89\n\tv_exp_f32 v90, v90\n\tv_exp_f32 v91, v91\n\tv_smfmac_f32_32x32x32_bf16 v[32:47], v[156:159], v[72:79], v224\n\t"
        "v_exp_f32 v92, v92\n\tv_exp_f32 v93, v93\n\tv_exp_f32 v94, v94\n\tv_exp_f32 v95, v95\n\t"
        "v_mfma_f32_32x32x16_bf16 v[64:79], v[144:147], v[136:139], 0\n\tv_exp_f32 v96, v96\n\tv_exp_f32 v97, v97\n\tv_exp_f32 v98, v98\n\t"
        "v_exp_f32 v99, v99\n\tv_mfma_f32_32x32x16_bf16 v[64:79], v[148:151], v[140:143], v[64:79]\n\tv_exp_f32 v100, v100\n\tv_exp_f32 v101, v101\n\t"
        "v_exp_f32 v102, v102\n\tv_exp_f32 v103, v103\n\tv_smfmac_f32_32x32x32_bf16 v[48:63], v[200:203], v[80:87], v224\n\tv_exp_f32 v104, v104\n\t"
        "v_exp_f32 v105, v105\n\tv_exp_f32 v106, v106\n\tv_exp_f32 v107, v107\n\tv_smfmac_f32_32x32x32_bf16 v[48:63], v[204:207], v[88:95], v224\n\t"
        "v_exp_f32 v108, v108\n\tv_exp_f32 v109, v109\n\tv_exp_f32 v110, v110\n\tv_exp_f32 v111, v111\n\tds_write_b128 %[p2], v[32:35] offset:0\n\t"
        "ds_write_b128 %[p2], v[36:39] offset:1024\n\tds_write_b128 %[p2], v[40:43] offset:2048\n\tds_write_b128 %[p2], v[44:47] offset:3072\n\t"
        "v_exp_f32 v64, v64\n\tv_exp_f32 v65, v65\n\tv_exp_f32 v66, v66\n\tv_exp_f32 v67, v67\n\tv_exp_f32 v68, v68\n\tv_exp_f32 v69, v69\n\t"
        "v_exp_f32 v70, v70\n\tv_exp_f32 v71, v71\n\tv_smfmac_f32_32x32x32_bf16 v[48:63], v[216:219], v[96:103], v224\n\tv_exp_f32 v72, v72\n\t"
        "v_exp_f32 v73, v73\n\tv_exp_f32 v74, v74\n\tv_exp_f32 v75, v75\n\tv_smfmac_f32_32x32x32_bf16 v[48:63], v[220:223], v[104:111], v224\n\t"
        "s_waitcnt lgkmcnt(0)\n\ts_barrier\n\tds_read_b128 v[0:3], %[pr] offset:0\n\tds_read_b128 v[4:7], %[pr] offset:1024\n\t"
        "ds_read_b128 v[8:11], %[pr] offset:2048\n\tds_read_b128 v[12:15], %[pr] offset:3072\n\tds_read_b128 v[16:19], %[pr] offset:4096\n\t"
        "ds_read_b128 v[20:23], %[pr] offset:5120\n\tds_read_b128 v[24:27], %[pr] offset:6144\n\tds_read_b128 v[28:31], %[pr] offset:7168\n\t"
        "ds_read_b128 v[32:35], %[pr] offset:8192\n\tds_read_b128 v[36:39], %[pr] offset:9216\n\tds_read_b128 v[40:43], %[pr] offset:10240\n\t"
        "ds_read_b128 v[44:47], %[pr] offset:11264\n\tv_exp_f32 v76, v76\n\tv_exp_f32 v77, v77\n\tv_exp_f32 v78, v78\n\tv_exp_f32 v79, v79\n\ts_nop 1\n\t"
        "v_smfmac_f32_32x32x32_bf16 v[48:63], v[152:155], v[64:71], v224\n\tv_smfmac_f32_32x32x32_bf16 v[48:63], v[156:159], v[72:79], v224\n\t"
        "s_waitcnt lgkmcnt(0)\n\ts_barrier\n\tv_pk_add_f32 v[0:1], v[0:1], v[16:17]\n\tv_pk_add_f32 v[2:3], v[2:3], v[18:19]\n\t"
        "v_pk_add_f32 v[4:5], v[4:5], v[20:21]\n\tv_pk_add_f32 v[6:7], v[6:7], v[22:23]\n\tv_pk_add_f32 v[8:9], v[8:9], v[24:25]\n\t"
        "v_pk_add_f32 v[10:11], v[10:11], v[26:27]\n\tv_pk_add_f32 v[12:13], v[12:13], v[28:29]\n\tv_pk_add_f32 v[14:15], v[14:15], v[30:31]\n\t"
        "v_pk_add_f32 v[0:1], v[0:1], v[32:33]\n\tv_pk_add_f32 v[2:3], v[2:3], v[34:35]\n\tv_pk_add_f32 v[4:5], v[4:5], v[36:37]\n\t"
        "v_pk_add_f32 v[6:7], v[6:7], v[38:39]\n\tv_pk_add_f32 v[8:9], v[8:9], v[40:41]\n\tv_pk_add_f32 v[10:11], v[10:11], v[42:43]\n\t"
        "v_pk_add_f32 v[12:13], v[12:13], v[44:45]\n\tv_pk_add_f32 v[14:15], v[14:15], v[46:47]\n\tv_pk_add_f32 v[0:1], v[0:1], v[48:49]\n\t"
        "v_pk_add_f32 v[2:3], v[2:3], v[50:51]\n\tv_pk_add_f32 v[4:5], v[4:5], v[52:53]\n\tv_pk_add_f32 v[6:7], v[6:7], v[54:55]\n\t"
        "v_pk_add_f32 v[8:9], v[8:9], v[56:57]\n\tv_pk_add_f32 v[10:11], v[10:11], v[58:59]\n\tv_pk_add_f32 v[12:13], v[12:13], v[60:61]\n\t"
        "v_pk_add_f32 v[14:15], v[14:15], v[62:63]\n\ts_branch 99f\n\t33:\n\ts_sub_u32 %[cap], %[cap], 1\n\ts_cbranch_scc0 73f\n\ts_mov_b32 %[cap], 0\n\t"
        "s_branch 53b\n\t73:\n\ts_sleep 1\n\ts_add_u32 %[so], %[kvb], 12288\n\tbuffer_load_dwordx4 v[192:195], %[vo], %[rs], %[so] offen offset:0 sc1\n\t"
        "buffer_load_dwordx4 v[196:199], %[vo], %[rs], %[so] offen offset:1024 sc1\n\t"
        "buffer_load_dwordx4 v[200:203], %[vo], %[rs], %[so] offen offset:2048 sc1\n\t"
        "buffer_load_dwordx4 v[204:207], %[vo], %[rs], %[so] offen offset:3072 sc1\n\ts_waitcnt vmcnt(0)\n\ts_branch 13b\n\t34:\n\t"
        "s_sub_u32 %[cap], %[cap], 1\n\ts_cbranch_scc0 74f\n\ts_mov_b32 %[cap], 0\n\ts_branch 54b\n\t74:\n\ts_sleep 1\n\ts_add_u32 %[so], %[kvb], 16384\n\t"
        "buffer_load_dwordx4 v[208:211], %[vo], %[rs], %[so] offen offset:0 sc1\n\t"
        "buffer_load_dwordx4 v[212:215], %[vo], %[rs], %[so] offen offset:1024 sc1\n\t"
        "buffer_load_dwordx4 v[216:219], %[vo], %[rs], %[so] offen offset:2048 sc1\n\t"
        "buffer_load_dwordx4 v[220:223], %[vo], %[rs], %[so] offen offset:3072 sc1\n\ts_waitcnt vmcnt(0)\n\ts_branch 14b\n\t35:\n\t"
        "s_sub_u32 %[cap], %[cap], 1\n\ts_cbranch_scc0 75f\n\ts_mov_b32 %[cap], 0\n\ts_branch 55b\n\t75:\n\ts_sleep 1\n\ts_add_u32 %[so], %[kvb], 20480\n\t"
        "buffer_load_dwordx4 v[144:147], %[vo], %[rs], %[so] offen offset:0 sc1\n\t"
        "buffer_load_dwordx4 v[148:151], %[vo], %[rs], %[so] offen offset:1024 sc1\n\t"
        "buffer_load_dwordx4 v[152:155], %[vo], %[rs], %[so] offen offset:2048 sc1\n\t"
        "buffer_load_dwordx4 v[156:159], %[vo], %[rs], %[so] offen offset:3072 sc1\n\ts_waitcnt vmcnt(0)\n\ts_branch 15b\n\t36:\n\t"
        "s_sub_u32 %[cap], %[cap], 1\n\ts_cbranch_scc0 76f\n\ts_mov_b32 %[cap], 0\n\ts_branch 56b\n\t76:\n\ts_sleep 1\n\ts_add_u32 %[so], %[kvb], 24576\n\t"
        "buffer_load_dwordx4 v[160:163], %[vo], %[rs], %[so] offen offset:0 sc1\n\t"
        "buffer_load_dwordx4 v[164:167], %[vo], %[rs], %[so] offen offset:1024 sc1\n\t"
        "buffer_load_dwordx4 v[168:171], %[vo], %[rs], %[so] offen offset:2048 sc1\n\t"
        "buffer_load_dwordx4 v[172:175], %[vo], %[rs], %[so] offen offset:3072 sc1\n\ts_waitcnt vmcnt(0)\n\ts_branch 16b\n\t37:\n\t"
        "s_sub_u32 %[cap], %[cap], 1\n\ts_cbranch_scc0 77f\n\ts_mov_b32 %[cap], 0\n\ts_branch 57b\n\t77:\n\ts_sleep 1\n\ts_add_u32 %[so], %[kvb], 28672\n\t"
        "buffer_load_dwordx4 v[176:179], %[vo], %[rs], %[so] offen offset:0 sc1\n\t"
        "buffer_load_dwordx4 v[180:183], %[vo], %[rs], %[so] offen offset:1024 sc1\n\t"
        "buffer_load_dwordx4 v[184:187], %[vo], %[rs], %[so] offen offset:2048 sc1\n\t"
        "buffer_load_dwordx4 v[188:191], %[vo], %[rs], %[so] offen offset:3072 sc1\n\ts_waitcnt vmcnt(0)\n\ts_branch 17b\n\t38:\n\t"
        "s_sub_u32 %[cap], %[cap], 1\n\ts_cbranch_scc0 78f\n\ts_mov_b32 %[cap], 0\n\ts_branch 58b\n\t78:\n\ts_sleep 1\n\ts_add_u32 %[so], %[kvb], 32768\n\t"
        "buffer_load_dwordx4 v[192:195], %[vo], %[rs], %[so] offen offset:0 sc1\n\t"
        "buffer_load_dwordx4 v[196:199], %[vo], %[rs], %[so] offen offset:1024 sc1\n\t"
        "buffer_load_dwordx4 v[200:203], %[vo], %[rs], %[so] offen offset:2048 sc1\n\t"
        "buffer_load_dwordx4 v[204:207], %[vo], %[rs], %[so] offen offset:3072 sc1\n\ts_waitcnt vmcnt(0)\n\ts_branch 18b\n\t39:\n\t"
        "s_sub_u32 %[cap], %[cap], 1\n\ts_cbranch_scc0 79f\n\ts_mov_b32 %[cap], 0\n\ts_branch 59b\n\t79:\n\ts_sleep 1\n\ts_add_u32 %[so], %[kvb], 36864\n\t"
        "buffer_load_dwordx4 v[208:211], %[vo], %[rs], %[so] offen offset:0 sc1\n\t"
        "buffer_load_dwordx4 v[212:215], %[vo], %[rs], %[so] offen offset:1024 sc1\n\t"
        "buffer_load_dwordx4 v[216:219], %[vo], %[rs], %[so] offen offset:2048 sc1\n\t"
        "buffer_load_dwordx4 v[220:223], %[vo], %[rs], %[so] offen offset:3072 sc1\n\ts_waitcnt vmcnt(0)\n\ts_branch 19b\n\t99:\n\ts_setprio 0\n\t"
        : "=&{v[0:15]}"(o), [so] "=&s"(so), [cap] "=&s"(cap)
        : [rs] "s"(rws), [kvb] "s"(WS_KV + (head * 64 + w * 16) * 4096), [vo] "v"(lane * 16),
          [q0] "v"(q + ((w + 1) & 3) * 2048), [q1] "v"(q + ((w + 2) & 3) * 2048), [q2] "v"(q + ((w + 3) & 3) * 2048), [q3] "v"(q + w * 2048),
          [p0] "v"(pp + ((w + 1) & 3) * 12288), [p1] "v"(pp + ((w + 2) & 3) * 12288 + 4096), [p2] "v"(pp + ((w + 3) & 3) * 12288 + 8192), [pr] "v"(pp + w * 12288)
        : "memory", "vcc", "scc",
          "v16", "v17", "v18", "v19", "v20", "v21", "v22", "v23", "v24", "v25", "v26", "v27", "v28", "v29", "v30", "v31", "v32", "v33", "v34", "v35",
          "v36", "v37", "v38", "v39", "v40", "v41", "v42", "v43", "v44", "v45", "v46", "v47", "v48", "v49", "v50", "v51", "v52", "v53", "v54", "v55",
          "v56", "v57", "v58", "v59", "v60", "v61", "v62", "v63", "v64", "v65", "v66", "v67", "v68", "v69", "v70", "v71", "v72", "v73", "v74", "v75",
          "v76", "v77", "v78", "v79", "v80", "v81", "v82", "v83", "v84", "v85", "v86", "v87", "v88", "v89", "v90", "v91", "v92", "v93", "v94", "v95",
          "v96", "v97", "v98", "v99", "v100", "v101", "v102", "v103", "v104", "v105", "v106", "v107", "v108", "v109", "v110", "v111", "v112", "v113",
          "v114", "v115", "v116", "v117", "v118", "v119", "v120", "v121", "v122", "v123", "v124", "v125", "v126", "v127", "v128", "v129", "v130",
          "v131", "v132", "v133", "v134", "v135", "v136", "v137", "v138", "v139", "v140", "v141", "v142", "v143", "v144", "v145", "v146", "v147",
          "v148", "v149", "v150", "v151", "v152", "v153", "v154", "v155", "v156", "v157", "v158", "v159", "v160", "v161", "v162", "v163", "v164",
          "v165", "v166", "v167", "v168", "v169", "v170", "v171", "v172", "v173", "v174", "v175", "v176", "v177", "v178", "v179", "v180", "v181",
          "v182", "v183", "v184", "v185", "v186", "v187", "v188", "v189", "v190", "v191", "v192", "v193", "v194", "v195", "v196", "v197", "v198",
          "v199", "v200", "v201", "v202", "v203", "v204", "v205", "v206", "v207", "v208", "v209", "v210", "v211", "v212", "v213", "v214", "v215",
          "v216", "v217", "v218", "v219", "v220", "v221", "v222", "v223", "v224", "v225");
    ST(8);
    cwait<0, 0>();
    float l = __uint_as_float(__builtin_amdgcn_permlane32_swap(__float_as_uint(o[12]), __float_as_uint(o[12]), false, false)[1]);
    float as[4];
#pragma unroll
    for (int k = 0; k < 4; ++k) as[k] = (fabsf(o[4 * k]) + fabsf(o[4 * k + 1])) + (fabsf(o[4 * k + 2]) + fabsf(o[4 * k + 3]));
    if (__builtin_expect(!__all(l >= 0x1p-80f && l <= 0x1p80f && (as[0] + as[1]) + (as[2] + as[3]) <= 0x1p120f), 0)) {
        const u32x4 qf[2] = {ldsr128(q + w * 2048), ldsr128(q + w * 2048 + 1024)};
        o = redo_o(rws, lane, head, qf);
        l = __uint_as_float(__builtin_amdgcn_permlane32_swap(__float_as_uint(o[12]), __float_as_uint(o[12]), false, false)[1]);
        cwait<0, 0>();
    }
    if (lane >= 32) o[12] = 0.f;
    o *= __builtin_amdgcn_rcpf(l);
#pragma unroll
    for (int s = 0; s < 2; ++s) st128<SC1>(pack8(o, s), rws, lane * 16, WS_O + (((rb * 4 + w) * 16 + head) * 2 + s) * 1024);
    ST(24);
}

__device__ __forceinline__ int fc2_kstep(int p, int rot) { return mod7((p % 14) / 2 + rot) * 8 + (p / 14) * 2 + (p & 1); }
__device__ __forceinline__ int fc2_rot(int j) { return (j % 7 + 2 * (j / 7)) % 7; }
__device__ __forceinline__ unsigned w2src(int c, int hw, int i, int j) {
    const int ks = (hw & 1) ? fc2_kstep(4 * c + 2 + (i >> 1), fc2_rot(j)) : fc2_kstep(4 * c + (i >> 1), fc2_rot(j));
    return (((hw >> 1) * 56 + ks) * 16 + (i & 1) * 8) * 896 + (j % 7) * 128;
}
__device__ __forceinline__ void attn_helper(const Params& p, LAS char* L, rsrc_t rws, int hw, int lane, int rb, int head, int xcd, int j, unsigned tag, u32x4 (&sg)[9][4], u32x4 (&xr)[2], u32x4& ext ST_ARG) {
    const rsrc_t rw = mkrs(p.wo, D * D * 2), r1 = mkrs(p.w1, D * F * 2), r2 = mkrs(p.w2, F * D * 2);
    const unsigned v2 = slab_voff(lane, 896), v1 = slab_voff(lane, 3584);
    const int ct = j % 7, ct1 = j % 14, col = ct1 * 128 + hw * 32 + (lane & 31);
    auto fill = [&](int q) INL {
        const int rt = xcd * 8 + (j / 7) * 2 + (hw & 1), ks = (j % 7) * 2 + (hw >> 1);
        const unsigned v = j < 28 ? lane * 16 : 0x7F000000u;
        if (q < 2) fill16(rws, lane * 16, WS_O + (((rb * 4 + hw) * 16 + head) * 2 + q) * 1024);
        else if (q < 4) fill16(rws, v, WS_H + (rt * 28 + ks * 2 + q - 2) * 1024);
        else if (q == 4) fill16(rws, lane < 16 ? v : 0x7F000000u, WS_HS + (rt * 14 + ks) * 256);
        else if (q < NFILL) fill16(rws, v, WS_G + ((xcd * 8 + (j / 14) * 4 + hw) * 112 + ct1 * 8 + q - 5) * 1024);
    };
#pragma unroll
    for (int i = 0; i < 2; ++i) xr[i] = __builtin_amdgcn_raw_buffer_load_b128(mkrs(p.x, S * D * 2), (lane >> 3) * 896 + (lane & 7) * 16, ((xcd * 4 + j / 7) * 64 + hw * 16 + i * 8) * 896 + ct * 128, 0);
    const unsigned b = __builtin_amdgcn_raw_buffer_load_b16(mkrs(p.b1, F * 2), lane < 32 ? col * 2 : 0x7F000000u, 0, 0);
    SBAR();
    if (hw == 0 && blockIdx.x < 196) {
        vm_wait<0>();
        f32x16 c[2] = {};
#pragma unroll
        for (int k = 0; k < 8; ++k) c[k & 1] = mfma((u32x4){ONES, ONES, ONES, ONES}, wfrag<1024>(L + P1_TMP + slab_rd(lane, k & 1) + (k >> 1) * 2048), c[k & 1]);
        __builtin_amdgcn_raw_buffer_store_b64((u32x2){__float_as_uint(lane < 32 ? c[0][0] : c[1][0]), tag}, rws, WS_CS + ((blockIdx.x % 7) * F + (blockIdx.x / 7) * 64 + lane) * 8, 0, SC1);
    }
#pragma unroll
    for (int q = 0; q < QFILL; ++q) fill(q);
    if (hw == 1) wait_word(rws, WS_F1 + ((head >> 1) * 32 + (lane & 31)) * 128, tag);
    ST(11);
    bar();
    ST(5);
    __builtin_amdgcn_s_setprio(3);
    __builtin_amdgcn_s_sleep(HSL);
    unsigned wsrc = (hw + 7 * (ct & 3)) * 8 * 896 + ct * 128;
#pragma unroll
    for (int i = 0; i < 7; ++i) {
        dma16(rw, v2, wsrc, L_WO + (i * 4 + hw) * 1024);
        dma16(rw, v2, wsrc + 224 * 896, L_WO + (28 + i * 4 + hw) * 1024);
        wsrc += 32 * 896; if (wsrc >= 224 * 896) wsrc -= 224 * 896;
        vm_wait<HD>();
    }
    ST(12);
#pragma unroll
    for (int k = 0; k < 36; ++k) {
        sg[k >> 2][k & 3] = __builtin_amdgcn_raw_buffer_load_b128(r2, v2, w2src(5 + (k >> 2), hw, k & 3, j), 0);
        if (k % 2 == 1) fill(QFILL + k / 2);
        vm_wait<HD>();
    }
    ST(14);
    u32x2 cv[4];
#pragma unroll
    for (int k = 0; k < 4; ++k) cv[k] = __builtin_amdgcn_raw_buffer_load_b64(rws, (((lane >> 5) * 3 + k) * F + col) * 8, WS_CS, SC1);
    SBAR();
#pragma unroll
    for (int i = 0; i < NE; ++i) { W1DMA(0, i * 2 + (hw >> 1), hw & 1); vm_wait<HD>(); }
#pragma unroll 1
    for (unsigned spins = 0; spins < (1u << 20) && !__all(cv[0][1] == tag && cv[1][1] == tag && cv[2][1] == tag && cv[3][1] == tag); ++spins) {
        asm volatile("" ::: "memory");
#pragma unroll
        for (int k = 0; k < 4; ++k) cv[k] = __builtin_amdgcn_raw_buffer_load_b64(rws, (((lane >> 5) * 3 + k) * F + col) * 8, WS_CS, SC1);
    }
    {
        float lo, lo2;
        const unsigned ch = bfsplit(sadd(sadd(lane < 32 ? __uint_as_float(cv[0][0]) : 0.f, __uint_as_float(cv[1][0])), sadd(__uint_as_float(cv[2][0]), __uint_as_float(cv[3][0]))), lo), cl = bfsplit(lo, lo2);
        ext = (u32x4){ch | (ch << 16), cl | (b << 16), 0u, b << 16};
    }
    ST(7);
    cwait<0, 0>();
    bar();
    if (hw == 3) set_flag(rws, WS_F2 + (rb * 16 + head) * 128, tag, lane);
    __builtin_amdgcn_s_setprio(0);
    bar();
    ST(8);
}

__device__ __forceinline__ void gelu4(int stage, float a0, float a1, float a2, float a3, float (&g)[4], float k0, float k1, float irs, unsigned& p0, unsigned& p1) {
    if (stage == 0)
        asm volatile("v_mul_f32 %0, %4, %4\n\tv_mul_f32 %1, %5, %5\n\tv_mul_f32 %2, %6, %6\n\tv_mul_f32 %3, %7, %7\n\t"
                     "v_fma_f32 %0, %0, %9, %8\n\tv_fma_f32 %1, %1, %9, %8\n\tv_fma_f32 %2, %2, %9, %8\n\tv_fma_f32 %3, %3, %9, %8\n\t"
                     "v_mul_f32 %0, %4, %0\n\tv_mul_f32 %1, %5, %1"
                     : "=&v"(g[0]), "=&v"(g[1]), "=&v"(g[2]), "=&v"(g[3]) : "v"(a0), "v"(a1), "v"(a2), "v"(a3), "v"(k0), "v"(k1));
    if (stage == 1)
        asm volatile("v_mul_f32 %2, %4, %2\n\tv_mul_f32 %3, %5, %3\n\tv_exp_f32 %0, %0\n\tv_exp_f32 %1, %1\n\tv_exp_f32 %2, %2\n\tv_exp_f32 %3, %3"
                     : "+v"(g[0]), "+v"(g[1]), "+v"(g[2]), "+v"(g[3]) : "v"(a2), "v"(a3));
    if (stage == 2)
        asm volatile("v_fma_f32 %0, %0, %4, %4\n\tv_fma_f32 %1, %1, %4, %4\n\tv_fma_f32 %2, %2, %4, %4\n\tv_fma_f32 %3, %3, %4, %4\n\t"
                     "v_rcp_f32 %0, %0\n\tv_rcp_f32 %1, %1\n\tv_rcp_f32 %2, %2"
                     : "+v"(g[0]), "+v"(g[1]), "+v"(g[2]), "+v"(g[3]) : "v"(irs));
    if (stage == 3)
        asm volatile("v_rcp_f32 %5, %5\n\tv_mul_f32 %2, %6, %2\n\tv_mul_f32 %3, %7, %3\n\tv_mul_f32 %4, %8, %4\n\tv_mul_f32 %5, %9, %5\n\t"
                     "v_cvt_pk_bf16_f32 %0, %2, %3\n\tv_cvt_pk_bf16_f32 %1, %4, %5"
                     : "=&v"(p0), "=&v"(p1), "+v"(g[0]), "+v"(g[1]), "+v"(g[2]), "+v"(g[3]) : "v"(a0), "v"(a1), "v"(a2), "v"(a3));
    SBAR();
}

__device__ __forceinline__ void mlp_helper(const Params& p, LAS char* L, rsrc_t rws, int hw, int lane, int xcd, int j, unsigned tag, const u32x4 (&sg)[9][4], const u32x4 (&xr)[2], const u32x4& ext ST_ARG) {
    const int r = lane & 31, hh = lane >> 5, ct1 = j % 14, ct = j % 7;
    const int b4 = xcd * 4 + j / 7;
    const rsrc_t r1 = mkrs(p.w1, D * F * 2), r2 = mkrs(p.w2, F * D * 2);
    const unsigned v1 = slab_voff(lane, 3584), v2 = slab_voff(lane, 896);
    auto w2dma = [&](int c) INL {
#pragma unroll
        for (int i = 0; i < 4; ++i) dma16(r2, v2, w2src(c, hw, i, j), w2slot(c) + hw * 4096 + i * 1024);
    };
    auto w2st = [&](int c) INL {
#pragma unroll
        for (int i = 0; i < 4; ++i) *(LAS u32x4*)(L + w2slot(c) + hw * 4096 + i * 1024 + lane * 16) = sg[c - 5][i];
        SBAR();
    };
    auto w1rest = [&](int i) INL { if (i < 14) W1DMA(0, i * 2 + (hw >> 1), hw & 1); else W1DMA(1, (i - 14) * 2 + (hw >> 1), hw & 1); };
#pragma unroll
    for (int i = NE; i < T1; ++i) { w1rest(i); vm_wait<TD0>(); }
    bar();
#pragma unroll
    for (int i = 0; i < 2; ++i) *(LAS u32x4*)(L + L_XR + (hw * 16 + i * 8 + (lane >> 3)) * 144 + (lane & 7) * 16) = xr[i];
    *(LAS u32x4*)(L + L_EXT + hw * 1024 + lane * 16) = ext;
#pragma unroll
    for (int i = T1; i < T2; ++i) { w1rest(i); vm_wait<TD>(); }
    lds_bar();
    if (hw == 3) set_flag(rws, WS_F3 + (b4 * 8 + ct) * 128, tag, lane);
#pragma unroll
    for (int i = T2; i < 28; ++i) w1rest(i);
    wait_bar<0>();
    const int rt1 = xcd * 8 + (j / 14) * 4 + hw;
    float s1, s2;
#pragma unroll 1
    for (unsigned spins = 0; spins < SPIN_CAP; ++spins) {
        asm volatile("" ::: "memory");
        unsigned mx = 0; s1 = 0.f; s2 = 0.f;
#pragma unroll
        for (int k = 0; k < 7; ++k) {
            const u32x2 v = __builtin_amdgcn_raw_buffer_load_b64(rws, (hh * 7 * 32 + r) * 8, WS_HS + (rt1 * 14 + k) * 256, SC1);
            mx = umax(mx, umax(v[0], v[1])); s1 = sadd(s1, __uint_as_float(v[0])); s2 = sadd(s2, __uint_as_float(v[1]));
        }
        if (!__any(mx == FILL)) break;
    }
    PIN(s1); PIN(s2);
    float mu = halfsum(s1) * (1.0f / D); PIN(mu);
    const float var = fmaxf(halfsum(s2) * (1.0f / D) - mu * mu, 0.f), r1s = __builtin_amdgcn_rsqf(var + EPS);
    const float rs = r1s * __builtin_amdgcn_rsqf(var * r1s * r1s + EPS), irs = __builtin_amdgcn_rcpf(rs);
    float t0, t1;
    const unsigned mh = bfsplit(-mu, t0); PIN(t0);
    const unsigned ml = bfsplit(t0, t1), ih = bfsplit(irs, t0), il = bfsplit(t0, t1);
    const u32x4 xe = {mh | (ml << 16), mh | (ih << 16), mh | (ml << 16), mh | (il << 16)};
    constexpr float C0 = -2.0f * 0.7978845608028654f * 1.4426950408889634f, C1 = C0 * 0.044715f;
    float k0 = C0 * rs; PIN(k0);
    const f32x4 kk = {k0, C1 * rs * rs * rs, irs, 0.f};
    *(LAS f32x4*)(L + L_GK + hw * 2048 + lane * 16) = kk;
    *(LAS u32x4*)(L + L_GK + hw * 2048 + 1024 + lane * 16) = xe;
    const u32x4 e0 = ldsr128(L + L_EXT + lane * 16), e1 = ldsr128(L + L_EXT + 1024 + lane * 16);
    ST(10);
    lds_bar();
    {
        __builtin_amdgcn_s_setprio(3);
        f32x16 a[2];
#pragma unroll
        for (int k = 0; k < 8; ++k) {
            const f32x4 v = *(LAS f32x4*)(L + w2slot(k >> 2) + hw * 4096 + (k & 3) * 1024 + lane * 16);
#pragma unroll
            for (int i = 0; i < 4; ++i) a[k >> 2][4 * (k & 3) + i] = v[i];
        }
        const unsigned gb = WS_G + (rt1 * 112 + ct1 * 8) * 1024;
        a[0] = mfma(e0, xe, a[0]); a[1] = mfma(e1, xe, a[1]);
        PIN(a[0]); PIN(a[1]); SBAR();
        w2dma(0);
        asm volatile("s_nop 7" ::: "memory");
        float g[2][4];
        sfor<4>([&](auto ic) INL {
            constexpr int f = ic.value;
            unsigned o[4];
#pragma unroll
            for (int st = 0; st < 4; ++st) {
#pragma unroll
                for (int q = 0; q < 2; ++q) { const f32x16& v = a[f >> 1]; const int i = 8 * (f & 1) + 4 * q; gelu4(st, v[i], v[i + 1], v[i + 2], v[i + 3], g[q], kk[0], kk[1], kk[2], o[2 * q], o[2 * q + 1]); }
                if (st < 3 && f < 3) { const int n = 4 + 3 * f + st; dma16(r2, v2, w2src(n >> 2, hw, n & 3, j), w2slot(n >> 2) + hw * 4096 + (n & 3) * 1024); }
            }
            st128<SC1>((u32x4){o[0], o[1], o[2], o[3]}, rws, gb + lane * 16, f * 1024);
            asm volatile("s_nop 1" ::: "memory");
        });
        __builtin_amdgcn_s_setprio(0);
        ST(15);
#pragma unroll
        for (int n = 13; n < 20; ++n) dma16(r2, v2, w2src(n >> 2, hw, n & 3, j), w2slot(n >> 2) + hw * 4096 + (n & 3) * 1024);
        vm_wait<0>();
    }
    bar();
    w2st(5); w2st(6); w2st(7); w2st(8);
    lds_bar();
    w2st(9); w2st(10);
    lds_bar();
    w2st(11); w2st(12); w2st(13);
    lds_bar();
    bar();
    bar();
}

__device__ __forceinline__ void mlp_compute(const Params& p, LAS char* L, rsrc_t rws, int w, int lane, int xcd, int j, unsigned tag ST_ARG) {
    const int r = lane & 31, hh = lane >> 5;
    const int r4 = j / 7, ct = j % 7, m = w & 1, kh = w >> 1;
    const int rt = xcd * 8 + r4 * 2 + m;
    const int sr[2] = {slab_rd(lane, 0), slab_rd(lane, 1)}, sk[2] = {slab_rd(lane, kh), slab_rd(lane, 1 - kh)};
    const int ct1 = j % 14, rt1 = xcd * 8 + (j / 14) * 4 + w;
    const unsigned f3 = WS_F3 + (((rt1 >> 2) * 2 + (lane >= 7 && lane < 14)) * 8 + (lane < 14 ? mod7(lane) : 0)) * 128;
    unsigned fl3;
    u32x4 hpk[2];
    {
        f32x16 acc[2];
        LAS char *w0 = L + L_WO + sk[0] + kh * 224 * 128, *w1 = L + L_WO + sk[1] + kh * 224 * 128;
        unsigned so, cap, pend;
        asm volatile(
            "buffer_load_dword v226, %[fo], %[rs], 0 offen sc1\n\tds_read_b64_tr_b16 v[0:1], %[w00] offset:0\n\t"
            "ds_read_b64_tr_b16 v[2:3], %[w00] offset:1024\n\tds_read_b64_tr_b16 v[4:5], %[w10] offset:0\n\t"
            "ds_read_b64_tr_b16 v[6:7], %[w10] offset:1024\n\tv_mov_b32 v192, 0\n\tv_mov_b32 v193, 0\n\tds_read_b64_tr_b16 v[8:9], %[w00] offset:2048\n\t"
            "ds_read_b64_tr_b16 v[10:11], %[w01] offset:3072\n\tds_read_b64_tr_b16 v[12:13], %[w10] offset:2048\n\t"
            "ds_read_b64_tr_b16 v[14:15], %[w11] offset:3072\n\tv_mov_b32 v194, 0\n\tv_mov_b32 v195, 0\n\t"
            "ds_read_b64_tr_b16 v[16:17], %[w00] offset:3584\n\tds_read_b64_tr_b16 v[18:19], %[w00] offset:4608\n\t"
            "ds_read_b64_tr_b16 v[20:21], %[w10] offset:3584\n\tds_read_b64_tr_b16 v[22:23], %[w10] offset:4608\n\tv_mov_b32 v196, 0\n\t"
            "v_mov_b32 v197, 0\n\tds_read_b64_tr_b16 v[24:25], %[w00] offset:5632\n\tds_read_b64_tr_b16 v[26:27], %[w01] offset:6656\n\t"
            "ds_read_b64_tr_b16 v[28:29], %[w10] offset:5632\n\tds_read_b64_tr_b16 v[30:31], %[w11] offset:6656\n\tv_mov_b32 v198, 0\n\t"
            "v_mov_b32 v199, 0\n\tds_read_b64_tr_b16 v[32:33], %[w00] offset:7168\n\tds_read_b64_tr_b16 v[34:35], %[w00] offset:8192\n\t"
            "ds_read_b64_tr_b16 v[36:37], %[w10] offset:7168\n\tds_read_b64_tr_b16 v[38:39], %[w10] offset:8192\n\tv_mov_b32 v200, 0\n\t"
            "v_mov_b32 v201, 0\n\tds_read_b64_tr_b16 v[40:41], %[w00] offset:9216\n\tds_read_b64_tr_b16 v[42:43], %[w01] offset:10240\n\t"
            "ds_read_b64_tr_b16 v[44:45], %[w10] offset:9216\n\tds_read_b64_tr_b16 v[46:47], %[w11] offset:10240\n\tv_mov_b32 v202, 0\n\t"
            "v_mov_b32 v203, 0\n\tds_read_b64_tr_b16 v[48:49], %[w00] offset:10752\n\tds_read_b64_tr_b16 v[50:51], %[w00] offset:11776\n\t"
            "ds_read_b64_tr_b16 v[52:53], %[w10] offset:10752\n\tds_read_b64_tr_b16 v[54:55], %[w10] offset:11776\n\tv_mov_b32 v204, 0\n\t"
            "v_mov_b32 v205, 0\n\tds_read_b64_tr_b16 v[56:57], %[w00] offset:12800\n\tds_read_b64_tr_b16 v[58:59], %[w01] offset:13824\n\t"
            "ds_read_b64_tr_b16 v[60:61], %[w10] offset:12800\n\tds_read_b64_tr_b16 v[62:63], %[w11] offset:13824\n\tv_mov_b32 v206, 0\n\t"
            "v_mov_b32 v207, 0\n\tds_read_b64_tr_b16 v[64:65], %[w00] offset:14336\n\tds_read_b64_tr_b16 v[66:67], %[w00] offset:15360\n\t"
            "ds_read_b64_tr_b16 v[68:69], %[w10] offset:14336\n\tds_read_b64_tr_b16 v[70:71], %[w10] offset:15360\n\tv_mov_b32 v208, 0\n\t"
            "v_mov_b32 v209, 0\n\tds_read_b64_tr_b16 v[72:73], %[w00] offset:16384\n\tds_read_b64_tr_b16 v[74:75], %[w01] offset:17408\n\t"
            "ds_read_b64_tr_b16 v[76:77], %[w10] offset:16384\n\tds_read_b64_tr_b16 v[78:79], %[w11] offset:17408\n\tv_mov_b32 v210, 0\n\t"
            "v_mov_b32 v211, 0\n\tds_read_b64_tr_b16 v[80:81], %[w00] offset:17920\n\tds_read_b64_tr_b16 v[82:83], %[w00] offset:18944\n\t"
            "ds_read_b64_tr_b16 v[84:85], %[w10] offset:17920\n\tds_read_b64_tr_b16 v[86:87], %[w10] offset:18944\n\tv_mov_b32 v212, 0\n\t"
            "v_mov_b32 v213, 0\n\tds_read_b64_tr_b16 v[88:89], %[w00] offset:19968\n\tds_read_b64_tr_b16 v[90:91], %[w01] offset:20992\n\t"
            "ds_read_b64_tr_b16 v[92:93], %[w10] offset:19968\n\tds_read_b64_tr_b16 v[94:95], %[w11] offset:20992\n\tv_mov_b32 v214, 0\n\t"
            "v_mov_b32 v215, 0\n\tds_read_b64_tr_b16 v[96:97], %[w00] offset:21504\n\tds_read_b64_tr_b16 v[98:99], %[w00] offset:22528\n\t"
            "ds_read_b64_tr_b16 v[100:101], %[w10] offset:21504\n\tds_read_b64_tr_b16 v[102:103], %[w10] offset:22528\n\tv_mov_b32 v216, 0\n\t"
            "v_mov_b32 v217, 0\n\tds_read_b64_tr_b16 v[104:105], %[w00] offset:23552\n\tds_read_b64_tr_b16 v[106:107], %[w01] offset:24576\n\t"
            "ds_read_b64_tr_b16 v[108:109], %[w10] offset:23552\n\tds_read_b64_tr_b16 v[110:111], %[w11] offset:24576\n\tv_mov_b32 v218, 0\n\t"
            "v_mov_b32 v219, 0\n\tds_read_b64_tr_b16 v[112:113], %[w00] offset:25088\n\tds_read_b64_tr_b16 v[114:115], %[w00] offset:26112\n\t"
            "ds_read_b64_tr_b16 v[116:117], %[w10] offset:25088\n\tds_read_b64_tr_b16 v[118:119], %[w10] offset:26112\n\tv_mov_b32 v220, 0\n\t"
            "v_mov_b32 v221, 0\n\tds_read_b64_tr_b16 v[120:121], %[w00] offset:27136\n\tds_read_b64_tr_b16 v[122:123], %[w01] offset:28160\n\t"
            "ds_read_b64_tr_b16 v[124:125], %[w10] offset:27136\n\tds_read_b64_tr_b16 v[126:127], %[w11] offset:28160\n\tv_mov_b32 v222, 0\n\t"
            "v_mov_b32 v223, 0\n\ts_mov_b32 %[cap], 0x100000\n\ts_waitcnt lgkmcnt(0)\n\ts_barrier\n\t1:\n\ts_waitcnt vmcnt(0)\n\t"
            "v_cmp_ne_u32 vcc, %[tag], v226\n\ts_cbranch_vccz 2f\n\ts_sub_u32 %[cap], %[cap], 1\n\ts_cbranch_scc1 2f\n\t"
            "buffer_load_dword v226, %[fo], %[rs], 0 offen sc1\n\ts_branch 1b\n\t2:\n\ts_mov_b32 %[pend], 0x10000\n\ts_add_u32 %[so], %[rot], 0\n\t"
            "s_and_b32 %[so], %[so], 0x3fff\n\ts_add_u32 %[so], %[so], %[ob]\n\tbuffer_load_dwordx4 v[128:131], %[vo], %[rs], %[so] offen sc1\n\t"
            "buffer_load_dwordx4 v[132:135], %[vo], %[rs], %[so] offen offset:1024 sc1\n\ts_add_u32 %[so], %[rot], 2048\n\t"
            "s_and_b32 %[so], %[so], 0x3fff\n\ts_add_u32 %[so], %[so], %[ob]\n\tbuffer_load_dwordx4 v[136:139], %[vo], %[rs], %[so] offen sc1\n\t"
            "buffer_load_dwordx4 v[140:143], %[vo], %[rs], %[so] offen offset:1024 sc1\n\ts_add_u32 %[so], %[rot], 4096\n\t"
            "s_and_b32 %[so], %[so], 0x3fff\n\ts_add_u32 %[so], %[so], %[ob]\n\tbuffer_load_dwordx4 v[144:147], %[vo], %[rs], %[so] offen sc1\n\t"
            "buffer_load_dwordx4 v[148:151], %[vo], %[rs], %[so] offen offset:1024 sc1\n\ts_add_u32 %[so], %[rot], 6144\n\t"
            "s_and_b32 %[so], %[so], 0x3fff\n\ts_add_u32 %[so], %[so], %[ob]\n\tbuffer_load_dwordx4 v[152:155], %[vo], %[rs], %[so] offen sc1\n\t"
            "buffer_load_dwordx4 v[156:159], %[vo], %[rs], %[so] offen offset:1024 sc1\n\ts_add_u32 %[so], %[rot], 8192\n\t"
            "s_and_b32 %[so], %[so], 0x3fff\n\ts_add_u32 %[so], %[so], %[ob]\n\tbuffer_load_dwordx4 v[160:163], %[vo], %[rs], %[so] offen sc1\n\t"
            "buffer_load_dwordx4 v[164:167], %[vo], %[rs], %[so] offen offset:1024 sc1\n\ts_waitcnt vmcnt(8)\n\tv_max3_u32 v224, v128, v129, v130\n\t"
            "v_max3_u32 v225, v131, v132, v133\n\tv_max3_u32 v224, v224, v134, v135\n\tv_max_u32 v224, v224, v225\n\tv_cmp_eq_u32 vcc, -1, v224\n\t"
            "10:\n\ts_cbranch_vccnz 30f\n\t20:\n\tv_mfma_f32_32x32x16_bf16 v[192:207], v[0:3], v[128:131], v[192:207]\n\t"
            "s_add_u32 %[so], %[rot], 10240\n\ts_and_b32 %[so], %[so], 0x3fff\n\ts_add_u32 %[so], %[so], %[ob]\n\t"
            "buffer_load_dwordx4 v[168:171], %[vo], %[rs], %[so] offen sc1\n\t"
            "buffer_load_dwordx4 v[172:175], %[vo], %[rs], %[so] offen offset:1024 sc1\n\t"
            "v_mfma_f32_32x32x16_bf16 v[208:223], v[4:7], v[128:131], v[208:223]\n\t"
            "v_mfma_f32_32x32x16_bf16 v[192:207], v[8:11], v[132:135], v[192:207]\n\ts_waitcnt vmcnt(8)\n\tv_max3_u32 v224, v136, v137, v138\n\t"
            "v_max3_u32 v225, v139, v140, v141\n\tv_max3_u32 v224, v224, v142, v143\n\tv_max_u32 v224, v224, v225\n\tv_cmp_eq_u32 vcc, -1, v224\n\t"
            "v_mfma_f32_32x32x16_bf16 v[208:223], v[12:15], v[132:135], v[208:223]\n\t11:\n\ts_cbranch_vccnz 31f\n\t21:\n\t"
            "v_mfma_f32_32x32x16_bf16 v[192:207], v[16:19], v[136:139], v[192:207]\n\ts_add_u32 %[so], %[rot], 12288\n\t"
            "s_and_b32 %[so], %[so], 0x3fff\n\ts_add_u32 %[so], %[so], %[ob]\n\tbuffer_load_dwordx4 v[176:179], %[vo], %[rs], %[so] offen sc1\n\t"
            "buffer_load_dwordx4 v[180:183], %[vo], %[rs], %[so] offen offset:1024 sc1\n\t"
            "v_mfma_f32_32x32x16_bf16 v[208:223], v[20:23], v[136:139], v[208:223]\n\t"
            "v_mfma_f32_32x32x16_bf16 v[192:207], v[24:27], v[140:143], v[192:207]\n\ts_waitcnt vmcnt(8)\n\tv_max3_u32 v224, v144, v145, v146\n\t"
            "v_max3_u32 v225, v147, v148, v149\n\tv_max3_u32 v224, v224, v150, v151\n\tv_max_u32 v224, v224, v225\n\tv_cmp_eq_u32 vcc, -1, v224\n\t"
            "v_mfma_f32_32x32x16_bf16 v[208:223], v[28:31], v[140:143], v[208:223]\n\t12:\n\ts_cbranch_vccnz 32f\n\t22:\n\t"
            "v_mfma_f32_32x32x16_bf16 v[192:207], v[32:35], v[144:147], v[192:207]\n\ts_add_u32 %[so], %[rot], 14336\n\t"
            "s_and_b32 %[so], %[so], 0x3fff\n\ts_add_u32 %[so], %[so], %[ob]\n\tbuffer_load_dwordx4 v[184:187], %[vo], %[rs], %[so] offen sc1\n\t"
            "buffer_load_dwordx4 v[188:191], %[vo], %[rs], %[so] offen offset:1024 sc1\n\t"
            "v_mfma_f32_32x32x16_bf16 v[208:223], v[36:39], v[144:147], v[208:223]\n\t"
            "v_mfma_f32_32x32x16_bf16 v[192:207], v[40:43], v[148:151], v[192:207]\n\ts_waitcnt vmcnt(8)\n\tv_max3_u32 v224, v152, v153, v154\n\t"
            "v_max3_u32 v225, v155, v156, v157\n\tv_max3_u32 v224, v224, v158, v159\n\tv_max_u32 v224, v224, v225\n\tv_cmp_eq_u32 vcc, -1, v224\n\t"
            "v_mfma_f32_32x32x16_bf16 v[208:223], v[44:47], v[148:151], v[208:223]\n\t13:\n\ts_cbranch_vccnz 33f\n\t23:\n\t"
            "v_mfma_f32_32x32x16_bf16 v[192:207], v[48:51], v[152:155], v[192:207]\n\t"
            "v_mfma_f32_32x32x16_bf16 v[208:223], v[52:55], v[152:155], v[208:223]\n\t"
            "v_mfma_f32_32x32x16_bf16 v[192:207], v[56:59], v[156:159], v[192:207]\n\ts_waitcnt vmcnt(6)\n\tv_max3_u32 v224, v160, v161, v162\n\t"
            "v_max3_u32 v225, v163, v164, v165\n\tv_max3_u32 v224, v224, v166, v167\n\tv_max_u32 v224, v224, v225\n\tv_cmp_eq_u32 vcc, -1, v224\n\t"
            "v_mfma_f32_32x32x16_bf16 v[208:223], v[60:63], v[156:159], v[208:223]\n\t14:\n\ts_cbranch_vccnz 34f\n\t24:\n\t"
            "v_mfma_f32_32x32x16_bf16 v[192:207], v[64:67], v[160:163], v[192:207]\n\t"
            "v_mfma_f32_32x32x16_bf16 v[208:223], v[68:71], v[160:163], v[208:223]\n\t"
            "v_mfma_f32_32x32x16_bf16 v[192:207], v[72:75], v[164:167], v[192:207]\n\ts_waitcnt vmcnt(4)\n\tv_max3_u32 v224, v168, v169, v170\n\t"
            "v_max3_u32 v225, v171, v172, v173\n\tv_max3_u32 v224, v224, v174, v175\n\tv_max_u32 v224, v224, v225\n\tv_cmp_eq_u32 vcc, -1, v224\n\t"
            "v_mfma_f32_32x32x16_bf16 v[208:223], v[76:79], v[164:167], v[208:223]\n\t15:\n\ts_cbranch_vccnz 35f\n\t25:\n\t"
            "v_mfma_f32_32x32x16_bf16 v[192:207], v[80:83], v[168:171], v[192:207]\n\t"
            "v_mfma_f32_32x32x16_bf16 v[208:223], v[84:87], v[168:171], v[208:223]\n\t"
            "v_mfma_f32_32x32x16_bf16 v[192:207], v[88:91], v[172:175], v[192:207]\n\ts_waitcnt vmcnt(2)\n\tv_max3_u32 v224, v176, v177, v178\n\t"
            "v_max3_u32 v225, v179, v180, v181\n\tv_max3_u32 v224, v224, v182, v183\n\tv_max_u32 v224, v224, v225\n\tv_cmp_eq_u32 vcc, -1, v224\n\t"
            "v_mfma_f32_32x32x16_bf16 v[208:223], v[92:95], v[172:175], v[208:223]\n\t16:\n\ts_cbranch_vccnz 36f\n\t26:\n\t"
            "v_mfma_f32_32x32x16_bf16 v[192:207], v[96:99], v[176:179], v[192:207]\n\t"
            "v_mfma_f32_32x32x16_bf16 v[208:223], v[100:103], v[176:179], v[208:223]\n\t"
            "v_mfma_f32_32x32x16_bf16 v[192:207], v[104:107], v[180:183], v[192:207]\n\ts_waitcnt vmcnt(0)\n\tv_max3_u32 v224, v184, v185, v186\n\t"
            "v_max3_u32 v225, v187, v188, v189\n\tv_max3_u32 v224, v224, v190, v191\n\tv_max_u32 v224, v224, v225\n\tv_cmp_eq_u32 vcc, -1, v224\n\t"
            "v_mfma_f32_32x32x16_bf16 v[208:223], v[108:111], v[180:183], v[208:223]\n\t17:\n\ts_cbranch_vccnz 37f\n\t27:\n\t"
            "v_mfma_f32_32x32x16_bf16 v[192:207], v[112:115], v[184:187], v[192:207]\n\t"
            "v_mfma_f32_32x32x16_bf16 v[208:223], v[116:119], v[184:187], v[208:223]\n\t"
            "v_mfma_f32_32x32x16_bf16 v[192:207], v[120:123], v[188:191], v[192:207]\n\t"
            "v_mfma_f32_32x32x16_bf16 v[208:223], v[124:127], v[188:191], v[208:223]\n\ts_branch 99f\n\t30:\n\ts_sub_u32 %[pend], %[pend], 1\n\t"
            "s_cbranch_scc0 40f\n\ts_mov_b32 %[pend], 0\n\ts_branch 20b\n\t40:\n\ts_sleep 1\n\ts_add_u32 %[so], %[rot], 0\n\t"
            "s_and_b32 %[so], %[so], 0x3fff\n\ts_add_u32 %[so], %[so], %[ob]\n\tbuffer_load_dwordx4 v[128:131], %[vo], %[rs], %[so] offen sc1\n\t"
            "buffer_load_dwordx4 v[132:135], %[vo], %[rs], %[so] offen offset:1024 sc1\n\ts_waitcnt vmcnt(0)\n\tv_max3_u32 v224, v128, v129, v130\n\t"
            "v_max3_u32 v225, v131, v132, v133\n\tv_max3_u32 v224, v224, v134, v135\n\tv_max_u32 v224, v224, v225\n\tv_cmp_eq_u32 vcc, -1, v224\n\t"
            "s_branch 10b\n\t31:\n\ts_sub_u32 %[pend], %[pend], 1\n\ts_cbranch_scc0 41f\n\ts_mov_b32 %[pend], 0\n\ts_branch 21b\n\t41:\n\ts_sleep 1\n\t"
            "s_add_u32 %[so], %[rot], 2048\n\ts_and_b32 %[so], %[so], 0x3fff\n\ts_add_u32 %[so], %[so], %[ob]\n\t"
            "buffer_load_dwordx4 v[136:139], %[vo], %[rs], %[so] offen sc1\n\t"
            "buffer_load_dwordx4 v[140:143], %[vo], %[rs], %[so] offen offset:1024 sc1\n\ts_waitcnt vmcnt(0)\n\tv_max3_u32 v224, v136, v137, v138\n\t"
            "v_max3_u32 v225, v139, v140, v141\n\tv_max3_u32 v224, v224, v142, v143\n\tv_max_u32 v224, v224, v225\n\tv_cmp_eq_u32 vcc, -1, v224\n\t"
            "s_branch 11b\n\t32:\n\ts_sub_u32 %[pend], %[pend], 1\n\ts_cbranch_scc0 42f\n\ts_mov_b32 %[pend], 0\n\ts_branch 22b\n\t42:\n\ts_sleep 1\n\t"
            "s_add_u32 %[so], %[rot], 4096\n\ts_and_b32 %[so], %[so], 0x3fff\n\ts_add_u32 %[so], %[so], %[ob]\n\t"
            "buffer_load_dwordx4 v[144:147], %[vo], %[rs], %[so] offen sc1\n\t"
            "buffer_load_dwordx4 v[148:151], %[vo], %[rs], %[so] offen offset:1024 sc1\n\ts_waitcnt vmcnt(0)\n\tv_max3_u32 v224, v144, v145, v146\n\t"
            "v_max3_u32 v225, v147, v148, v149\n\tv_max3_u32 v224, v224, v150, v151\n\tv_max_u32 v224, v224, v225\n\tv_cmp_eq_u32 vcc, -1, v224\n\t"
            "s_branch 12b\n\t33:\n\ts_sub_u32 %[pend], %[pend], 1\n\ts_cbranch_scc0 43f\n\ts_mov_b32 %[pend], 0\n\ts_branch 23b\n\t43:\n\ts_sleep 1\n\t"
            "s_add_u32 %[so], %[rot], 6144\n\ts_and_b32 %[so], %[so], 0x3fff\n\ts_add_u32 %[so], %[so], %[ob]\n\t"
            "buffer_load_dwordx4 v[152:155], %[vo], %[rs], %[so] offen sc1\n\t"
            "buffer_load_dwordx4 v[156:159], %[vo], %[rs], %[so] offen offset:1024 sc1\n\ts_waitcnt vmcnt(0)\n\tv_max3_u32 v224, v152, v153, v154\n\t"
            "v_max3_u32 v225, v155, v156, v157\n\tv_max3_u32 v224, v224, v158, v159\n\tv_max_u32 v224, v224, v225\n\tv_cmp_eq_u32 vcc, -1, v224\n\t"
            "s_branch 13b\n\t34:\n\ts_sub_u32 %[pend], %[pend], 1\n\ts_cbranch_scc0 44f\n\ts_mov_b32 %[pend], 0\n\ts_branch 24b\n\t44:\n\ts_sleep 1\n\t"
            "s_add_u32 %[so], %[rot], 8192\n\ts_and_b32 %[so], %[so], 0x3fff\n\ts_add_u32 %[so], %[so], %[ob]\n\t"
            "buffer_load_dwordx4 v[160:163], %[vo], %[rs], %[so] offen sc1\n\t"
            "buffer_load_dwordx4 v[164:167], %[vo], %[rs], %[so] offen offset:1024 sc1\n\ts_waitcnt vmcnt(0)\n\tv_max3_u32 v224, v160, v161, v162\n\t"
            "v_max3_u32 v225, v163, v164, v165\n\tv_max3_u32 v224, v224, v166, v167\n\tv_max_u32 v224, v224, v225\n\tv_cmp_eq_u32 vcc, -1, v224\n\t"
            "s_branch 14b\n\t35:\n\ts_sub_u32 %[pend], %[pend], 1\n\ts_cbranch_scc0 45f\n\ts_mov_b32 %[pend], 0\n\ts_branch 25b\n\t45:\n\ts_sleep 1\n\t"
            "s_add_u32 %[so], %[rot], 10240\n\ts_and_b32 %[so], %[so], 0x3fff\n\ts_add_u32 %[so], %[so], %[ob]\n\t"
            "buffer_load_dwordx4 v[168:171], %[vo], %[rs], %[so] offen sc1\n\t"
            "buffer_load_dwordx4 v[172:175], %[vo], %[rs], %[so] offen offset:1024 sc1\n\ts_waitcnt vmcnt(0)\n\tv_max3_u32 v224, v168, v169, v170\n\t"
            "v_max3_u32 v225, v171, v172, v173\n\tv_max3_u32 v224, v224, v174, v175\n\tv_max_u32 v224, v224, v225\n\tv_cmp_eq_u32 vcc, -1, v224\n\t"
            "s_branch 15b\n\t36:\n\ts_sub_u32 %[pend], %[pend], 1\n\ts_cbranch_scc0 46f\n\ts_mov_b32 %[pend], 0\n\ts_branch 26b\n\t46:\n\ts_sleep 1\n\t"
            "s_add_u32 %[so], %[rot], 12288\n\ts_and_b32 %[so], %[so], 0x3fff\n\ts_add_u32 %[so], %[so], %[ob]\n\t"
            "buffer_load_dwordx4 v[176:179], %[vo], %[rs], %[so] offen sc1\n\t"
            "buffer_load_dwordx4 v[180:183], %[vo], %[rs], %[so] offen offset:1024 sc1\n\ts_waitcnt vmcnt(0)\n\tv_max3_u32 v224, v176, v177, v178\n\t"
            "v_max3_u32 v225, v179, v180, v181\n\tv_max3_u32 v224, v224, v182, v183\n\tv_max_u32 v224, v224, v225\n\tv_cmp_eq_u32 vcc, -1, v224\n\t"
            "s_branch 16b\n\t37:\n\ts_sub_u32 %[pend], %[pend], 1\n\ts_cbranch_scc0 47f\n\ts_mov_b32 %[pend], 0\n\ts_branch 27b\n\t47:\n\ts_sleep 1\n\t"
            "s_add_u32 %[so], %[rot], 14336\n\ts_and_b32 %[so], %[so], 0x3fff\n\ts_add_u32 %[so], %[so], %[ob]\n\t"
            "buffer_load_dwordx4 v[184:187], %[vo], %[rs], %[so] offen sc1\n\t"
            "buffer_load_dwordx4 v[188:191], %[vo], %[rs], %[so] offen offset:1024 sc1\n\ts_waitcnt vmcnt(0)\n\tv_max3_u32 v224, v184, v185, v186\n\t"
            "v_max3_u32 v225, v187, v188, v189\n\tv_max3_u32 v224, v224, v190, v191\n\tv_max_u32 v224, v224, v225\n\tv_cmp_eq_u32 vcc, -1, v224\n\t"
            "s_branch 17b\n\t99:\n\ts_nop 10\n\t"
            : "=&{v[192:207]}"(acc[0]), "=&{v[208:223]}"(acc[1]), [so] "=&s"(so), [cap] "=&s"(cap), [pend] "=&s"(pend)
            : [rs] "s"(rws), [tag] "s"(tag), [rot] "s"(ct * 4096), [ob] "s"(WS_O + (rt * 32 + kh * 16) * 1024), [vo] "v"(lane * 16),
              [fo] "v"(WS_F2 + ((rt >> 2) * 16 + kh * 8 + (lane & 7)) * 128), [w00] "v"(w0), [w01] "v"(w0 - hh * 512), [w10] "v"(w1), [w11] "v"(w1 - hh * 512)
            : "memory", "vcc", "scc",
              "v0", "v1", "v2", "v3", "v4", "v5", "v6", "v7", "v8", "v9", "v10", "v11", "v12", "v13", "v14", "v15", "v16", "v17", "v18", "v19", "v20",
              "v21", "v22", "v23", "v24", "v25", "v26", "v27", "v28", "v29", "v30", "v31", "v32", "v33", "v34", "v35", "v36", "v37", "v38", "v39",
              "v40", "v41", "v42", "v43", "v44", "v45", "v46", "v47", "v48", "v49", "v50", "v51", "v52", "v53", "v54", "v55", "v56", "v57", "v58",
              "v59", "v60", "v61", "v62", "v63", "v64", "v65", "v66", "v67", "v68", "v69", "v70", "v71", "v72", "v73", "v74", "v75", "v76", "v77",
              "v78", "v79", "v80", "v81", "v82", "v83", "v84", "v85", "v86", "v87", "v88", "v89", "v90", "v91", "v92", "v93", "v94", "v95", "v96",
              "v97", "v98", "v99", "v100", "v101", "v102", "v103", "v104", "v105", "v106", "v107", "v108", "v109", "v110", "v111", "v112", "v113",
              "v114", "v115", "v116", "v117", "v118", "v119", "v120", "v121", "v122", "v123", "v124", "v125", "v126", "v127", "v128", "v129", "v130",
              "v131", "v132", "v133", "v134", "v135", "v136", "v137", "v138", "v139", "v140", "v141", "v142", "v143", "v144", "v145", "v146", "v147",
              "v148", "v149", "v150", "v151", "v152", "v153", "v154", "v155", "v156", "v157", "v158", "v159", "v160", "v161", "v162", "v163", "v164",
              "v165", "v166", "v167", "v168", "v169", "v170", "v171", "v172", "v173", "v174", "v175", "v176", "v177", "v178", "v179", "v180", "v181",
              "v182", "v183", "v184", "v185", "v186", "v187", "v188", "v189", "v190", "v191", "v224", "v225", "v226");
        const f32x16 snd = acc[1];
        f32x16 hv = acc[0];
#pragma unroll
        for (int i = 0; i < 4; ++i) *(LAS f32x4*)(L + L_RED1 + (w * 4 + i) * 1024 + lane * 16) = (f32x4){snd[4 * i], snd[4 * i + 1], snd[4 * i + 2], snd[4 * i + 3]};
        ST(11);
        lds_bar();
#pragma unroll
        for (int i = 0; i < 4; ++i) {
            const f32x4 o = *(LAS f32x4*)(L + L_RED1 + ((w ^ 2) * 4 + i) * 1024 + lane * 16);
            const u32x2 xr = *(LAS u32x2*)(L + L_XR + (m * 32 + r) * 144 + kh * 64 + i * 16 + hh * 8);
            hv[4 * i] += o[0] + bflo(xr[0]); hv[4 * i + 1] += o[1] + bfhi(xr[0]);
            hv[4 * i + 2] += o[2] + bflo(xr[1]); hv[4 * i + 3] += o[3] + bfhi(xr[1]);
        }
        hpk[0] = pack8(hv, 0); hpk[1] = pack8(hv, 1);
        PIN(hpk[0]); PIN(hpk[1]);
        fl3 = __builtin_amdgcn_raw_buffer_load_b32(rws, f3, 0, SC1);
        SBAR();
#pragma unroll
        for (int s = 0; s < 2; ++s) { st128<SC1>(hpk[s], rws, lane * 16, WS_H + (rt * 28 + ct * 4 + kh * 2 + s) * 1024); }
        float s1 = 0.f, s2 = 0.f;
#pragma unroll
        for (int k = 0; k < 8; ++k) { const unsigned u = hpk[k >> 2][k & 3]; s1 = dot2(u, ONES, s1); s2 = dot2(u, u, s2); }
        __builtin_amdgcn_raw_buffer_store_b64((u32x2){__float_as_uint(halfsum(s1)), __float_as_uint(halfsum(s2))}, rws,
                                              hh ? 0x7FFFFFF0u : WS_HS + ((rt * 14 + ct * 2 + kh) * 32 + r) * 8, 0, SC1);
        ST(12);
    }
    {
        u32x2 bh[3][4];
        auto FR = [&](int u, int t) INL { return (u32x4){bh[u % 3][2 * t][0], bh[u % 3][2 * t][1], bh[u % 3][2 * t + 1][0], bh[u % 3][2 * t + 1][1]}; };
        auto R1 = [&](int u, int i) INL { bh[u % 3][i] = trread(L + L_W1 + sr[i >> 1] + u * 2048 + (i & 1) * 1024); };
#pragma unroll
        for (int k = 0; k < 8; ++k) R1(k >> 2, k & 3);
        if (!__all(fl3 == tag)) wait_word(rws, f3, tag);
        cwait<0, 15>();
        ST(13);
        u32x4 af[28];
        auto aoff = [&](int u) INL { int uu = u + 2 * ct1; if (uu >= 28) uu -= 28; return WS_H + (rt1 * 28 + uu) * 1024; };
        auto aload = [&](int u) INL { af[u] = ldh(rws, lane * 16, aoff(u)); };
        constexpr int WIN = 12;
#pragma unroll
        for (int u = 0; u < 8; ++u) aload(u);
        SBAR();
        f32x16 acc[4] = {};
        u32x2 b2h[4][2];
        LAS char* w1b[2] = {L + L_W1 + SLAB + sr[0], L + L_W1 + SLAB + sr[1]};
        PIN(w1b[0]); PIN(w1b[1]);
        auto R2 = [&](int z) INL { b2h[z & 3][0] = trread(w1b[z / 28] + (z % 28) * 2048); b2h[z & 3][1] = trread(w1b[z / 28] + (z % 28) * 2048 + 1024); };
        u32x4 xe, ext[2];
        f32x4 kk;
        bar();
#pragma unroll
        for (int u = 8; u < WIN; ++u) aload(u);
        SBAR();
        unsigned mx;
        unsigned long long late;
#pragma unroll
        for (int u = 0; u < 4; ++u) scan(mx, af[u], u == 0);
        settle4(__any(mx == FILL), af[0], af[1], af[2], af[3], rws, lane * 16, aoff(0), aoff(2));
        ST(9);
        sfor<28>([&](auto ic) INL {
            constexpr int u = ic.value, out = (u + WIN < 28 ? u + WIN : 28) - (u + 5);
            acc[0] = mfma(FR(u, 0), af[u], acc[0]); PIN(acc[0]); SBAR();
            if constexpr (u + 2 < 28) { cwait<(u + 4 < 28 ? out : 63), 4>(); R1(u + 2, 0); R1(u + 2, 1); } else R2(u - 26);
            if constexpr (u + 4 < 28) scan(mx, af[u + 4], (u & 3) == 0);
            SBAR();
            acc[1] = mfma(FR(u, 1), af[u], acc[1]); PIN(acc[1]); SBAR();
            if constexpr (u + 2 < 28) { cwait<63, 4>(); R1(u + 2, 2); R1(u + 2, 3); }
            if constexpr (u == 27) R2(2);
            if constexpr ((u & 3) == 3 && u + 1 < 28) { late = __builtin_amdgcn_ballot_w64(mx == FILL); asm volatile("" : "+s"(late)); }
            SBAR();
            if constexpr (u + WIN < 28) aload(u + WIN);
            SBAR();
            if constexpr ((u & 3) == 3 && u + 1 < 28) { settle4(late != 0, af[u + 1], af[u + 2], af[u + 3], af[u + 4], rws, lane * 16, aoff(u + 1), aoff(u + 3)); SBAR(); }
        });
        ST(14);
        float g[4];
        unsigned gp[4];
        unsigned gdst = WS_G + (rt1 * 112 + ct1 * 8) * 1024 + lane * 16;
        LAS char* xw = L + w2slot(0) + w * 4096 + lane * 16;
        PIN(gdst); PIN(xw);
        sfor<56>([&](auto ic) INL {
            constexpr int z = ic.value, t = 2 + z / 28;
            acc[t] = mfma((u32x4){b2h[z & 3][0][0], b2h[z & 3][0][1], b2h[z & 3][1][0], b2h[z & 3][1][1]}, af[z % 28], acc[t]); PIN(acc[t]); SBAR();
            if constexpr (z == 7) { cwait<63, 4>(); bar(); ST(25); ext[0] = ldsr128(L + L_EXT + 2048 + lane * 16); ext[1] = ldsr128(L + L_EXT + 3072 + lane * 16);
                                    kk = *(LAS f32x4*)(L + L_GK + w * 2048 + lane * 16); xe = ldsr128(L + L_GK + w * 2048 + 1024 + lane * 16); }
            if constexpr (z + 3 < 56) { if constexpr (z > 12) cwait<63, 2>(); R2(z + 3); SBAR(); }
            if constexpr (z >= 1 && z < 5) {
#pragma unroll
                for (int k = 2 * (z - 1); k < 2 * (z - 1) + 2; ++k) { const f32x16& v = acc[k >> 2]; const int i = k & 3; *(LAS f32x4*)(xw + (k >> 2) * 16384 + i * 1024) = (f32x4){v[4 * i], v[4 * i + 1], v[4 * i + 2], v[4 * i + 3]}; }
                SBAR();
            }
            if constexpr (z == 14) { acc[2] = mfma(ext[0], xe, acc[2]); acc[3] = mfma(ext[1], xe, acc[3]); PIN(acc[2]); PIN(acc[3]); SBAR(); }
            constexpr int e = z - 38;
            if constexpr (e >= 0 && e < 16) {
                constexpr int i = e >> 2;
                gelu4(e & 3, acc[2][4 * i], acc[2][4 * i + 1], acc[2][4 * i + 2], acc[2][4 * i + 3], g, kk[0], kk[1], kk[2], gp[2 * (i & 1)], gp[2 * (i & 1) + 1]);
                if constexpr ((e & 7) == 7) { st128<SC1>((u32x4){gp[0], gp[1], gp[2], gp[3]}, rws, gdst, (4 + (e >> 3)) * 1024); SBAR(); }
            }
        });
        ST(15);
        constexpr int WN2 = 12;
        u32x4 ag[WN2];
        const int rot2 = fc2_rot(j);
        const unsigned abase = WS_G + (rt * 112 + kh * 56) * 1024;
        auto goff = [&](int u) INL { return abase + fc2_kstep(u, rot2) * 1024; };
        asm volatile("s_nop 11" : "+v"(acc[3]));
        {
            float g2[2][4];
#pragma unroll
            for (int f = 0; f < 2; ++f) {
#pragma unroll
                for (int st = 0; st < 4; ++st) {
#pragma unroll
                    for (int q = 0; q < 2; ++q) { const int i = 8 * f + 4 * q; gelu4(st, acc[3][i], acc[3][i + 1], acc[3][i + 2], acc[3][i + 3], g2[q], kk[0], kk[1], kk[2], gp[2 * q], gp[2 * q + 1]); }
                    if (st < 3) { ag[3 * f + st] = ldh(rws, lane * 16, goff(3 * f + st)); SBAR(); }
                }
                st128<SC1>((u32x4){gp[0], gp[1], gp[2], gp[3]}, rws, gdst, (6 + f) * 1024);
                asm volatile("s_nop 1" ::: "memory");
            }
        }
#pragma unroll
        for (int u = 6; u < WN2; ++u) ag[u] = ldh(rws, lane * 16, goff(u));
        SBAR();
        ST(16);
        LAS char* w2b[3][2];
#pragma unroll
        for (int k = 0; k < 6; ++k) { w2b[k >> 1][k & 1] = L + sk[k & 1] + kh * 8192 + (k >> 1) * 48 * K1; PIN(w2b[k >> 1][k & 1]); }
        auto R3 = [&](int u, int i) INL { bh[u % 3][i] = trread(w2b[w2slot(u >> 2) / (48 * K1)][i >> 1] + w2slot(u >> 2) % (48 * K1) + (u & 3) * 2048 + (i & 1) * 1024); };
        bar();
        ST(17);
#pragma unroll
        for (int k = 0; k < 8; ++k) R3(k >> 2, k & 3);
#pragma unroll
        for (int u = 0; u < 4; ++u) scan(mx, ag[u], u == 0);
        settle4(__any(mx == FILL), ag[0], ag[1], ag[2], ag[3], rws, lane * 16, goff(0), goff(2));
        ST(18);
        f32x16 a2[2] = {};
        u32x2 bb[4];
        sfor<56>([&](auto ic) INL {
            constexpr int u = ic.value, out = (u + WN2 < 60 ? u + WN2 : 60) - (u + 5);
            a2[0] = mfma(FR(u, 0), ag[u % WN2], a2[0]); PIN(a2[0]); SBAR();
            if constexpr (u == 16 || u == 30 || u == 41) bar();
            if constexpr (u == 16) ST(19);
            if constexpr (u + 2 < 56) { cwait<(u + 4 < 56 ? out : 63), 4>(); R3(u + 2, 0); R3(u + 2, 1); }
            if constexpr (u + 4 < 56) scan(mx, ag[(u + 4) % WN2], (u & 3) == 0);
            SBAR();
            a2[1] = mfma(FR(u, 1), ag[u % WN2], a2[1]); PIN(a2[1]); SBAR();
            if constexpr (u + 2 < 56) { cwait<63, 4>(); R3(u + 2, 2); R3(u + 2, 3); }
            if constexpr ((u & 3) == 3 && u + 1 < 56) { late = __builtin_amdgcn_ballot_w64(mx == FILL); asm volatile("" : "+s"(late)); }
            SBAR();
            if constexpr (u + WN2 < 56) ag[u % WN2] = ldh(rws, lane * 16, goff(u + WN2));
            else if constexpr (u + WN2 < 60) bb[u + WN2 - 56] = __builtin_amdgcn_raw_buffer_load_b64(mkrs(p.b2, D * 2), hh * 8 + (u + WN2 - 56) * 16, ct * 128 + kh * 64, 0);
            SBAR();
            if constexpr ((u & 3) == 3 && u + 1 < 56) { settle4(late != 0, ag[(u + 1) % WN2], ag[(u + 2) % WN2], ag[(u + 3) % WN2], ag[(u + 4) % WN2], rws, lane * 16, goff(u + 1), goff(u + 3)); SBAR(); }
        });
        ST(20);
        const f32x16 snd = a2[1];
        f32x16 v = a2[0];
#pragma unroll
        for (int i = 0; i < 4; ++i) *(LAS f32x4*)(L + L_RED2 + (w * 4 + i) * 1024 + lane * 16) = (f32x4){snd[4 * i], snd[4 * i + 1], snd[4 * i + 2], snd[4 * i + 3]};
        lds_bar();
        unsigned o[8];
#pragma unroll
        for (int i = 0; i < 4; ++i) {
            const f32x4 t = *(LAS f32x4*)(L + L_RED2 + ((w ^ 2) * 4 + i) * 1024 + lane * 16);
            const unsigned h0 = hpk[i >> 1][2 * (i & 1)], h1 = hpk[i >> 1][2 * (i & 1) + 1];
            o[2 * i] = cvtpk(v[4 * i] + t[0] + bflo(bb[i][0]) + bflo(h0), v[4 * i + 1] + t[1] + bfhi(bb[i][0]) + bfhi(h0));
            o[2 * i + 1] = cvtpk(v[4 * i + 2] + t[2] + bflo(bb[i][1]) + bflo(h1), v[4 * i + 3] + t[3] + bfhi(bb[i][1]) + bfhi(h1));
        }
#pragma unroll
        for (int i = 0; i < 4; ++i) *(LAS u32x2*)(L + (m * 32 + r) * 144 + kh * 64 + i * 16 + hh * 8) = (u32x2){o[2 * i], o[2 * i + 1]};
        lds_bar();
#pragma unroll
        for (int k = 0; k < 2; ++k) {
            const int row = w * 16 + k * 8 + (lane >> 3);
            st128<OAUX>(ldsr128(L + row * 144 + (lane & 7) * 16), mkrs(p.out, S * D * 2), ((xcd * 4 + r4) * 64 + row) * 896 + (lane & 7) * 16, ct * 128);
        }
        ST(21);
    }
}

__global__ void __launch_bounds__(NT, 2) k_block(Params p) {
    extern __shared__ __attribute__((aligned(16))) char smem[];
    LAS char* L = (LAS char*)smem;
    ST_DECL
    ST(0);
    if (__builtin_amdgcn_readfirstlane(threadIdx.x >> 6) == 1) {
        unsigned long long pc;
        asm volatile("s_getpc_b64 %0" : "=s"(pc));
        dma4(mkrs((const void*)pc, CODE_LEN), ((threadIdx.x & 63) * 8 + ((blockIdx.x >> 3) & 7)) * 128, 0, L_QF + 2048);
    }
    asm volatile("ds_write_b32 %0, %1\n\ts_waitcnt lgkmcnt(0)\n\ts_barrier" ::"v"(P1_SY + (threadIdx.x & 7) * 4), "v"(0) : "memory");
    const int lane = threadIdx.x & 63, w = __builtin_amdgcn_readfirstlane(threadIdx.x >> 6);
    const int b = blockIdx.x, xcd = b & 7, j = b >> 3;
    const int rb = (xcd & 1) * 8 + (j & 7), sub = j >> 3, head = 4 * (xcd >> 1) + sub, pair = 2 * (xcd >> 1) + (sub >> 1), kind = sub & 1;
    const rsrc_t rws = mkrs(p.ws, WS_END);
    const int rot = ((j & 7) + 2 * sub) % 7;
    if (w == 4 || w == 5) { ST(1); p1_chunk(p, 0, w - 4, lane, rb, pair, kind, rot); }
    else if (w == 6) {
        const unsigned long long a0 = (unsigned long long)p.ws + (lane < 36 ? lane : 0) * (448 * 1024);
        unsigned lo = (unsigned)a0, hi = (unsigned)(a0 >> 32);
        const void* q[6] = {p.out, p.w1, p.w2, p.wo, p.b1, p.b2};
        const unsigned last[6] = {S * D * 2 - 4, D * F * 2 - 4, D * F * 2 - 4, D * D * 2 - 4, F * 2 - 4, D * 2 - 4};
#pragma unroll
        for (int k = 0; k < 12; ++k) {
            const unsigned long long a = (unsigned long long)q[k >> 1] + (k & 1 ? last[k >> 1] : 0);
            asm("v_writelane_b32 %0, %2, %4\n\tv_writelane_b32 %1, %3, %4" : "+v"(lo), "+v"(hi) : "s"((unsigned)a), "s"((unsigned)(a >> 32)), "i"(40 + k));
        }
        asm volatile("s_mov_b32 m0, %0\n\ts_nop 0\n\tglobal_load_lds_dword %1, off sc1" ::"s"(P1_SY + 256), "v"(((unsigned long long)hi << 32) | lo) : "memory");
    }
    unsigned tag;
    if (w < 4) {
        if (kind) p1_compute<true>(L, rws, w, lane, rb, pair, head, tag ST_PASS);
        else p1_compute<false>(L, rws, w, lane, rb, pair, head, tag ST_PASS);
        ST(4);
        attn_compute(L, rws, w, lane, rb, head ST_PASS);
        if (j < 28) mlp_compute(p, L, rws, w, lane, xcd, j, tag ST_PASS);
    } else {
        if (w < 6) p1_loader(p, L, w - 4, lane, rb, pair, kind, head, rot ST_PASS);
        else p1_aux(p, L, rws, w - 6, lane, rb, pair, kind, head ST_PASS);
        tag = get_tag(L);
        ST(4);
        u32x4 sg[9][4];
        u32x4 xr[2];
        u32x4 ext;
        attn_helper(p, L, rws, w - 4, lane, rb, head, xcd, j, tag, sg, xr, ext ST_PASS);
        if (j < 28) mlp_helper(p, L, rws, w - 4, lane, xcd, j, tag, sg, xr, ext ST_PASS);
    }
    if (b == 0 && w == 0) set_flag(rws, WS_GEN, tag, lane);
    ST_FLUSH();
}

extern "C" void kernel_launch(void* const* d_in, const int* in_sizes, int n_in, void* d_out, int out_size, void* d_ws, size_t ws_size, hipStream_t stream) {
    Params p;
    p.x = d_in[0]; p.wqkv = d_in[1]; p.wo = d_in[2]; p.w1 = d_in[4]; p.b1 = d_in[5]; p.w2 = d_in[6]; p.b2 = d_in[7];
    p.out = d_out; p.ws = d_ws;
    (void)hipFuncSetAttribute((const void*)k_block, hipFuncAttributeMaxDynamicSharedMemorySize, LDS_BYTES);
    k_block<<<NWG, NT, LDS_BYTES, stream>>>(p);
}
```
